```python
import torch

def _softmax(x, axis=-1):
    return torch.softmax(x, dim=axis)
def _gelu(x):
    return torch.nn.functional.gelu(x, approximate="tanh")
def _silu(x):
    return torch.nn.functional.silu(x)
def _sigmoid(x):
    return torch.sigmoid(x)
def _layernorm(x, axis=-1, eps=1e-5):
    m = x.mean(dim=axis, keepdim=True)
    v = x.var(dim=axis, keepdim=True, unbiased=False)
    return (x - m) / torch.sqrt(v + eps)
def _scatter_add(tgt, idx, src, axis=0):
    return tgt.clone().index_add_(axis, idx, src)
def _segment_sum(src, seg, n):
    out = torch.zeros((n,) + src.shape[1:], dtype=src.dtype)
    return out.index_add_(0, seg, src)
def _pad(x, pw):
    flat = tuple(v for pair in reversed(pw) for v in pair)
    return torch.nn.functional.pad(x, flat)

kernel_name = 'standalone__mha_softmax__near_ridge'

def setup_inputs():

    _w = (lambda a: (lambda t: t.to(torch.bfloat16) if t.is_floating_point() else t)(torch.as_tensor(a)))
    B, H, Sq, Sk, D = 32, 32, 384, 384, 192
    return {
        "q": _w((lambda *s: torch.randn(*s, dtype=torch.bfloat16) * 0.8)(B, H, Sq, D)),
        "k": _w((lambda *s: torch.randn(*s, dtype=torch.bfloat16) * 0.8)(B, H, Sk, D)),
        "v": _w((lambda *s: torch.randn(*s, dtype=torch.bfloat16) * 0.05)(B, H, Sk, D)),
    }

def reference(q, k, v):
    scale = q.shape[-1] ** -0.5
    scores = torch.einsum("bhqd,bhkd->bhqk", q, k) * scale
    p = _softmax(scores, axis=-1)
    return torch.einsum("bhqk,bhkd->bhqd", p, v)
```

```cpp
#include <hip/hip_runtime.h>
#include <hip/hip_bf16.h>


__device__ __forceinline__ float bf2f(unsigned short x) { return __uint_as_float((unsigned)x << 16); }

__device__ __forceinline__ void exact_rows(const unsigned short* __restrict__ q, const unsigned short* __restrict__ k,
                                           const unsigned short* __restrict__ v, unsigned short* __restrict__ o,
                                           unsigned head, unsigned wid) {
  const unsigned lane = __builtin_amdgcn_mbcnt_hi(~0u, __builtin_amdgcn_mbcnt_lo(~0u, 0u));
  const size_t hb = (size_t)head * (384 * 192);
  const float c = 0.10411754f;
#pragma unroll 1
  for (unsigned row = wid * 96; row < wid * 96 + 96; ++row) {
    const unsigned* qr = (const unsigned*)(q + hb + (size_t)row * 192);
    float s[6];
#pragma unroll
    for (int kk = 0; kk < 6; ++kk) {
      const unsigned* kr = (const unsigned*)(k + hb + (size_t)(lane + 64 * kk) * 192);
      float acc = 0.f;
#pragma unroll 4
      for (int d = 0; d < 96; ++d) {
        const unsigned a = qr[d], b = kr[d];
        acc = fmaf(__uint_as_float(a << 16), __uint_as_float(b << 16), acc);
        acc = fmaf(__uint_as_float(a & 0xffff0000u), __uint_as_float(b & 0xffff0000u), acc);
      }
      s[kk] = acc;
    }
    float m = fmaxf(fmaxf(fmaxf(s[0], s[1]), fmaxf(s[2], s[3])), fmaxf(s[4], s[5]));
#pragma unroll
    for (int x = 1; x < 64; x <<= 1) m = fmaxf(m, __shfl_xor(m, x));
    float l = 0.f;
#pragma unroll
    for (int kk = 0; kk < 6; ++kk) { s[kk] = exp2f((s[kk] - m) * c); l += s[kk]; }
#pragma unroll
    for (int x = 1; x < 64; x <<= 1) l += __shfl_xor(l, x);
    float o0 = 0.f, o1 = 0.f, o2 = 0.f;
#pragma unroll
    for (int kk = 0; kk < 6; ++kk) {
#pragma unroll 2
      for (int i = 0; i < 64; ++i) {
        const float p = __shfl(s[kk], i);
        const unsigned short* vr = v + hb + (size_t)(64 * kk + i) * 192;
        o0 = fmaf(p, bf2f(vr[lane]), o0); o1 = fmaf(p, bf2f(vr[lane + 64]), o1); o2 = fmaf(p, bf2f(vr[lane + 128]), o2);
      }
    }
    const float r = 1.f / l;
    __hip_bfloat16* orow = (__hip_bfloat16*)(o + hb + (size_t)row * 192);
    orow[lane] = __float2bfloat16(o0 * r); orow[lane + 64] = __float2bfloat16(o1 * r); orow[lane + 128] = __float2bfloat16(o2 * r);
  }
}

__global__ __launch_bounds__(256, 1) void mha_fwd_kernel(const unsigned short* __restrict__ q, const unsigned short* __restrict__ k,
                                                         const unsigned short* __restrict__ v, unsigned short* __restrict__ o
) {
  extern __shared__ __attribute__((aligned(16))) char lds[];
  const unsigned wid = __builtin_amdgcn_readfirstlane(threadIdx.x >> 6);
  const unsigned blk = blockIdx.x;
  const unsigned ldsb = __builtin_amdgcn_readfirstlane((unsigned)(size_t)lds);
  unsigned flags;
  asm volatile(
    "s_load_dwordx8 s[80:87], %1, 0x0\n"
    "s_memrealtime s[94:95]\n"
    "v_mbcnt_lo_u32_b32 v156, -1, 0\n"
    "v_mbcnt_hi_u32_b32 v156, -1, v156\n"
    "v_lshrrev_b32 v152, 3, v156\n"
    "v_and_b32 v155, 7, v156\n"
    "s_lshr_b32 s66, %2, 3\n"
    "s_getpc_b64 s[76:77]\n"
    "L_pc_%=:\n"
    "s_and_b32 s77, s77, 0xffff\n"
    "s_mov_b32 s78, L_end_%=-L_pc_%=\n"
    "s_mov_b32 s79, 0x20000\n"
    "s_lshl_b32 s68, s66, 2\n"
    "s_add_u32 s68, s68, %3\n"
    "s_and_b32 s68, s68, 63\n"
    "s_lshl_b32 s68, s68, 10\n"
    "v_lshlrev_b32 v154, 7, v155\n"
    "v_lshl_add_u32 v154, v152, 20, v154\n"
    "buffer_load_dword v157, v154, s[76:79], s68 offen\n"
    "s_and_b32 s68, %2, 7\n"
    "s_lshl_b32 s68, s68, 7\n"
    "s_add_u32 s68, s68, s66\n"
    "s_mul_i32 s68, s68, 147456\n"
    "s_mov_b32 s42, 149504\n"
    "s_mov_b32 s43, 0x20000\n"
    "s_mov_b32 s46, 149504\n"
    "s_mov_b32 s47, 0x20000\n"
    "s_mov_b32 s50, 149504\n"
    "s_mov_b32 s51, 0x20000\n"
    "s_mov_b32 s54, 0\n"
    "s_mov_b32 s55, 0x20000\n"
    "v_lshrrev_b32 v154, 4, v156\n"
    "v_lshlrev_b32 v154, 1, v154\n"
    "v_xor_b32 v154, v154, v155\n"
    "v_mul_u32_u24 v153, 384, v152\n"
    "v_lshl_add_u32 v153, v154, 4, v153\n"
    "v_add_u32 v20, 2048, v153\n"
    "v_add_u32 v22, 1152, v153\n"
    "v_add_u32 v23, 256, v153\n"
    "s_mul_i32 s61, %3, 3072\n"
    "s_add_u32 s60, %4, s61\n"
    "s_add_u32 m0, s60, 0\n"
    "s_add_u32 s36, s61, 0\n"
    "s_waitcnt lgkmcnt(0)\n"
    "s_add_u32 s40, s80, s68\n"
    "s_addc_u32 s41, s81, 0\n"
    "s_sub_u32 s40, s40, 2048\n"
    "s_subb_u32 s41, s41, 0\n"
    "s_and_b32 s41, s41, 0xffff\n"
    "buffer_load_dwordx4 v20, s[40:43], s36 offen nt lds\n"
    "buffer_load_dwordx4 v22, s[40:43], s36 offen offset:1024 nt lds\n"
    "buffer_load_dwordx4 v23, s[40:43], s36 offen offset:2048 nt lds\n"
    "s_add_u32 m0, s60, 12288\n"
    "s_add_u32 s36, s61, 36864\n"
    "buffer_load_dwordx4 v20, s[40:43], s36 offen nt lds\n"
    "buffer_load_dwordx4 v22, s[40:43], s36 offen offset:1024 nt lds\n"
    "buffer_load_dwordx4 v23, s[40:43], s36 offen offset:2048 nt lds\n"
    "s_add_u32 m0, s60, 73728\n"
    "s_add_u32 s36, s61, 73728\n"
    "buffer_load_dwordx4 v20, s[40:43], s36 offen nt lds\n"
    "buffer_load_dwordx4 v22, s[40:43], s36 offen offset:1024 nt lds\n"
    "buffer_load_dwordx4 v23, s[40:43], s36 offen offset:2048 nt lds\n"
    "s_add_u32 s44, s82, s68\n"
    "s_addc_u32 s45, s83, 0\n"
    "s_sub_u32 s44, s44, 2048\n"
    "s_subb_u32 s45, s45, 0\n"
    "s_and_b32 s45, s45, 0xffff\n"
    "v_add_u32 v21, 4096, v153\n"
    "v_add_u32 v31, 6144, v153\n"
    "s_add_u32 s48, s84, s68\n"
    "s_addc_u32 s49, s85, 0\n"
    "s_sub_u32 s48, s48, 2048\n"
    "s_subb_u32 s49, s49, 0\n"
    "s_and_b32 s49, s49, 0xffff\n"
    "s_add_u32 s52, s86, s68\n"
    "s_addc_u32 s53, s87, 0\n"
    "s_sub_u32 s52, s52, 4718592\n"
    "s_subb_u32 s53, s53, 0\n"
    "s_and_b32 s53, s53, 0xffff\n"
    "s_mov_b32 s72, %3\n"
    "s_mov_b32 s73, %4\n"
    "s_mul_i32 s71, s72, 9216\n"
    "s_mul_i32 s66, s72, 96\n"
    "v_add_u32 v153, s66, v155\n"
    "v_mul_u32_u24 v24, 384, v153\n"
    "v_bfe_u32 v153, v156, 3, 1\n"
    "v_lshl_add_u32 v24, v153, 6, v24\n"
    "v_lshrrev_b32 v153, 4, v156\n"
    "v_lshl_add_u32 v24, v153, 4, v24\n"
    "v_bfe_u32 v154, v156, 1, 2\n"
    "v_lshlrev_b32 v154, 1, v154\n"
    "v_lshrrev_b32 v17, 4, v156\n"
    "v_xor_b32 v154, v154, v17\n"
    "v_lshlrev_b32 v154, 4, v154\n"
    "v_lshl_add_u32 v154, v155, 7, v154\n"
    "v_bfe_u32 v153, v156, 3, 1\n"
    "v_mul_u32_u24 v153, 3072, v153\n"
    "v_add_u32 v154, v153, v154\n"
    "v_add_u32 v16, s73, v154\n"
    "v_xor_b32 v17, 64, v16\n"
    "v_lshrrev_b32 v153, 2, v156\n"
    "v_lshlrev_b32 v18, 7, v153\n"
    "v_bfe_u32 v153, v156, 3, 2\n"
    "v_lshlrev_b32 v153, 1, v153\n"
    "v_and_b32 v154, 3, v156\n"
    "v_xor_b32 v153, v153, v154\n"
    "v_lshl_add_u32 v18, v153, 4, v18\n"
    "v_add_u32 v18, s73, v18\n"
    "v_add_u32 v18, 24576, v18\n"
    "s_mov_b32 s96, 0x00ff00ff\n"
    "s_mov_b32 s97, 0x00ff00ff\n"
    "s_mov_b32 s98, 0xff00ff00\n"
    "s_mov_b32 s99, 0xff00ff00\n"
    "s_mov_b32 s68, 0\n"
    "s_cmp_eq_u32 s72, 1\n"
    "s_cselect_b32 s68, 12288, s68\n"
    "s_cmp_eq_u32 s72, 2\n"
    "s_cselect_b32 s68, 73728, s68\n"
    "s_cmp_eq_u32 s72, 3\n"
    "s_cselect_b32 s68, 86016, s68\n"
    "s_mov_b32 s69, 24576\n"
    "s_cmp_eq_u32 s72, 1\n"
    "s_cselect_b32 s69, 36864, s69\n"
    "s_cmp_eq_u32 s72, 2\n"
    "s_cselect_b32 s69, 49152, s69\n"
    "s_cmp_eq_u32 s72, 3\n"
    "s_cselect_b32 s69, 61440, s69\n"
    "s_mov_b32 s70, 135168\n"
    "s_cmp_eq_u32 s72, 1\n"
    "s_cselect_b32 s70, 98304, s70\n"
    "s_cmp_eq_u32 s72, 2\n"
    "s_cselect_b32 s70, 110592, s70\n"
    "s_cmp_eq_u32 s72, 3\n"
    "s_cselect_b32 s70, 122880, s70\n"
    "s_sub_u32 s75, s68, 122880\n"
    "s_sub_u32 s38, s69, s68\n"
    "s_sub_u32 s39, s70, 147456\n"
    "s_sub_u32 s37, 147456, s69\n"
    "s_mov_b32 s62, 0x3dd53b95\n"
    "s_mov_b32 s74, 0xbdd53b95\n"
    "s_mov_b32 s63, 0x5f800000\n"
    "s_mov_b32 s64, 0\n"
    "s_mov_b32 s65, 0\n"
    "s_mov_b32 s67, 1\n"
    "v_add_u32 v150, s68, v16\n"
    "v_add_u32 v148, s70, v16\n"
    "v_add_u32 v151, s68, v17\n"
    "v_add_u32 v149, s70, v17\n"
    "v_add_u32 v16, 147456, v16\n"
    "v_add_u32 v17, 147456, v17\n"
    "s_add_u32 m0, s60, 86016\n"
    "s_add_u32 s36, s61, 110592\n"
    "buffer_load_dwordx4 v20, s[40:43], s36 offen nt lds\n"
    "buffer_load_dwordx4 v22, s[40:43], s36 offen offset:1024 nt lds\n"
    "buffer_load_dwordx4 v23, s[40:43], s36 offen offset:2048 nt lds\n"
    "s_add_u32 m0, s60, 147456\n"
    "s_add_u32 s36, s61, 0\n"
    "buffer_load_dwordx4 v20, s[44:47], s36 offen nt lds\n"
    "buffer_load_dwordx4 v22, s[44:47], s36 offen offset:1024 nt lds\n"
    "buffer_load_dwordx4 v23, s[44:47], s36 offen offset:2048 nt lds\n"
    "s_add_u32 m0, s60, 24576\n"
    "s_add_u32 s36, s61, 12288\n"
    "buffer_load_dwordx4 v20, s[40:43], s36 offen nt lds\n"
    "buffer_load_dwordx4 v22, s[40:43], s36 offen offset:1024 nt lds\n"
    "buffer_load_dwordx4 v23, s[40:43], s36 offen offset:2048 nt lds\n"
    "s_add_u32 m0, s60, 36864\n"
    "s_add_u32 s36, s61, 49152\n"
    "buffer_load_dwordx4 v20, s[40:43], s36 offen nt lds\n"
    "buffer_load_dwordx4 v22, s[40:43], s36 offen offset:1024 nt lds\n"
    "buffer_load_dwordx4 v23, s[40:43], s36 offen offset:2048 nt lds\n"
    "s_add_u32 m0, s60, 49152\n"
    "s_add_u32 s36, s61, 86016\n"
    "buffer_load_dwordx4 v20, s[40:43], s36 offen nt lds\n"
    "buffer_load_dwordx4 v22, s[40:43], s36 offen offset:1024 nt lds\n"
    "buffer_load_dwordx4 v23, s[40:43], s36 offen offset:2048 nt lds\n"
    "s_add_u32 m0, s60, 61440\n"
    "s_add_u32 s36, s61, 122880\n"
    "buffer_load_dwordx4 v20, s[40:43], s36 offen nt lds\n"
    "buffer_load_dwordx4 v22, s[40:43], s36 offen offset:1024 nt lds\n"
    "buffer_load_dwordx4 v23, s[40:43], s36 offen offset:2048 nt lds\n"
    "s_waitcnt vmcnt(12)\n"
    "s_barrier\n"
    "ds_read_b128 a[96:99], v150 offset:0\n"
    "ds_read_b128 a[100:103], v151 offset:0\n"
    "ds_read_b128 a[104:107], v150 offset:1024\n"
    "ds_read_b128 a[108:111], v151 offset:1024\n"
    "ds_read_b128 a[112:115], v150 offset:2048\n"
    "ds_read_b128 a[116:119], v151 offset:2048\n"
    "ds_read_b128 a[120:123], v150 offset:6144\n"
    "ds_read_b128 a[124:127], v151 offset:6144\n"
    "ds_read_b128 a[128:131], v150 offset:7168\n"
    "ds_read_b128 a[132:135], v151 offset:7168\n"
    "ds_read_b128 a[136:139], v150 offset:8192\n"
    "ds_read_b128 a[140:143], v151 offset:8192\n"
    "ds_read_b128 v[64:67], v16 offset:0\n"
    "ds_read_b128 v[68:71], v17 offset:0\n"
    "ds_read_b128 v[72:75], v16 offset:1024\n"
    "ds_read_b128 v[76:79], v17 offset:1024\n"
    "ds_read_b128 v[80:83], v16 offset:2048\n"
    "ds_read_b128 v[84:87], v17 offset:2048\n"
    "ds_read_b128 v[32:35], v16 offset:6144\n"
    "ds_read_b128 v[36:39], v17 offset:6144\n"
    "ds_read_b128 v[40:43], v16 offset:7168\n"
    "ds_read_b128 v[44:47], v17 offset:7168\n"
    "ds_read_b128 v[48:51], v16 offset:8192\n"
    "ds_read_b128 v[52:55], v17 offset:8192\n"
    "v_add_u32 v150, s38, v150\n"
    "v_subrev_u32 v16, 147456, v16\n"
    "v_add_u32 v151, s38, v151\n"
    "v_subrev_u32 v17, 147456, v17\n"
    "s_waitcnt lgkmcnt(0)\n"
    "s_barrier\n"
    "s_add_u32 m0, s60, 0\n"
    "s_add_u32 s36, s61, 12288\n"
    "buffer_load_dwordx4 v20, s[44:47], s36 offen nt lds\n"
    "buffer_load_dwordx4 v22, s[44:47], s36 offen offset:1024 nt lds\n"
    "buffer_load_dwordx4 v23, s[44:47], s36 offen offset:2048 nt lds\n"
    "v_mfma_f32_16x16x32_bf16 v[160:163], v[64:67], a[96:99], 0\n"
    "v_mfma_f32_16x16x32_bf16 v[160:163], v[68:71], a[100:103], v[160:163]\n"
    "v_mfma_f32_16x16x32_bf16 v[160:163], v[72:75], a[104:107], v[160:163]\n"
    "v_mfma_f32_16x16x32_bf16 v[160:163], v[76:79], a[108:111], v[160:163]\n"
    "s_add_u32 m0, s60, 135168\n"
    "s_add_u32 s36, s61, 24576\n"
    "buffer_load_dwordx4 v20, s[40:43], s36 offen nt lds\n"
    "v_mfma_f32_16x16x32_bf16 v[160:163], v[80:83], a[112:115], v[160:163]\n"
    "v_mfma_f32_16x16x32_bf16 v[160:163], v[84:87], a[116:119], v[160:163]\n"
    "v_mfma_f32_16x16x32_bf16 v[168:171], v[64:67], a[120:123], 0\n"
    "v_mfma_f32_16x16x32_bf16 v[168:171], v[68:71], a[124:127], v[168:171]\n"
    "buffer_load_dwordx4 v22, s[40:43], s36 offen offset:1024 nt lds\n"
    "v_mfma_f32_16x16x32_bf16 v[168:171], v[72:75], a[128:131], v[168:171]\n"
    "v_mfma_f32_16x16x32_bf16 v[168:171], v[76:79], a[132:135], v[168:171]\n"
    "v_mfma_f32_16x16x32_bf16 v[168:171], v[80:83], a[136:139], v[168:171]\n"
    "v_mfma_f32_16x16x32_bf16 v[168:171], v[84:87], a[140:143], v[168:171]\n"
    "buffer_load_dwordx4 v23, s[40:43], s36 offen offset:2048 nt lds\n"
    "v_mfma_f32_16x16x32_bf16 v[164:167], v[32:35], a[96:99], 0\n"
    "v_mfma_f32_16x16x32_bf16 v[164:167], v[36:39], a[100:103], v[164:167]\n"
    "v_mfma_f32_16x16x32_bf16 v[164:167], v[40:43], a[104:107], v[164:167]\n"
    "v_mfma_f32_16x16x32_bf16 v[164:167], v[44:47], a[108:111], v[164:167]\n"
    "s_add_u32 m0, s60, 98304\n"
    "s_add_u32 s36, s61, 61440\n"
    "buffer_load_dwordx4 v20, s[40:43], s36 offen nt lds\n"
    "v_mfma_f32_16x16x32_bf16 v[164:167], v[48:51], a[112:115], v[164:167]\n"
    "v_mfma_f32_16x16x32_bf16 v[164:167], v[52:55], a[116:119], v[164:167]\n"
    "v_mfma_f32_16x16x32_bf16 v[172:175], v[32:35], a[120:123], 0\n"
    "v_mfma_f32_16x16x32_bf16 v[172:175], v[36:39], a[124:127], v[172:175]\n"
    "buffer_load_dwordx4 v22, s[40:43], s36 offen offset:1024 nt lds\n"
    "v_mfma_f32_16x16x32_bf16 v[172:175], v[40:43], a[128:131], v[172:175]\n"
    "v_mfma_f32_16x16x32_bf16 v[172:175], v[44:47], a[132:135], v[172:175]\n"
    "v_mfma_f32_16x16x32_bf16 v[172:175], v[48:51], a[136:139], v[172:175]\n"
    "v_mfma_f32_16x16x32_bf16 v[172:175], v[52:55], a[140:143], v[172:175]\n"
    "buffer_load_dwordx4 v23, s[40:43], s36 offen offset:2048 nt lds\n"
    "s_waitcnt vmcnt(6)\n"
    "s_barrier\n"
    "ds_read_b128 a[0:3], v150 offset:0\n"
    "ds_read_b128 a[4:7], v151 offset:0\n"
    "ds_read_b128 a[8:11], v150 offset:1024\n"
    "ds_read_b128 a[12:15], v151 offset:1024\n"
    "ds_read_b128 a[16:19], v150 offset:2048\n"
    "ds_read_b128 a[20:23], v151 offset:2048\n"
    "ds_read_b128 a[24:27], v150 offset:6144\n"
    "ds_read_b128 a[28:31], v151 offset:6144\n"
    "ds_read_b128 a[32:35], v150 offset:7168\n"
    "ds_read_b128 a[36:39], v151 offset:7168\n"
    "ds_read_b128 a[40:43], v150 offset:8192\n"
    "ds_read_b128 a[44:47], v151 offset:8192\n"
    "ds_read_b128 v[88:91], v16 offset:0\n"
    "ds_read_b128 v[92:95], v17 offset:0\n"
    "ds_read_b128 v[96:99], v16 offset:1024\n"
    "ds_read_b128 v[100:103], v17 offset:1024\n"
    "ds_read_b128 v[104:107], v16 offset:2048\n"
    "ds_read_b128 v[108:111], v17 offset:2048\n"
    "ds_read_b128 v[112:115], v16 offset:6144\n"
    "ds_read_b128 v[116:119], v17 offset:6144\n"
    "ds_read_b128 v[120:123], v16 offset:7168\n"
    "ds_read_b128 v[124:127], v17 offset:7168\n"
    "ds_read_b128 v[128:131], v16 offset:8192\n"
    "ds_read_b128 v[132:135], v17 offset:8192\n"
    "v_max3_f32 v136, v160, v161, v162\n"
    "v_max3_f32 v138, v168, v169, v170\n"
    "v_max3_f32 v137, v163, v164, v165\n"
    "v_max3_f32 v139, v171, v172, v173\n"
    "v_max3_f32 v136, v136, v166, v167\n"
    "v_max3_f32 v138, v138, v174, v175\n"
    "v_max_f32 v136, v136, v137\n"
    "v_max_f32 v138, v138, v139\n"
    "v_mov_b32 v137, v136\n"
    "v_mov_b32 v139, v138\n"
    "s_nop 0\n"
    "v_permlane16_swap_b32 v136, v137\n"
    "v_permlane16_swap_b32 v138, v139\n"
    "s_waitcnt lgkmcnt(15)\n"
    "v_mfma_f32_16x16x32_bf16 v[176:179], v[64:67], a[0:3], 0\n"
    "v_mfma_f32_16x16x32_bf16 v[176:179], v[68:71], a[4:7], v[176:179]\n"
    "v_max_f32 v136, v136, v137\n"
    "v_max_f32 v138, v138, v139\n"
    "v_mov_b32 v137, v136\n"
    "v_mov_b32 v139, v138\n"
    "v_mfma_f32_16x16x32_bf16 v[176:179], v[72:75], a[8:11], v[176:179]\n"
    "v_mfma_f32_16x16x32_bf16 v[176:179], v[76:79], a[12:15], v[176:179]\n"
    "v_permlane32_swap_b32 v136, v137\n"
    "v_permlane32_swap_b32 v138, v139\n"
    "v_max_f32 v136, v136, v137\n"
    "v_mfma_f32_16x16x32_bf16 v[176:179], v[80:83], a[16:19], v[176:179]\n"
    "v_max_f32 v138, v138, v139\n"
    "v_mfma_f32_16x16x32_bf16 v[176:179], v[84:87], a[20:23], v[176:179]\n"
    "s_add_u32 m0, s60, 110592\n"
    "s_add_u32 s36, s61, 98304\n"
    "buffer_load_dwordx4 v20, s[40:43], s36 offen nt lds\n"
    "v_mul_f32 v25, s74, v136\n"
    "v_mul_f32 v26, s74, v138\n"
    "v_fma_f32 v160, v160, s62, v25\n"
    "v_fma_f32 v168, v168, s62, v26\n"
    "v_mfma_f32_16x16x32_bf16 v[184:187], v[64:67], a[24:27], 0\n"
    "v_exp_f32 v160, v160\n"
    "v_mfma_f32_16x16x32_bf16 v[184:187], v[68:71], a[28:31], v[184:187]\n"
    "v_fma_f32 v169, v169, s62, v26\n"
    "v_fma_f32 v161, v161, s62, v25\n"
    "v_fma_f32 v170, v170, s62, v26\n"
    "v_fma_f32 v162, v162, s62, v25\n"
    "v_mfma_f32_16x16x32_bf16 v[184:187], v[72:75], a[32:35], v[184:187]\n"
    "v_exp_f32 v168, v168\n"
    "s_waitcnt lgkmcnt(14)\n"
    "v_mfma_f32_16x16x32_bf16 v[184:187], v[76:79], a[36:39], v[184:187]\n"
    "v_fma_f32 v163, v163, s62, v25\n"
    "v_fma_f32 v171, v171, s62, v26\n"
    "v_fma_f32 v164, v164, s62, v25\n"
    "v_exp_f32 v169, v169\n"
    "s_waitcnt lgkmcnt(13)\n"
    "v_mfma_f32_16x16x32_bf16 v[184:187], v[80:83], a[40:43], v[184:187]\n"
    "v_fma_f32 v165, v165, s62, v25\n"
    "s_waitcnt lgkmcnt(12)\n"
    "v_mfma_f32_16x16x32_bf16 v[184:187], v[84:87], a[44:47], v[184:187]\n"
    "buffer_load_dwordx4 v22, s[40:43], s36 offen offset:1024 nt lds\n"
    "v_fma_f32 v172, v172, s62, v26\n"
    "v_exp_f32 v161, v161\n"
    "v_exp_f32 v170, v170\n"
    "v_exp_f32 v162, v162\n"
    "v_mfma_f32_16x16x32_bf16 v[180:183], v[32:35], a[0:3], 0\n"
    "v_fma_f32 v173, v173, s62, v26\n"
    "v_mfma_f32_16x16x32_bf16 v[180:183], v[36:39], a[4:7], v[180:183]\n"
    "v_exp_f32 v163, v163\n"
    "v_exp_f32 v171, v171\n"
    "v_cvt_pk_bf16_f32 v160, v160, v161\n"
    "v_mfma_f32_16x16x32_bf16 v[180:183], v[40:43], a[8:11], v[180:183]\n"
    "v_cvt_pk_bf16_f32 v168, v168, v169\n"
    "v_mfma_f32_16x16x32_bf16 v[180:183], v[44:47], a[12:15], v[180:183]\n"
    "v_fma_f32 v166, v166, s62, v25\n"
    "v_fma_f32 v174, v174, s62, v26\n"
    "v_exp_f32 v164, v164\n"
    "v_exp_f32 v172, v172\n"
    "v_mfma_f32_16x16x32_bf16 v[180:183], v[48:51], a[16:19], v[180:183]\n"
    "v_fma_f32 v167, v167, s62, v25\n"
    "v_mfma_f32_16x16x32_bf16 v[180:183], v[52:55], a[20:23], v[180:183]\n"
    "buffer_load_dwordx4 v23, s[40:43], s36 offen offset:2048 nt lds\n"
    "v_fma_f32 v175, v175, s62, v26\n"
    "v_cvt_pk_bf16_f32 v162, v162, v163\n"
    "v_exp_f32 v173, v173\n"
    "v_exp_f32 v165, v165\n"
    "v_mfma_f32_16x16x32_bf16 v[188:191], v[32:35], a[24:27], 0\n"
    "v_exp_f32 v174, v174\n"
    "v_mfma_f32_16x16x32_bf16 v[188:191], v[36:39], a[28:31], v[188:191]\n"
    "v_accvgpr_write_b32 a144, v160\n"
    "v_cvt_pk_bf16_f32 v170, v170, v171\n"
    "v_exp_f32 v166, v166\n"
    "v_accvgpr_write_b32 a148, v168\n"
    "v_mfma_f32_16x16x32_bf16 v[188:191], v[40:43], a[32:35], v[188:191]\n"
    "v_exp_f32 v167, v167\n"
    "v_mfma_f32_16x16x32_bf16 v[188:191], v[44:47], a[36:39], v[188:191]\n"
    "v_exp_f32 v175, v175\n"
    "v_cvt_pk_bf16_f32 v164, v164, v165\n"
    "v_cvt_pk_bf16_f32 v172, v172, v173\n"
    "v_mfma_f32_16x16x32_bf16 v[188:191], v[48:51], a[40:43], v[188:191]\n"
    "v_accvgpr_write_b32 a145, v162\n"
    "v_mfma_f32_16x16x32_bf16 v[188:191], v[52:55], a[44:47], v[188:191]\n"
    "s_add_u32 m0, s60, 122880\n"
    "s_add_u32 s36, s61, 135168\n"
    "buffer_load_dwordx4 v20, s[40:43], s36 offen nt lds\n"
    "v_accvgpr_write_b32 a149, v170\n"
    "v_cvt_pk_bf16_f32 v166, v166, v167\n"
    "v_cvt_pk_bf16_f32 v174, v174, v175\n"
    "v_accvgpr_write_b32 a146, v164\n"
    "s_waitcnt lgkmcnt(0)\n"
    "s_barrier\n"
    "v_mfma_f32_16x16x32_bf16 v[208:211], v[88:91], a[96:99], 0\n"
    "v_mfma_f32_16x16x32_bf16 v[208:211], v[92:95], a[100:103], v[208:211]\n"
    "v_accvgpr_write_b32 a150, v172\n"
    "v_accvgpr_write_b32 a147, v166\n"
    "v_accvgpr_write_b32 a151, v174\n"
    "v_max3_f32 v140, v176, v177, v178\n"
    "v_mfma_f32_16x16x32_bf16 v[208:211], v[96:99], a[104:107], v[208:211]\n"
    "v_mfma_f32_16x16x32_bf16 v[208:211], v[100:103], a[108:111], v[208:211]\n"
    "v_max3_f32 v142, v184, v185, v186\n"
    "v_max3_f32 v141, v179, v180, v181\n"
    "v_max3_f32 v143, v187, v188, v189\n"
    "v_max3_f32 v140, v140, v182, v183\n"
    "v_mfma_f32_16x16x32_bf16 v[208:211], v[104:107], a[112:115], v[208:211]\n"
    "v_max3_f32 v142, v142, v190, v191\n"
    "v_mfma_f32_16x16x32_bf16 v[208:211], v[108:111], a[116:119], v[208:211]\n"
    "buffer_load_dwordx4 v22, s[40:43], s36 offen offset:1024 nt lds\n"
    "v_max_f32 v140, v140, v141\n"
    "v_max_f32 v142, v142, v143\n"
    "v_mov_b32 v141, v140\n"
    "v_mov_b32 v143, v142\n"
    "v_mfma_f32_16x16x32_bf16 v[216:219], v[88:91], a[120:123], 0\n"
    "v_permlane16_swap_b32 v140, v141\n"
    "v_mfma_f32_16x16x32_bf16 v[216:219], v[92:95], a[124:127], v[216:219]\n"
    "v_permlane16_swap_b32 v142, v143\n"
    "v_max_f32 v140, v140, v141\n"
    "v_max_f32 v142, v142, v143\n"
    "v_mfma_f32_16x16x32_bf16 v[216:219], v[96:99], a[128:131], v[216:219]\n"
    "v_mov_b32 v141, v140\n"
    "v_mfma_f32_16x16x32_bf16 v[216:219], v[100:103], a[132:135], v[216:219]\n"
    "v_mov_b32 v143, v142\n"
    "v_permlane32_swap_b32 v140, v141\n"
    "s_nop 0\n"
    "v_permlane32_swap_b32 v142, v143\n"
    "v_max_f32 v140, v140, v141\n"
    "v_mfma_f32_16x16x32_bf16 v[216:219], v[104:107], a[136:139], v[216:219]\n"
    "v_max_f32 v142, v142, v143\n"
    "v_mfma_f32_16x16x32_bf16 v[216:219], v[108:111], a[140:143], v[216:219]\n"
    "buffer_load_dwordx4 v23, s[40:43], s36 offen offset:2048 nt lds\n"
    "v_mul_f32 v27, s74, v140\n"
    "v_mul_f32 v28, s74, v142\n"
    "v_fma_f32 v176, v176, s62, v27\n"
    "v_fma_f32 v184, v184, s62, v28\n"
    "v_mfma_f32_16x16x32_bf16 v[212:215], v[112:115], a[96:99], 0\n"
    "v_fma_f32 v177, v177, s62, v27\n"
    "v_mfma_f32_16x16x32_bf16 v[212:215], v[116:119], a[100:103], v[212:215]\n"
    "v_fma_f32 v185, v185, s62, v28\n"
    "v_fma_f32 v178, v178, s62, v27\n"
    "v_fma_f32 v186, v186, s62, v28\n"
    "v_exp_f32 v176, v176\n"
    "v_mfma_f32_16x16x32_bf16 v[212:215], v[120:123], a[104:107], v[212:215]\n"
    "v_exp_f32 v184, v184\n"
    "v_mfma_f32_16x16x32_bf16 v[212:215], v[124:127], a[108:111], v[212:215]\n"
    "v_fma_f32 v179, v179, s62, v27\n"
    "v_fma_f32 v187, v187, s62, v28\n"
    "v_exp_f32 v177, v177\n"
    "v_exp_f32 v185, v185\n"
    "v_mfma_f32_16x16x32_bf16 v[212:215], v[128:131], a[112:115], v[212:215]\n"
    "v_fma_f32 v180, v180, s62, v27\n"
    "v_mfma_f32_16x16x32_bf16 v[212:215], v[132:135], a[116:119], v[212:215]\n"
    "s_add_u32 m0, s60, 12288\n"
    "s_add_u32 s36, s61, 24576\n"
    "buffer_load_dwordx4 v20, s[44:47], s36 offen nt lds\n"
    "v_fma_f32 v188, v188, s62, v28\n"
    "v_exp_f32 v178, v178\n"
    "v_fma_f32 v189, v189, s62, v28\n"
    "v_fma_f32 v181, v181, s62, v27\n"
    "v_mfma_f32_16x16x32_bf16 v[220:223], v[112:115], a[120:123], 0\n"
    "v_exp_f32 v186, v186\n"
    "v_mfma_f32_16x16x32_bf16 v[220:223], v[116:119], a[124:127], v[220:223]\n"
    "v_exp_f32 v179, v179\n"
    "v_exp_f32 v187, v187\n"
    "v_cvt_pk_bf16_f32 v176, v176, v177\n"
    "v_mfma_f32_16x16x32_bf16 v[220:223], v[120:123], a[128:131], v[220:223]\n"
    "v_cvt_pk_bf16_f32 v184, v184, v185\n"
    "v_mfma_f32_16x16x32_bf16 v[220:223], v[124:127], a[132:135], v[220:223]\n"
    "v_fma_f32 v182, v182, s62, v27\n"
    "v_exp_f32 v188, v188\n"
    "v_fma_f32 v183, v183, s62, v27\n"
    "v_fma_f32 v190, v190, s62, v28\n"
    "v_mfma_f32_16x16x32_bf16 v[220:223], v[128:131], a[136:139], v[220:223]\n"
    "v_exp_f32 v180, v180\n"
    "v_mfma_f32_16x16x32_bf16 v[220:223], v[132:135], a[140:143], v[220:223]\n"
    "buffer_load_dwordx4 v22, s[44:47], s36 offen offset:1024 nt lds\n"
    "v_fma_f32 v191, v191, s62, v28\n"
    "v_exp_f32 v181, v181\n"
    "v_exp_f32 v189, v189\n"
    "v_exp_f32 v182, v182\n"
    "v_mfma_f32_16x16x32_bf16 v[224:227], v[88:91], a[0:3], 0\n"
    "v_mfma_f32_16x16x32_bf16 v[224:227], v[92:95], a[4:7], v[224:227]\n"
    "v_cvt_pk_bf16_f32 v186, v186, v187\n"
    "v_cvt_pk_bf16_f32 v178, v178, v179\n"
    "v_accvgpr_write_b32 a156, v184\n"
    "v_accvgpr_write_b32 a152, v176\n"
    "v_mfma_f32_16x16x32_bf16 v[224:227], v[96:99], a[8:11], v[224:227]\n"
    "v_mfma_f32_16x16x32_bf16 v[224:227], v[100:103], a[12:15], v[224:227]\n"
    "v_exp_f32 v190, v190\n"
    "v_cvt_pk_bf16_f32 v180, v180, v181\n"
    "v_exp_f32 v191, v191\n"
    "v_mfma_f32_16x16x32_bf16 v[224:227], v[104:107], a[16:19], v[224:227]\n"
    "v_accvgpr_write_b32 a153, v178\n"
    "v_mfma_f32_16x16x32_bf16 v[224:227], v[108:111], a[20:23], v[224:227]\n"
    "buffer_load_dwordx4 v23, s[44:47], s36 offen offset:2048 nt lds\n"
    "v_cvt_pk_bf16_f32 v188, v188, v189\n"
    "v_exp_f32 v183, v183\n"
    "v_accvgpr_write_b32 a157, v186\n"
    "v_cvt_pk_bf16_f32 v182, v182, v183\n"
    "v_mfma_f32_16x16x32_bf16 v[232:235], v[88:91], a[24:27], 0\n"
    "v_cvt_pk_bf16_f32 v190, v190, v191\n"
    "v_mfma_f32_16x16x32_bf16 v[232:235], v[92:95], a[28:31], v[232:235]\n"
    "v_accvgpr_write_b32 a154, v180\n"
    "v_accvgpr_write_b32 a158, v188\n"
    "v_accvgpr_write_b32 a155, v182\n"
    "v_accvgpr_write_b32 a159, v190\n"
    "v_mfma_f32_16x16x32_bf16 v[232:235], v[96:99], a[32:35], v[232:235]\n"
    "v_mfma_f32_16x16x32_bf16 v[232:235], v[100:103], a[36:39], v[232:235]\n"
    "v_mfma_f32_16x16x32_bf16 v[232:235], v[104:107], a[40:43], v[232:235]\n"
    "v_mfma_f32_16x16x32_bf16 v[232:235], v[108:111], a[44:47], v[232:235]\n"
    "s_add_u32 m0, s60, 24576\n"
    "s_add_u32 s36, s61, 36864\n"
    "buffer_load_dwordx4 v20, s[44:47], s36 offen nt lds\n"
    "v_mfma_f32_16x16x32_bf16 v[228:231], v[112:115], a[0:3], 0\n"
    "v_mfma_f32_16x16x32_bf16 v[228:231], v[116:119], a[4:7], v[228:231]\n"
    "v_mfma_f32_16x16x32_bf16 v[228:231], v[120:123], a[8:11], v[228:231]\n"
    "v_mfma_f32_16x16x32_bf16 v[228:231], v[124:127], a[12:15], v[228:231]\n"
    "v_mfma_f32_16x16x32_bf16 v[228:231], v[128:131], a[16:19], v[228:231]\n"
    "v_mfma_f32_16x16x32_bf16 v[228:231], v[132:135], a[20:23], v[228:231]\n"
    "buffer_load_dwordx4 v22, s[44:47], s36 offen offset:1024 nt lds\n"
    "v_mfma_f32_16x16x32_bf16 v[236:239], v[112:115], a[24:27], 0\n"
    "v_mfma_f32_16x16x32_bf16 v[236:239], v[116:119], a[28:31], v[236:239]\n"
    "v_mfma_f32_16x16x32_bf16 v[236:239], v[120:123], a[32:35], v[236:239]\n"
    "v_mfma_f32_16x16x32_bf16 v[236:239], v[124:127], a[36:39], v[236:239]\n"
    "v_mfma_f32_16x16x32_bf16 v[236:239], v[128:131], a[40:43], v[236:239]\n"
    "v_mfma_f32_16x16x32_bf16 v[236:239], v[132:135], a[44:47], v[236:239]\n"
    "buffer_load_dwordx4 v23, s[44:47], s36 offen offset:2048 nt lds\n"
    "s_add_u32 m0, s60, 36864\n"
    "s_add_u32 s36, s61, 49152\n"
    "buffer_load_dwordx4 v20, s[44:47], s36 offen nt lds\n"
    "s_waitcnt vmcnt(7)\n"
    "s_barrier\n"
    "ds_read_b128 a[48:51], v148 offset:0\n"
    "ds_read_b128 a[52:55], v149 offset:0\n"
    "ds_read_b128 a[56:59], v148 offset:1024\n"
    "ds_read_b128 a[60:63], v149 offset:1024\n"
    "ds_read_b128 a[64:67], v148 offset:2048\n"
    "ds_read_b128 a[68:71], v149 offset:2048\n"
    "ds_read_b128 a[72:75], v148 offset:6144\n"
    "ds_read_b128 a[76:79], v149 offset:6144\n"
    "ds_read_b128 a[80:83], v148 offset:7168\n"
    "ds_read_b128 a[84:87], v149 offset:7168\n"
    "ds_read_b128 a[88:91], v148 offset:8192\n"
    "ds_read_b128 a[92:95], v149 offset:8192\n"
    "s_waitcnt lgkmcnt(11)\n"
    "v_mfma_f32_16x16x32_bf16 v[192:195], v[64:67], a[48:51], 0\n"
    "s_waitcnt lgkmcnt(10)\n"
    "v_mfma_f32_16x16x32_bf16 v[192:195], v[68:71], a[52:55], v[192:195]\n"
    "s_waitcnt lgkmcnt(9)\n"
    "v_mfma_f32_16x16x32_bf16 v[192:195], v[72:75], a[56:59], v[192:195]\n"
    "s_waitcnt lgkmcnt(8)\n"
    "v_mfma_f32_16x16x32_bf16 v[192:195], v[76:79], a[60:63], v[192:195]\n"
    "buffer_load_dwordx4 v22, s[44:47], s36 offen offset:1024 nt lds\n"
    "s_waitcnt lgkmcnt(7)\n"
    "v_mfma_f32_16x16x32_bf16 v[192:195], v[80:83], a[64:67], v[192:195]\n"
    "s_waitcnt lgkmcnt(6)\n"
    "v_mfma_f32_16x16x32_bf16 v[192:195], v[84:87], a[68:71], v[192:195]\n"
    "s_waitcnt lgkmcnt(5)\n"
    "v_mfma_f32_16x16x32_bf16 v[200:203], v[64:67], a[72:75], 0\n"
    "s_waitcnt lgkmcnt(4)\n"
    "v_mfma_f32_16x16x32_bf16 v[200:203], v[68:71], a[76:79], v[200:203]\n"
    "buffer_load_dwordx4 v23, s[44:47], s36 offen offset:2048 nt lds\n"
    "s_waitcnt lgkmcnt(3)\n"
    "v_mfma_f32_16x16x32_bf16 v[200:203], v[72:75], a[80:83], v[200:203]\n"
    "s_waitcnt lgkmcnt(2)\n"
    "v_mfma_f32_16x16x32_bf16 v[200:203], v[76:79], a[84:87], v[200:203]\n"
    "s_waitcnt lgkmcnt(1)\n"
    "v_mfma_f32_16x16x32_bf16 v[200:203], v[80:83], a[88:91], v[200:203]\n"
    "s_waitcnt lgkmcnt(0)\n"
    "v_mfma_f32_16x16x32_bf16 v[200:203], v[84:87], a[92:95], v[200:203]\n"
    "s_add_u32 m0, s60, 49152\n"
    "s_add_u32 s36, s61, 61440\n"
    "buffer_load_dwordx4 v20, s[44:47], s36 offen nt lds\n"
    "v_mfma_f32_16x16x32_bf16 v[196:199], v[32:35], a[48:51], 0\n"
    "v_mfma_f32_16x16x32_bf16 v[196:199], v[36:39], a[52:55], v[196:199]\n"
    "v_mfma_f32_16x16x32_bf16 v[196:199], v[40:43], a[56:59], v[196:199]\n"
    "v_mfma_f32_16x16x32_bf16 v[196:199], v[44:47], a[60:63], v[196:199]\n"
    "buffer_load_dwordx4 v22, s[44:47], s36 offen offset:1024 nt lds\n"
    "v_mfma_f32_16x16x32_bf16 v[196:199], v[48:51], a[64:67], v[196:199]\n"
    "v_mfma_f32_16x16x32_bf16 v[196:199], v[52:55], a[68:71], v[196:199]\n"
    "v_mfma_f32_16x16x32_bf16 v[204:207], v[32:35], a[72:75], 0\n"
    "v_mfma_f32_16x16x32_bf16 v[204:207], v[36:39], a[76:79], v[204:207]\n"
    "buffer_load_dwordx4 v23, s[44:47], s36 offen offset:2048 nt lds\n"
    "v_mfma_f32_16x16x32_bf16 v[204:207], v[40:43], a[80:83], v[204:207]\n"
    "v_mfma_f32_16x16x32_bf16 v[204:207], v[44:47], a[84:87], v[204:207]\n"
    "v_mfma_f32_16x16x32_bf16 v[204:207], v[48:51], a[88:91], v[204:207]\n"
    "v_mfma_f32_16x16x32_bf16 v[204:207], v[52:55], a[92:95], v[204:207]\n"
    "buffer_store_dwordx4 v[112:115], v24, s[52:55], 0 offen\n"
    "v_mfma_f32_16x16x32_bf16 v[240:243], v[88:91], a[48:51], 0\n"
    "v_mfma_f32_16x16x32_bf16 v[240:243], v[92:95], a[52:55], v[240:243]\n"
    "v_max3_f32 v144, v192, v193, v194\n"
    "v_max3_f32 v146, v200, v201, v202\n"
    "v_max3_f32 v145, v195, v196, v197\n"
    "s_nop 1\n"
    "v_max3_f32 v147, v203, v204, v205\n"
    "v_max3_f32 v144, v144, v198, v199\n"
    "v_mfma_f32_16x16x32_bf16 v[240:243], v[96:99], a[56:59], v[240:243]\n"
    "v_mfma_f32_16x16x32_bf16 v[240:243], v[100:103], a[60:63], v[240:243]\n"
    "s_add_u32 m0, s60, 61440\n"
    "s_add_u32 s36, s61, 73728\n"
    "buffer_load_dwordx4 v20, s[44:47], s36 offen nt lds\n"
    "v_max3_f32 v146, v146, v206, v207\n"
    "v_max_f32 v144, v144, v145\n"
    "v_max_f32 v146, v146, v147\n"
    "v_mov_b32 v145, v144\n"
    "v_mov_b32 v147, v146\n"
    "v_mfma_f32_16x16x32_bf16 v[240:243], v[104:107], a[64:67], v[240:243]\n"
    "v_permlane16_swap_b32 v144, v145\n"
    "v_mfma_f32_16x16x32_bf16 v[240:243], v[108:111], a[68:71], v[240:243]\n"
    "v_permlane16_swap_b32 v146, v147\n"
    "v_max_f32 v144, v144, v145\n"
    "v_max_f32 v146, v146, v147\n"
    "v_mov_b32 v145, v144\n"
    "v_mfma_f32_16x16x32_bf16 v[248:251], v[88:91], a[72:75], 0\n"
    "v_mov_b32 v147, v146\n"
    "v_mfma_f32_16x16x32_bf16 v[248:251], v[92:95], a[76:79], v[248:251]\n"
    "buffer_store_dwordx4 v[112:115], v24, s[52:55], 0 offen\n"
    "v_permlane32_swap_b32 v144, v145\n"
    "v_permlane32_swap_b32 v146, v147\n"
    "v_max_f32 v144, v144, v145\n"
    "v_max_f32 v146, v146, v147\n"
    "v_mfma_f32_16x16x32_bf16 v[248:251], v[96:99], a[80:83], v[248:251]\n"
    "v_mul_f32 v29, s74, v144\n"
    "v_mfma_f32_16x16x32_bf16 v[248:251], v[100:103], a[84:87], v[248:251]\n"
    "v_mul_f32 v30, s74, v146\n"
    "v_fma_f32 v192, v192, s62, v29\n"
    "v_fma_f32 v200, v200, s62, v30\n"
    "v_fma_f32 v193, v193, s62, v29\n"
    "v_fma_f32 v201, v201, s62, v30\n"
    "v_mfma_f32_16x16x32_bf16 v[248:251], v[104:107], a[88:91], v[248:251]\n"
    "v_fma_f32 v194, v194, s62, v29\n"
    "v_mfma_f32_16x16x32_bf16 v[248:251], v[108:111], a[92:95], v[248:251]\n"
    "buffer_load_dwordx4 v22, s[44:47], s36 offen offset:1024 nt lds\n"
    "v_exp_f32 v192, v192\n"
    "v_fma_f32 v195, v195, s62, v29\n"
    "v_exp_f32 v193, v193\n"
    "v_exp_f32 v194, v194\n"
    "v_mfma_f32_16x16x32_bf16 v[244:247], v[112:115], a[48:51], 0\n"
    "v_fma_f32 v196, v196, s62, v29\n"
    "v_mfma_f32_16x16x32_bf16 v[244:247], v[116:119], a[52:55], v[244:247]\n"
    "v_fma_f32 v197, v197, s62, v29\n"
    "v_cvt_pk_bf16_f32 v192, v192, v193\n"
    "v_exp_f32 v195, v195\n"
    "v_fma_f32 v198, v198, s62, v29\n"
    "v_exp_f32 v196, v196\n"
    "v_mfma_f32_16x16x32_bf16 v[244:247], v[120:123], a[56:59], v[244:247]\n"
    "v_exp_f32 v197, v197\n"
    "v_mfma_f32_16x16x32_bf16 v[244:247], v[124:127], a[60:63], v[244:247]\n"
    "buffer_load_dwordx4 v23, s[44:47], s36 offen offset:2048 nt lds\n"
    "v_fma_f32 v199, v199, s62, v29\n"
    "v_cvt_pk_bf16_f32 v194, v194, v195\n"
    "v_accvgpr_write_b32 a160, v192\n"
    "v_exp_f32 v198, v198\n"
    "v_exp_f32 v199, v199\n"
    "v_mfma_f32_16x16x32_bf16 v[244:247], v[128:131], a[64:67], v[244:247]\n"
    "v_cvt_pk_bf16_f32 v196, v196, v197\n"
    "v_mfma_f32_16x16x32_bf16 v[244:247], v[132:135], a[68:71], v[244:247]\n"
    "v_mfma_f32_16x16x32_bf16 v[252:255], v[112:115], a[72:75], 0\n"
    "v_mfma_f32_16x16x32_bf16 v[252:255], v[116:119], a[76:79], v[252:255]\n"
    "buffer_store_dwordx4 v[112:115], v24, s[52:55], 0 offen\n"
    "v_mfma_f32_16x16x32_bf16 v[252:255], v[120:123], a[80:83], v[252:255]\n"
    "v_mfma_f32_16x16x32_bf16 v[252:255], v[124:127], a[84:87], v[252:255]\n"
    "v_mfma_f32_16x16x32_bf16 v[252:255], v[128:131], a[88:91], v[252:255]\n"
    "v_mfma_f32_16x16x32_bf16 v[252:255], v[132:135], a[92:95], v[252:255]\n"
    "s_add_u32 m0, s60, 73728\n"
    "s_add_u32 s36, s61, 86016\n"
    "buffer_load_dwordx4 v20, s[44:47], s36 offen nt lds\n"
    "s_waitcnt vmcnt(13)\n"
    "s_barrier\n"
    "ds_read_b128 v[0:3], v16 offset:12288\n"
    "ds_read_b128 v[4:7], v17 offset:12288\n"
    "ds_read_b128 v[8:11], v16 offset:13312\n"
    "s_waitcnt lgkmcnt(0)\n"
    "s_add_u32 s94, s94, 791\n"
    "s_mul_i32 s66, %2, 77\n"
    "s_and_b32 s66, s66, 255\n"
    "s_mul_i32 s66, s66, 400\n"
    "s_lshr_b32 s66, s66, 8\n"
    "s_sub_u32 s94, s94, s66\n"
    "s_waitcnt lgkmcnt(0)\n"
    "s_nop 7\n"
    "s_branch L_ent_%=\n"
    "L_top_%=:\n"
    "v_mfma_f32_16x16x32_bf16 v[160:163], v[64:67], a[96:99], 0\n"
    "v_accvgpr_read_b32 v56, a80\n"
    "v_accvgpr_read_b32 v57, a81\n"
    "v_mfma_f32_16x16x32_bf16 v[160:163], v[68:71], a[100:103], v[160:163]\n"
    "ds_read_b128 v[32:35], v16 offset:6144\n"
    "v_accvgpr_read_b32 v58, a82\n"
    "v_mfma_f32_16x16x32_bf16 v[160:163], v[72:75], a[104:107], v[160:163]\n"
    "v_accvgpr_read_b32 v59, a83\n"
    "v_mul_f32 v56, v56, v29\n"
    "v_mfma_f32_16x16x32_bf16 v[160:163], v[76:79], a[108:111], v[160:163]\n"
    "ds_read_b128 v[36:39], v17 offset:6144\n"
    "v_mul_f32 v57, v57, v29\n"
    "v_mfma_f32_16x16x32_bf16 v[160:163], v[80:83], a[112:115], v[160:163]\n"
    "v_mul_f32 v58, v58, v29\n"
    "v_mul_f32 v59, v59, v29\n"
    "v_mfma_f32_16x16x32_bf16 v[160:163], v[84:87], a[116:119], v[160:163]\n"
    "ds_read_b128 v[40:43], v16 offset:7168\n"
    "v_cvt_pk_bf16_f32 v104, v56, v57\n"
    "v_mfma_f32_16x16x32_bf16 v[168:171], v[64:67], a[120:123], 0\n"
    "v_cvt_pk_bf16_f32 v105, v58, v59\n"
    "v_accvgpr_read_b32 v60, a84\n"
    "v_mfma_f32_16x16x32_bf16 v[168:171], v[68:71], a[124:127], v[168:171]\n"
    "ds_read_b128 v[44:47], v17 offset:7168\n"
    "v_accvgpr_read_b32 v61, a85\n"
    "v_mfma_f32_16x16x32_bf16 v[168:171], v[72:75], a[128:131], v[168:171]\n"
    "v_accvgpr_read_b32 v62, a86\n"
    "v_accvgpr_read_b32 v63, a87\n"
    "v_mfma_f32_16x16x32_bf16 v[168:171], v[76:79], a[132:135], v[168:171]\n"
    "ds_read_b128 v[48:51], v16 offset:8192\n"
    "s_add_u32 m0, s60, 36864\n"
    "v_mul_f32 v60, v60, v29\n"
    "v_mfma_f32_16x16x32_bf16 v[168:171], v[80:83], a[136:139], v[168:171]\n"
    "v_mul_f32 v61, v61, v29\n"
    "v_mul_f32 v62, v62, v29\n"
    "v_mfma_f32_16x16x32_bf16 v[168:171], v[84:87], a[140:143], v[168:171]\n"
    "ds_read_b128 v[52:55], v17 offset:8192\n"
    "s_add_u32 s36, s61, 49152\n"
    "v_mul_f32 v63, v63, v29\n"
    "v_mfma_f32_16x16x32_bf16 v[176:179], v[64:67], a[0:3], 0\n"
    "v_cvt_pk_bf16_f32 v106, v60, v61\n"
    "v_cvt_pk_bf16_f32 v107, v62, v63\n"
    "v_mfma_f32_16x16x32_bf16 v[176:179], v[68:71], a[4:7], v[176:179]\n"
    "buffer_load_dwordx4 v20, s[44:47], s36 offen nt lds\n"
    "v_mfma_f32_16x16x32_bf16 v[176:179], v[72:75], a[8:11], v[176:179]\n"
    "v_accvgpr_read_b32 v56, a88\n"
    "v_accvgpr_read_b32 v57, a89\n"
    "v_mfma_f32_16x16x32_bf16 v[176:179], v[76:79], a[12:15], v[176:179]\n"
    "v_accvgpr_read_b32 v58, a90\n"
    "v_accvgpr_read_b32 v59, a91\n"
    "v_mfma_f32_16x16x32_bf16 v[176:179], v[80:83], a[16:19], v[176:179]\n"
    "v_mul_f32 v56, v56, v30\n"
    "v_mul_f32 v57, v57, v30\n"
    "v_mfma_f32_16x16x32_bf16 v[176:179], v[84:87], a[20:23], v[176:179]\n"
    "v_mul_f32 v58, v58, v30\n"
    "v_mul_f32 v59, v59, v30\n"
    "v_mfma_f32_16x16x32_bf16 v[184:187], v[64:67], a[24:27], 0\n"
    "v_cvt_pk_bf16_f32 v108, v56, v57\n"
    "v_cvt_pk_bf16_f32 v109, v58, v59\n"
    "v_mfma_f32_16x16x32_bf16 v[184:187], v[68:71], a[28:31], v[184:187]\n"
    "v_accvgpr_read_b32 v60, a92\n"
    "v_accvgpr_read_b32 v61, a93\n"
    "v_mfma_f32_16x16x32_bf16 v[184:187], v[72:75], a[32:35], v[184:187]\n"
    "v_accvgpr_read_b32 v62, a94\n"
    "v_accvgpr_read_b32 v63, a95\n"
    "v_mfma_f32_16x16x32_bf16 v[184:187], v[76:79], a[36:39], v[184:187]\n"
    "s_waitcnt vmcnt(17)\n"
    "s_barrier\n"
    "v_mul_f32 v60, v60, v30\n"
    "v_mfma_f32_16x16x32_bf16 v[184:187], v[80:83], a[40:43], v[184:187]\n"
    "v_mul_f32 v61, v61, v30\n"
    "v_mul_f32 v62, v62, v30\n"
    "v_mfma_f32_16x16x32_bf16 v[184:187], v[84:87], a[44:47], v[184:187]\n"
    "s_waitcnt lgkmcnt(0)\n"
    "buffer_load_dwordx4 v22, s[44:47], s36 offen offset:1024 nt lds\n"
    "v_mfma_f32_16x16x32_bf16 v[164:167], v[32:35], a[96:99], 0\n"
    "v_mul_f32 v63, v63, v30\n"
    "v_cvt_pk_bf16_f32 v110, v60, v61\n"
    "v_mfma_f32_16x16x32_bf16 v[164:167], v[36:39], a[100:103], v[164:167]\n"
    "v_cvt_pk_bf16_f32 v111, v62, v63\n"
    "s_mov_b64 vcc, s[96:97]\n"
    "v_cndmask_b32_dpp v112, v88, v212, vcc row_ror:8 row_mask:0xf bank_mask:0xf\n"
    "v_mfma_f32_16x16x32_bf16 v[164:167], v[40:43], a[104:107], v[164:167]\n"
    "v_cndmask_b32_dpp v113, v89, v213, vcc row_ror:8 row_mask:0xf bank_mask:0xf\n"
    "v_cndmask_b32_dpp v114, v90, v214, vcc row_ror:8 row_mask:0xf bank_mask:0xf\n"
    "v_mfma_f32_16x16x32_bf16 v[164:167], v[44:47], a[108:111], v[164:167]\n"
    "v_cndmask_b32_dpp v115, v91, v215, vcc row_ror:8 row_mask:0xf bank_mask:0xf\n"
    "v_cndmask_b32_dpp v116, v92, v220, vcc row_ror:8 row_mask:0xf bank_mask:0xf\n"
    "v_cndmask_b32_dpp v117, v93, v221, vcc row_ror:8 row_mask:0xf bank_mask:0xf\n"
    "v_mfma_f32_16x16x32_bf16 v[164:167], v[48:51], a[112:115], v[164:167]\n"
    "v_cndmask_b32_dpp v118, v94, v222, vcc row_ror:8 row_mask:0xf bank_mask:0xf\n"
    "v_cndmask_b32_dpp v119, v95, v223, vcc row_ror:8 row_mask:0xf bank_mask:0xf\n"
    "v_mfma_f32_16x16x32_bf16 v[164:167], v[52:55], a[116:119], v[164:167]\n"
    "v_cndmask_b32_dpp v120, v96, v228, vcc row_ror:8 row_mask:0xf bank_mask:0xf\n"
    "v_cndmask_b32_dpp v121, v97, v229, vcc row_ror:8 row_mask:0xf bank_mask:0xf\n"
    "v_mfma_f32_16x16x32_bf16 v[172:175], v[32:35], a[120:123], 0\n"
    "v_cndmask_b32_dpp v122, v98, v230, vcc row_ror:8 row_mask:0xf bank_mask:0xf\n"
    "v_cndmask_b32_dpp v123, v99, v231, vcc row_ror:8 row_mask:0xf bank_mask:0xf\n"
    "v_mfma_f32_16x16x32_bf16 v[172:175], v[36:39], a[124:127], v[172:175]\n"
    "v_cndmask_b32_dpp v124, v100, v236, vcc row_ror:8 row_mask:0xf bank_mask:0xf\n"
    "v_cndmask_b32_dpp v125, v101, v237, vcc row_ror:8 row_mask:0xf bank_mask:0xf\n"
    "v_cndmask_b32_dpp v126, v102, v238, vcc row_ror:8 row_mask:0xf bank_mask:0xf\n"
    "v_mfma_f32_16x16x32_bf16 v[172:175], v[40:43], a[128:131], v[172:175]\n"
    "v_cndmask_b32_dpp v127, v103, v239, vcc row_ror:8 row_mask:0xf bank_mask:0xf\n"
    "v_cndmask_b32_dpp v128, v104, v244, vcc row_ror:8 row_mask:0xf bank_mask:0xf\n"
    "v_mfma_f32_16x16x32_bf16 v[172:175], v[44:47], a[132:135], v[172:175]\n"
    "buffer_load_dwordx4 v23, s[44:47], s36 offen offset:2048 nt lds\n"
    "v_mfma_f32_16x16x32_bf16 v[172:175], v[48:51], a[136:139], v[172:175]\n"
    "v_cndmask_b32_dpp v129, v105, v245, vcc row_ror:8 row_mask:0xf bank_mask:0xf\n"
    "v_cndmask_b32_dpp v130, v106, v246, vcc row_ror:8 row_mask:0xf bank_mask:0xf\n"
    "v_mfma_f32_16x16x32_bf16 v[172:175], v[52:55], a[140:143], v[172:175]\n"
    "v_cndmask_b32_dpp v131, v107, v247, vcc row_ror:8 row_mask:0xf bank_mask:0xf\n"
    "v_cndmask_b32_dpp v132, v108, v252, vcc row_ror:8 row_mask:0xf bank_mask:0xf\n"
    "v_cndmask_b32_dpp v133, v109, v253, vcc row_ror:8 row_mask:0xf bank_mask:0xf\n"
    "v_cndmask_b32_dpp v134, v110, v254, vcc row_ror:8 row_mask:0xf bank_mask:0xf\n"
    "v_cndmask_b32_dpp v135, v111, v255, vcc row_ror:8 row_mask:0xf bank_mask:0xf\n"
    "v_mfma_f32_16x16x32_bf16 v[180:183], v[32:35], a[0:3], 0\n"
    "v_mfma_f32_16x16x32_bf16 v[180:183], v[36:39], a[4:7], v[180:183]\n"
    "ds_read_b128 a[48:51], v148 offset:0\n"
    "ds_read_b128 a[52:55], v149 offset:0\n"
    "v_mfma_f32_16x16x32_bf16 v[180:183], v[40:43], a[8:11], v[180:183]\n"
    "v_mfma_f32_16x16x32_bf16 v[180:183], v[44:47], a[12:15], v[180:183]\n"
    "ds_read_b128 a[56:59], v148 offset:1024\n"
    "ds_read_b128 a[60:63], v149 offset:1024\n"
    "ds_read_b128 a[64:67], v148 offset:2048\n"
    "ds_read_b128 a[68:71], v149 offset:2048\n"
    "v_mfma_f32_16x16x32_bf16 v[180:183], v[48:51], a[16:19], v[180:183]\n"
    "v_mfma_f32_16x16x32_bf16 v[180:183], v[52:55], a[20:23], v[180:183]\n"
    "s_add_u32 m0, s60, 49152\n"
    "ds_read_b128 a[72:75], v148 offset:6144\n"
    "v_mfma_f32_16x16x32_bf16 v[188:191], v[32:35], a[24:27], 0\n"
    "v_mfma_f32_16x16x32_bf16 v[188:191], v[36:39], a[28:31], v[188:191]\n"
    "s_add_u32 s36, s61, 61440\n"
    "ds_read_b128 a[76:79], v149 offset:6144\n"
    "v_mfma_f32_16x16x32_bf16 v[188:191], v[40:43], a[32:35], v[188:191]\n"
    "v_mfma_f32_16x16x32_bf16 v[188:191], v[44:47], a[36:39], v[188:191]\n"
    "buffer_load_dwordx4 v20, s[44:47], s36 offen nt lds\n"
    "ds_read_b128 a[80:83], v148 offset:7168\n"
    "ds_read_b128 a[84:87], v149 offset:7168\n"
    "ds_read_b128 a[88:91], v148 offset:8192\n"
    "ds_read_b128 a[92:95], v149 offset:8192\n"
    "v_mfma_f32_16x16x32_bf16 v[188:191], v[48:51], a[40:43], v[188:191]\n"
    "v_mfma_f32_16x16x32_bf16 v[188:191], v[52:55], a[44:47], v[188:191]\n"
    "s_mov_b64 vcc, s[98:99]\n"
    "v_cndmask_b32_dpp v88, v212, v88, vcc row_ror:8 row_mask:0xf bank_mask:0xf\n"
    "s_waitcnt lgkmcnt(11)\n"
    "v_mfma_f32_16x16x32_bf16 v[192:195], v[64:67], a[48:51], 0\n"
    "v_cndmask_b32_dpp v89, v213, v89, vcc row_ror:8 row_mask:0xf bank_mask:0xf\n"
    "v_cndmask_b32_dpp v90, v214, v90, vcc row_ror:8 row_mask:0xf bank_mask:0xf\n"
    "s_waitcnt lgkmcnt(10)\n"
    "v_mfma_f32_16x16x32_bf16 v[192:195], v[68:71], a[52:55], v[192:195]\n"
    "v_cndmask_b32_dpp v91, v215, v91, vcc row_ror:8 row_mask:0xf bank_mask:0xf\n"
    "v_cndmask_b32_dpp v92, v220, v92, vcc row_ror:8 row_mask:0xf bank_mask:0xf\n"
    "s_waitcnt lgkmcnt(9)\n"
    "v_mfma_f32_16x16x32_bf16 v[192:195], v[72:75], a[56:59], v[192:195]\n"
    "v_cndmask_b32_dpp v93, v221, v93, vcc row_ror:8 row_mask:0xf bank_mask:0xf\n"
    "v_cndmask_b32_dpp v94, v222, v94, vcc row_ror:8 row_mask:0xf bank_mask:0xf\n"
    "s_waitcnt lgkmcnt(8)\n"
    "v_mfma_f32_16x16x32_bf16 v[192:195], v[76:79], a[60:63], v[192:195]\n"
    "v_subrev_u32 v16, 147456, v16\n"
    "v_cndmask_b32_dpp v95, v223, v95, vcc row_ror:8 row_mask:0xf bank_mask:0xf\n"
    "s_waitcnt lgkmcnt(7)\n"
    "v_mfma_f32_16x16x32_bf16 v[192:195], v[80:83], a[64:67], v[192:195]\n"
    "v_cndmask_b32_dpp v96, v228, v96, vcc row_ror:8 row_mask:0xf bank_mask:0xf\n"
    "v_cndmask_b32_dpp v97, v229, v97, vcc row_ror:8 row_mask:0xf bank_mask:0xf\n"
    "s_waitcnt lgkmcnt(6)\n"
    "v_mfma_f32_16x16x32_bf16 v[192:195], v[84:87], a[68:71], v[192:195]\n"
    "v_subrev_u32 v17, 147456, v17\n"
    "v_cndmask_b32_dpp v98, v230, v98, vcc row_ror:8 row_mask:0xf bank_mask:0xf\n"
    "s_waitcnt lgkmcnt(5)\n"
    "v_mfma_f32_16x16x32_bf16 v[200:203], v[64:67], a[72:75], 0\n"
    "v_cndmask_b32_dpp v99, v231, v99, vcc row_ror:8 row_mask:0xf bank_mask:0xf\n"
    "v_cndmask_b32_dpp v100, v236, v100, vcc row_ror:8 row_mask:0xf bank_mask:0xf\n"
    "s_waitcnt lgkmcnt(4)\n"
    "v_mfma_f32_16x16x32_bf16 v[200:203], v[68:71], a[76:79], v[200:203]\n"
    "v_cndmask_b32_dpp v101, v237, v101, vcc row_ror:8 row_mask:0xf bank_mask:0xf\n"
    "v_cndmask_b32_dpp v102, v238, v102, vcc row_ror:8 row_mask:0xf bank_mask:0xf\n"
    "s_waitcnt lgkmcnt(3)\n"
    "v_mfma_f32_16x16x32_bf16 v[200:203], v[72:75], a[80:83], v[200:203]\n"
    "v_cndmask_b32_dpp v103, v239, v103, vcc row_ror:8 row_mask:0xf bank_mask:0xf\n"
    "v_cndmask_b32_dpp v104, v244, v104, vcc row_ror:8 row_mask:0xf bank_mask:0xf\n"
    "s_waitcnt lgkmcnt(2)\n"
    "v_mfma_f32_16x16x32_bf16 v[200:203], v[76:79], a[84:87], v[200:203]\n"
    "v_cndmask_b32_dpp v105, v245, v105, vcc row_ror:8 row_mask:0xf bank_mask:0xf\n"
    "v_cndmask_b32_dpp v106, v246, v106, vcc row_ror:8 row_mask:0xf bank_mask:0xf\n"
    "s_waitcnt lgkmcnt(1)\n"
    "v_mfma_f32_16x16x32_bf16 v[200:203], v[80:83], a[88:91], v[200:203]\n"
    "v_cndmask_b32_dpp v107, v247, v107, vcc row_ror:8 row_mask:0xf bank_mask:0xf\n"
    "v_cndmask_b32_dpp v108, v252, v108, vcc row_ror:8 row_mask:0xf bank_mask:0xf\n"
    "s_waitcnt lgkmcnt(0)\n"
    "v_mfma_f32_16x16x32_bf16 v[200:203], v[84:87], a[92:95], v[200:203]\n"
    "buffer_load_dwordx4 v22, s[44:47], s36 offen offset:1024 nt lds\n"
    "v_mfma_f32_16x16x32_bf16 v[196:199], v[32:35], a[48:51], 0\n"
    "v_cndmask_b32_dpp v109, v253, v109, vcc row_ror:8 row_mask:0xf bank_mask:0xf\n"
    "v_cndmask_b32_dpp v110, v254, v110, vcc row_ror:8 row_mask:0xf bank_mask:0xf\n"
    "v_mfma_f32_16x16x32_bf16 v[196:199], v[36:39], a[52:55], v[196:199]\n"
    "s_waitcnt vmcnt(13)\n"
    "s_barrier\n"
    "v_cndmask_b32_dpp v111, v255, v111, vcc row_ror:8 row_mask:0xf bank_mask:0xf\n"
    "v_mfma_f32_16x16x32_bf16 v[196:199], v[40:43], a[56:59], v[196:199]\n"
    "v_max3_f32 v136, v160, v161, v162\n"
    "v_max3_f32 v137, v163, v164, v165\n"
    "v_mfma_f32_16x16x32_bf16 v[196:199], v[44:47], a[60:63], v[196:199]\n"
    "ds_read_b128 v[0:3], v16 offset:0\n"
    "v_max3_f32 v136, v136, v166, v167\n"
    "v_mfma_f32_16x16x32_bf16 v[196:199], v[48:51], a[64:67], v[196:199]\n"
    "v_max_f32 v136, v136, v137\n"
    "v_mov_b32 v137, v136\n"
    "v_mfma_f32_16x16x32_bf16 v[196:199], v[52:55], a[68:71], v[196:199]\n"
    "ds_read_b128 v[4:7], v17 offset:0\n"
    "v_max3_f32 v138, v168, v169, v170\n"
    "v_mfma_f32_16x16x32_bf16 v[204:207], v[32:35], a[72:75], 0\n"
    "v_max3_f32 v139, v171, v172, v173\n"
    "v_max3_f32 v138, v138, v174, v175\n"
    "v_mfma_f32_16x16x32_bf16 v[204:207], v[36:39], a[76:79], v[204:207]\n"
    "v_max_f32 v138, v138, v139\n"
    "v_mov_b32 v139, v138\n"
    "v_mfma_f32_16x16x32_bf16 v[204:207], v[40:43], a[80:83], v[204:207]\n"
    "v_max3_f32 v140, v176, v177, v178\n"
    "v_max3_f32 v141, v179, v180, v181\n"
    "v_mfma_f32_16x16x32_bf16 v[204:207], v[44:47], a[84:87], v[204:207]\n"
    "v_max3_f32 v140, v140, v182, v183\n"
    "v_max_f32 v140, v140, v141\n"
    "v_mov_b32 v141, v140\n"
    "v_max3_f32 v142, v184, v185, v186\n"
    "v_max3_f32 v143, v187, v188, v189\n"
    "v_mfma_f32_16x16x32_bf16 v[204:207], v[48:51], a[88:91], v[204:207]\n"
    "v_max3_f32 v142, v142, v190, v191\n"
    "v_max_f32 v142, v142, v143\n"
    "v_mfma_f32_16x16x32_bf16 v[204:207], v[52:55], a[92:95], v[204:207]\n"
    "s_waitcnt lgkmcnt(0)\n"
    "ds_read_b128 v[8:11], v16 offset:1024\n"
    "v_mov_b32 v143, v142\n"
    "v_permlane16_swap_b32 v136, v137\n"
    "v_mfma_f32_16x16x32_bf16 v[208:211], v[0:3], a[96:99], 0\n"
    "v_permlane16_swap_b32 v138, v139\n"
    "v_mfma_f32_16x16x32_bf16 v[208:211], v[4:7], a[100:103], v[208:211]\n"
    "ds_read_b128 v[12:15], v17 offset:1024\n"
    "v_permlane16_swap_b32 v140, v141\n"
    "v_permlane16_swap_b32 v142, v143\n"
    "v_mfma_f32_16x16x32_bf16 v[216:219], v[0:3], a[120:123], 0\n"
    "v_max_f32 v136, v136, v137\n"
    "v_max_f32 v138, v138, v139\n"
    "v_mfma_f32_16x16x32_bf16 v[216:219], v[4:7], a[124:127], v[216:219]\n"
    "buffer_load_dwordx4 v23, s[44:47], s36 offen offset:2048 nt lds\n"
    "v_max_f32 v140, v140, v141\n"
    "v_max_f32 v142, v142, v143\n"
    "v_mfma_f32_16x16x32_bf16 v[224:227], v[0:3], a[0:3], 0\n"
    "v_mov_b32 v137, v136\n"
    "v_mov_b32 v139, v138\n"
    "v_mfma_f32_16x16x32_bf16 v[224:227], v[4:7], a[4:7], v[224:227]\n"
    "v_mov_b32 v141, v140\n"
    "v_mov_b32 v143, v142\n"
    "v_permlane32_swap_b32 v136, v137\n"
    "v_permlane32_swap_b32 v138, v139\n"
    "v_mfma_f32_16x16x32_bf16 v[232:235], v[0:3], a[24:27], 0\n"
    "v_max_f32 v136, v136, v137\n"
    "v_max_f32 v138, v138, v139\n"
    "v_mfma_f32_16x16x32_bf16 v[232:235], v[4:7], a[28:31], v[232:235]\n"
    "s_mov_b32 s56, 256\n"
    "v_permlane32_swap_b32 v140, v141\n"
    "v_permlane32_swap_b32 v142, v143\n"
    "v_mfma_f32_16x16x32_bf16 v[240:243], v[0:3], a[48:51], 0\n"
    "v_max_f32 v140, v140, v141\n"
    "v_max_f32 v142, v142, v143\n"
    "v_mfma_f32_16x16x32_bf16 v[240:243], v[4:7], a[52:55], v[240:243]\n"
    "buffer_store_dwordx4 v[112:115], v24, s[52:55], s56 offen sc0 sc1\n"
    "v_mfma_f32_16x16x32_bf16 v[248:251], v[0:3], a[72:75], 0\n"
    "v_mul_f32 v25, s74, v136\n"
    "v_mul_f32 v26, s74, v138\n"
    "v_mfma_f32_16x16x32_bf16 v[248:251], v[4:7], a[76:79], v[248:251]\n"
    "s_waitcnt lgkmcnt(0)\n"
    "ds_read_b128 v[0:3], v16 offset:2048\n"
    "s_add_u32 m0, s60, 61440\n"
    "v_mul_f32 v27, s74, v140\n"
    "v_mul_f32 v28, s74, v142\n"
    "v_fma_f32 v160, v160, s62, v25\n"
    "v_mfma_f32_16x16x32_bf16 v[208:211], v[8:11], a[104:107], v[208:211]\n"
    "v_exp_f32 v160, v160\n"
    "v_mfma_f32_16x16x32_bf16 v[208:211], v[12:15], a[108:111], v[208:211]\n"
    "ds_read_b128 v[4:7], v17 offset:2048\n"
    "s_add_u32 s36, s61, 73728\n"
    "v_fma_f32 v161, v161, s62, v25\n"
    "v_fma_f32 v162, v162, s62, v25\n"
    "v_fma_f32 v163, v163, s62, v25\n"
    "v_mfma_f32_16x16x32_bf16 v[216:219], v[8:11], a[128:131], v[216:219]\n"
    "v_fma_f32 v164, v164, s62, v25\n"
    "v_fma_f32 v165, v165, s62, v25\n"
    "v_mfma_f32_16x16x32_bf16 v[216:219], v[12:15], a[132:135], v[216:219]\n"
    "buffer_load_dwordx4 v20, s[44:47], s36 offen nt lds\n"
    "v_exp_f32 v161, v161\n"
    "v_mfma_f32_16x16x32_bf16 v[224:227], v[8:11], a[8:11], v[224:227]\n"
    "v_exp_f32 v162, v162\n"
    "v_mfma_f32_16x16x32_bf16 v[224:227], v[12:15], a[12:15], v[224:227]\n"
    "v_exp_f32 v163, v163\n"
    "v_cvt_pk_bf16_f32 v160, v160, v161\n"
    "v_fma_f32 v166, v166, s62, v25\n"
    "v_exp_f32 v164, v164\n"
    "v_mfma_f32_16x16x32_bf16 v[232:235], v[8:11], a[32:35], v[232:235]\n"
    "v_fma_f32 v167, v167, s62, v25\n"
    "v_cvt_pk_bf16_f32 v162, v162, v163\n"
    "v_mfma_f32_16x16x32_bf16 v[232:235], v[12:15], a[36:39], v[232:235]\n"
    "v_exp_f32 v165, v165\n"
    "v_accvgpr_write_b32 a144, v160\n"
    "v_fma_f32 v168, v168, s62, v26\n"
    "v_exp_f32 v166, v166\n"
    "v_mfma_f32_16x16x32_bf16 v[240:243], v[8:11], a[56:59], v[240:243]\n"
    "v_exp_f32 v167, v167\n"
    "v_mfma_f32_16x16x32_bf16 v[240:243], v[12:15], a[60:63], v[240:243]\n"
    "v_fma_f32 v169, v169, s62, v26\n"
    "v_cvt_pk_bf16_f32 v164, v164, v165\n"
    "v_accvgpr_write_b32 a145, v162\n"
    "v_fma_f32 v170, v170, s62, v26\n"
    "v_exp_f32 v168, v168\n"
    "v_mfma_f32_16x16x32_bf16 v[248:251], v[8:11], a[80:83], v[248:251]\n"
    "v_fma_f32 v171, v171, s62, v26\n"
    "v_cvt_pk_bf16_f32 v166, v166, v167\n"
    "v_mfma_f32_16x16x32_bf16 v[248:251], v[12:15], a[84:87], v[248:251]\n"
    "s_waitcnt lgkmcnt(0)\n"
    "ds_read_b128 v[8:11], v16 offset:6144\n"
    "v_exp_f32 v169, v169\n"
    "v_accvgpr_write_b32 a146, v164\n"
    "v_fma_f32 v172, v172, s62, v26\n"
    "v_mfma_f32_16x16x32_bf16 v[208:211], v[0:3], a[112:115], v[208:211]\n"
    "v_exp_f32 v170, v170\n"
    "v_mfma_f32_16x16x32_bf16 v[208:211], v[4:7], a[116:119], v[208:211]\n"
    "ds_read_b128 v[12:15], v17 offset:6144\n"
    "v_fma_f32 v173, v173, s62, v26\n"
    "v_exp_f32 v171, v171\n"
    "v_cvt_pk_bf16_f32 v168, v168, v169\n"
    "v_mfma_f32_16x16x32_bf16 v[216:219], v[0:3], a[136:139], v[216:219]\n"
    "v_accvgpr_write_b32 a147, v166\n"
    "v_fma_f32 v174, v174, s62, v26\n"
    "v_mfma_f32_16x16x32_bf16 v[216:219], v[4:7], a[140:143], v[216:219]\n"
    "v_exp_f32 v172, v172\n"
    "v_fma_f32 v175, v175, s62, v26\n"
    "v_exp_f32 v173, v173\n"
    "v_mfma_f32_16x16x32_bf16 v[224:227], v[0:3], a[16:19], v[224:227]\n"
    "v_exp_f32 v174, v174\n"
    "v_mfma_f32_16x16x32_bf16 v[224:227], v[4:7], a[20:23], v[224:227]\n"
    "v_cvt_pk_bf16_f32 v170, v170, v171\n"
    "v_accvgpr_write_b32 a148, v168\n"
    "v_fma_f32 v176, v176, s62, v27\n"
    "v_fma_f32 v177, v177, s62, v27\n"
    "v_exp_f32 v175, v175\n"
    "v_mfma_f32_16x16x32_bf16 v[232:235], v[0:3], a[40:43], v[232:235]\n"
    "v_cvt_pk_bf16_f32 v172, v172, v173\n"
    "v_accvgpr_write_b32 a149, v170\n"
    "v_mfma_f32_16x16x32_bf16 v[232:235], v[4:7], a[44:47], v[232:235]\n"
    "s_memrealtime s[92:93]\n"
    "v_fma_f32 v178, v178, s62, v27\n"
    "v_exp_f32 v176, v176\n"
    "v_fma_f32 v179, v179, s62, v27\n"
    "v_mfma_f32_16x16x32_bf16 v[240:243], v[0:3], a[64:67], v[240:243]\n"
    "v_exp_f32 v177, v177\n"
    "v_mfma_f32_16x16x32_bf16 v[240:243], v[4:7], a[68:71], v[240:243]\n"
    "buffer_store_dwordx4 v[88:91], v24, s[52:55], s56 offen offset:3072 sc0 sc1\n"
    "v_mfma_f32_16x16x32_bf16 v[248:251], v[0:3], a[88:91], v[248:251]\n"
    "v_cvt_pk_bf16_f32 v174, v174, v175\n"
    "v_accvgpr_write_b32 a150, v172\n"
    "v_mfma_f32_16x16x32_bf16 v[248:251], v[4:7], a[92:95], v[248:251]\n"
    "s_waitcnt lgkmcnt(0)\n"
    "ds_read_b128 v[0:3], v16 offset:7168\n"
    "v_max3_f32 v144, v192, v193, v194\n"
    "v_max3_f32 v145, v195, v196, v197\n"
    "v_max3_f32 v144, v144, v198, v199\n"
    "v_max_f32 v144, v144, v145\n"
    "v_mfma_f32_16x16x32_bf16 v[212:215], v[8:11], a[96:99], 0\n"
    "v_mov_b32 v145, v144\n"
    "v_max3_f32 v146, v200, v201, v202\n"
    "v_mfma_f32_16x16x32_bf16 v[212:215], v[12:15], a[100:103], v[212:215]\n"
    "s_add_u32 s94, s94, 286\n"
    "s_sub_u32 s95, s94, s92\n"
    "s_cmp_gt_i32 s95, 3\n"
    "s_cbranch_scc1 L_pc0_%=\n"
    "L_pr0_%=:\n"
    "ds_read_b128 v[4:7], v17 offset:7168\n"
    "v_max3_f32 v147, v203, v204, v205\n"
    "v_mfma_f32_16x16x32_bf16 v[220:223], v[8:11], a[120:123], 0\n"
    "v_max3_f32 v146, v146, v206, v207\n"
    "v_max_f32 v146, v146, v147\n"
    "v_mfma_f32_16x16x32_bf16 v[220:223], v[12:15], a[124:127], v[220:223]\n"
    "buffer_load_dwordx4 v22, s[44:47], s36 offen offset:1024 nt lds\n"
    "v_mov_b32 v147, v146\n"
    "v_permlane16_swap_b32 v144, v145\n"
    "v_mfma_f32_16x16x32_bf16 v[228:231], v[8:11], a[0:3], 0\n"
    "v_permlane16_swap_b32 v146, v147\n"
    "v_mfma_f32_16x16x32_bf16 v[228:231], v[12:15], a[4:7], v[228:231]\n"
    "v_max_f32 v144, v144, v145\n"
    "v_max_f32 v146, v146, v147\n"
    "v_mov_b32 v145, v144\n"
    "v_mov_b32 v147, v146\n"
    "s_nop 0\n"
    "v_permlane32_swap_b32 v144, v145\n"
    "v_mfma_f32_16x16x32_bf16 v[236:239], v[8:11], a[24:27], 0\n"
    "v_max_f32 v144, v144, v145\n"
    "v_mul_f32 v29, s74, v144\n"
    "v_mfma_f32_16x16x32_bf16 v[236:239], v[12:15], a[28:31], v[236:239]\n"
    "v_permlane32_swap_b32 v146, v147\n"
    "v_max_f32 v146, v146, v147\n"
    "v_mul_f32 v30, s74, v146\n"
    "v_fma_f32 v180, v180, s62, v27\n"
    "v_mfma_f32_16x16x32_bf16 v[244:247], v[8:11], a[48:51], 0\n"
    "v_exp_f32 v178, v178\n"
    "v_mfma_f32_16x16x32_bf16 v[244:247], v[12:15], a[52:55], v[244:247]\n"
    "v_fma_f32 v181, v181, s62, v27\n"
    "v_exp_f32 v179, v179\n"
    "v_cvt_pk_bf16_f32 v176, v176, v177\n"
    "v_accvgpr_write_b32 a151, v174\n"
    "v_mfma_f32_16x16x32_bf16 v[252:255], v[8:11], a[72:75], 0\n"
    "v_fma_f32 v182, v182, s62, v27\n"
    "v_fma_f32 v183, v183, s62, v27\n"
    "v_mfma_f32_16x16x32_bf16 v[252:255], v[12:15], a[76:79], v[252:255]\n"
    "s_waitcnt lgkmcnt(0)\n"
    "ds_read_b128 v[8:11], v16 offset:8192\n"
    "v_exp_f32 v180, v180\n"
    "v_exp_f32 v181, v181\n"
    "v_mfma_f32_16x16x32_bf16 v[212:215], v[0:3], a[104:107], v[212:215]\n"
    "v_exp_f32 v182, v182\n"
    "v_mfma_f32_16x16x32_bf16 v[212:215], v[4:7], a[108:111], v[212:215]\n"
    "ds_read_b128 v[12:15], v17 offset:8192\n"
    "v_cvt_pk_bf16_f32 v178, v178, v179\n"
    "v_accvgpr_write_b32 a152, v176\n"
    "v_fma_f32 v184, v184, s62, v28\n"
    "v_fma_f32 v185, v185, s62, v28\n"
    "v_mfma_f32_16x16x32_bf16 v[220:223], v[0:3], a[128:131], v[220:223]\n"
    "v_cvt_pk_bf16_f32 v180, v180, v181\n"
    "v_accvgpr_write_b32 a153, v178\n"
    "v_mfma_f32_16x16x32_bf16 v[220:223], v[4:7], a[132:135], v[220:223]\n"
    "buffer_load_dwordx4 v23, s[44:47], s36 offen offset:2048 nt lds\n"
    "v_exp_f32 v183, v183\n"
    "v_fma_f32 v186, v186, s62, v28\n"
    "v_mfma_f32_16x16x32_bf16 v[228:231], v[0:3], a[8:11], v[228:231]\n"
    "v_exp_f32 v184, v184\n"
    "v_mfma_f32_16x16x32_bf16 v[228:231], v[4:7], a[12:15], v[228:231]\n"
    "v_fma_f32 v187, v187, s62, v28\n"
    "v_exp_f32 v185, v185\n"
    "v_cvt_pk_bf16_f32 v182, v182, v183\n"
    "v_accvgpr_write_b32 a154, v180\n"
    "v_mfma_f32_16x16x32_bf16 v[236:239], v[0:3], a[32:35], v[236:239]\n"
    "v_fma_f32 v188, v188, s62, v28\n"
    "v_fma_f32 v189, v189, s62, v28\n"
    "v_mfma_f32_16x16x32_bf16 v[236:239], v[4:7], a[36:39], v[236:239]\n"
    "s_mov_b32 s57, 6400\n"
    "v_exp_f32 v186, v186\n"
    "v_exp_f32 v187, v187\n"
    "v_cvt_pk_bf16_f32 v184, v184, v185\n"
    "v_mfma_f32_16x16x32_bf16 v[244:247], v[0:3], a[56:59], v[244:247]\n"
    "v_exp_f32 v188, v188\n"
    "v_mfma_f32_16x16x32_bf16 v[244:247], v[4:7], a[60:63], v[244:247]\n"
    "s_waitcnt vmcnt(11) lgkmcnt(0)\n"
    "s_barrier\n"
    "buffer_store_dwordx4 v[116:119], v24, s[52:55], s57 offen sc0 sc1\n"
    "v_mfma_f32_16x16x32_bf16 v[252:255], v[0:3], a[80:83], v[252:255]\n"
    "v_accvgpr_write_b32 a155, v182\n"
    "v_fma_f32 v190, v190, s62, v28\n"
    "v_mfma_f32_16x16x32_bf16 v[252:255], v[4:7], a[84:87], v[252:255]\n"
    "ds_read_b128 v[0:3], v16 offset:12288\n"
    "v_fma_f32 v191, v191, s62, v28\n"
    "v_exp_f32 v189, v189\n"
    "v_cvt_pk_bf16_f32 v186, v186, v187\n"
    "v_accvgpr_write_b32 a156, v184\n"
    "v_mfma_f32_16x16x32_bf16 v[212:215], v[8:11], a[112:115], v[212:215]\n"
    "v_exp_f32 v190, v190\n"
    "v_mfma_f32_16x16x32_bf16 v[212:215], v[12:15], a[116:119], v[212:215]\n"
    "ds_read_b128 v[4:7], v17 offset:12288\n"
    "v_fma_f32 v192, v192, s62, v29\n"
    "v_fma_f32 v193, v193, s62, v29\n"
    "v_exp_f32 v191, v191\n"
    "v_cvt_pk_bf16_f32 v188, v188, v189\n"
    "v_mfma_f32_16x16x32_bf16 v[220:223], v[8:11], a[136:139], v[220:223]\n"
    "v_accvgpr_write_b32 a157, v186\n"
    "v_fma_f32 v194, v194, s62, v29\n"
    "v_mfma_f32_16x16x32_bf16 v[220:223], v[12:15], a[140:143], v[220:223]\n"
    "v_exp_f32 v192, v192\n"
    "v_fma_f32 v195, v195, s62, v29\n"
    "v_exp_f32 v193, v193\n"
    "v_mfma_f32_16x16x32_bf16 v[228:231], v[8:11], a[16:19], v[228:231]\n"
    "v_exp_f32 v194, v194\n"
    "v_mfma_f32_16x16x32_bf16 v[228:231], v[12:15], a[20:23], v[228:231]\n"
    "s_add_u32 m0, s60, 73728\n"
    "v_cvt_pk_bf16_f32 v190, v190, v191\n"
    "v_accvgpr_write_b32 a158, v188\n"
    "v_fma_f32 v196, v196, s62, v29\n"
    "v_fma_f32 v197, v197, s62, v29\n"
    "v_mfma_f32_16x16x32_bf16 v[236:239], v[8:11], a[40:43], v[236:239]\n"
    "v_cvt_pk_bf16_f32 v192, v192, v193\n"
    "v_accvgpr_write_b32 a159, v190\n"
    "v_mfma_f32_16x16x32_bf16 v[236:239], v[12:15], a[44:47], v[236:239]\n"
    "s_add_u32 s36, s61, 86016\n"
    "v_exp_f32 v195, v195\n"
    "v_fma_f32 v198, v198, s62, v29\n"
    "v_exp_f32 v196, v196\n"
    "v_mfma_f32_16x16x32_bf16 v[244:247], v[8:11], a[64:67], v[244:247]\n"
    "v_exp_f32 v197, v197\n"
    "v_mfma_f32_16x16x32_bf16 v[244:247], v[12:15], a[68:71], v[244:247]\n"
    "buffer_load_dwordx4 v20, s[44:47], s36 offen nt lds\n"
    "v_fma_f32 v199, v199, s62, v29\n"
    "v_cvt_pk_bf16_f32 v194, v194, v195\n"
    "v_accvgpr_write_b32 a160, v192\n"
    "v_mfma_f32_16x16x32_bf16 v[252:255], v[8:11], a[88:91], v[252:255]\n"
    "v_fma_f32 v200, v200, s62, v30\n"
    "v_fma_f32 v201, v201, s62, v30\n"
    "v_mfma_f32_16x16x32_bf16 v[252:255], v[12:15], a[92:95], v[252:255]\n"
    "s_waitcnt lgkmcnt(0)\n"
    "ds_read_b128 v[8:11], v16 offset:13312\n"
    "v_exp_f32 v198, v198\n"
    "v_exp_f32 v199, v199\n"
    "v_cvt_pk_bf16_f32 v196, v196, v197\n"
    "L_ent_%=:\n"
    "v_mfma_f32_16x16x32_bf16 v[160:163], v[0:3], a[96:99], 0\n"
    "v_exp_f32 v200, v200\n"
    "v_mfma_f32_16x16x32_bf16 v[160:163], v[4:7], a[100:103], v[160:163]\n"
    "ds_read_b128 v[12:15], v17 offset:13312\n"
    "s_add_u32 s40, s40, 4718592\n"
    "s_addc_u32 s41, s41, 0\n"
    "s_cmp_eq_u32 s64, 3\n"
    "s_cselect_b32 s42, 0, s42\n"
    "v_accvgpr_write_b32 a161, v194\n"
    "v_fma_f32 v202, v202, s62, v30\n"
    "v_mfma_f32_16x16x32_bf16 v[168:171], v[0:3], a[120:123], 0\n"
    "v_fma_f32 v203, v203, s62, v30\n"
    "v_cvt_pk_bf16_f32 v198, v198, v199\n"
    "v_mfma_f32_16x16x32_bf16 v[168:171], v[4:7], a[124:127], v[168:171]\n"
    "v_exp_f32 v201, v201\n"
    "v_accvgpr_write_b32 a162, v196\n"
    "v_fma_f32 v204, v204, s62, v30\n"
    "v_exp_f32 v202, v202\n"
    "v_mfma_f32_16x16x32_bf16 v[176:179], v[0:3], a[0:3], 0\n"
    "v_exp_f32 v203, v203\n"
    "v_mfma_f32_16x16x32_bf16 v[176:179], v[4:7], a[4:7], v[176:179]\n"
    "v_fma_f32 v205, v205, s62, v30\n"
    "v_cvt_pk_bf16_f32 v200, v200, v201\n"
    "v_accvgpr_write_b32 a163, v198\n"
    "v_fma_f32 v206, v206, s62, v30\n"
    "v_exp_f32 v204, v204\n"
    "v_mfma_f32_16x16x32_bf16 v[184:187], v[0:3], a[24:27], 0\n"
    "v_fma_f32 v207, v207, s62, v30\n"
    "v_cvt_pk_bf16_f32 v202, v202, v203\n"
    "v_mfma_f32_16x16x32_bf16 v[184:187], v[4:7], a[28:31], v[184:187]\n"
    "v_exp_f32 v205, v205\n"
    "v_accvgpr_write_b32 a164, v200\n"
    "v_exp_f32 v206, v206\n"
    "v_exp_f32 v207, v207\n"
    "v_mfma_f32_16x16x32_bf16 v[192:195], v[0:3], a[48:51], 0\n"
    "v_cvt_pk_bf16_f32 v204, v204, v205\n"
    "v_accvgpr_write_b32 a165, v202\n"
    "v_mfma_f32_16x16x32_bf16 v[192:195], v[4:7], a[52:55], v[192:195]\n"
    "buffer_store_dwordx4 v[92:95], v24, s[52:55], s57 offen offset:3072 sc0 sc1\n"
    "v_cvt_pk_bf16_f32 v206, v206, v207\n"
    "v_accvgpr_write_b32 a166, v204\n"
    "v_accvgpr_write_b32 a167, v206\n"
    "v_mfma_f32_16x16x32_bf16 v[200:203], v[0:3], a[72:75], 0\n"
    "v_fma_f32 v208, v208, s62, v25\n"
    "v_fma_f32 v209, v209, s62, v25\n"
    "v_mfma_f32_16x16x32_bf16 v[200:203], v[4:7], a[76:79], v[200:203]\n"
    "s_waitcnt lgkmcnt(0)\n"
    "ds_read_b128 v[0:3], v16 offset:14336\n"
    "v_fma_f32 v210, v210, s62, v25\n"
    "v_exp_f32 v208, v208\n"
    "v_mfma_f32_16x16x32_bf16 v[160:163], v[8:11], a[104:107], v[160:163]\n"
    "v_exp_f32 v209, v209\n"
    "v_mfma_f32_16x16x32_bf16 v[160:163], v[12:15], a[108:111], v[160:163]\n"
    "ds_read_b128 v[4:7], v17 offset:14336\n"
    "v_fma_f32 v211, v211, s62, v25\n"
    "v_fma_f32 v212, v212, s62, v25\n"
    "v_exp_f32 v210, v210\n"
    "v_mfma_f32_16x16x32_bf16 v[168:171], v[8:11], a[128:131], v[168:171]\n"
    "v_fma_f32 v213, v213, s62, v25\n"
    "v_cvt_pk_bf16_f32 v208, v208, v209\n"
    "v_mfma_f32_16x16x32_bf16 v[168:171], v[12:15], a[132:135], v[168:171]\n"
    "buffer_load_dwordx4 v22, s[44:47], s36 offen offset:1024 nt lds\n"
    "v_exp_f32 v211, v211\n"
    "v_mfma_f32_16x16x32_bf16 v[176:179], v[8:11], a[8:11], v[176:179]\n"
    "v_exp_f32 v212, v212\n"
    "v_mfma_f32_16x16x32_bf16 v[176:179], v[12:15], a[12:15], v[176:179]\n"
    "v_fma_f32 v214, v214, s62, v25\n"
    "v_fma_f32 v215, v215, s62, v25\n"
    "v_exp_f32 v213, v213\n"
    "v_mfma_f32_16x16x32_bf16 v[184:187], v[8:11], a[32:35], v[184:187]\n"
    "v_cvt_pk_bf16_f32 v210, v210, v211\n"
    "v_accvgpr_write_b32 a168, v208\n"
    "v_mfma_f32_16x16x32_bf16 v[184:187], v[12:15], a[36:39], v[184:187]\n"
    "v_fma_f32 v216, v216, s62, v26\n"
    "v_exp_f32 v214, v214\n"
    "v_fma_f32 v217, v217, s62, v26\n"
    "v_mfma_f32_16x16x32_bf16 v[192:195], v[8:11], a[56:59], v[192:195]\n"
    "v_exp_f32 v215, v215\n"
    "v_mfma_f32_16x16x32_bf16 v[192:195], v[12:15], a[60:63], v[192:195]\n"
    "v_cvt_pk_bf16_f32 v212, v212, v213\n"
    "v_accvgpr_write_b32 a169, v210\n"
    "v_fma_f32 v218, v218, s62, v26\n"
    "v_exp_f32 v216, v216\n"
    "v_mfma_f32_16x16x32_bf16 v[200:203], v[8:11], a[80:83], v[200:203]\n"
    "v_fma_f32 v219, v219, s62, v26\n"
    "v_cvt_pk_bf16_f32 v214, v214, v215\n"
    "v_mfma_f32_16x16x32_bf16 v[200:203], v[12:15], a[84:87], v[200:203]\n"
    "s_waitcnt lgkmcnt(0)\n"
    "ds_read_b128 v[8:11], v16 offset:18432\n"
    "v_exp_f32 v217, v217\n"
    "v_accvgpr_write_b32 a170, v212\n"
    "v_mfma_f32_16x16x32_bf16 v[160:163], v[0:3], a[112:115], v[160:163]\n"
    "v_exp_f32 v218, v218\n"
    "v_mfma_f32_16x16x32_bf16 v[160:163], v[4:7], a[116:119], v[160:163]\n"
    "ds_read_b128 v[12:15], v17 offset:18432\n"
    "v_fma_f32 v220, v220, s62, v26\n"
    "v_fma_f32 v221, v221, s62, v26\n"
    "v_exp_f32 v219, v219\n"
    "v_mfma_f32_16x16x32_bf16 v[168:171], v[0:3], a[136:139], v[168:171]\n"
    "v_cvt_pk_bf16_f32 v216, v216, v217\n"
    "v_accvgpr_write_b32 a171, v214\n"
    "v_mfma_f32_16x16x32_bf16 v[168:171], v[4:7], a[140:143], v[168:171]\n"
    "v_fma_f32 v222, v222, s62, v26\n"
    "v_exp_f32 v220, v220\n"
    "v_fma_f32 v223, v223, s62, v26\n"
    "v_mfma_f32_16x16x32_bf16 v[176:179], v[0:3], a[16:19], v[176:179]\n"
    "v_exp_f32 v221, v221\n"
    "v_mfma_f32_16x16x32_bf16 v[176:179], v[4:7], a[20:23], v[176:179]\n"
    "v_cvt_pk_bf16_f32 v218, v218, v219\n"
    "v_accvgpr_write_b32 a172, v216\n"
    "v_fma_f32 v224, v224, s62, v27\n"
    "v_exp_f32 v222, v222\n"
    "v_mfma_f32_16x16x32_bf16 v[184:187], v[0:3], a[40:43], v[184:187]\n"
    "v_fma_f32 v225, v225, s62, v27\n"
    "v_cvt_pk_bf16_f32 v220, v220, v221\n"
    "v_mfma_f32_16x16x32_bf16 v[184:187], v[4:7], a[44:47], v[184:187]\n"
    "s_mov_b32 s56, 12544\n"
    "v_exp_f32 v223, v223\n"
    "v_accvgpr_write_b32 a173, v218\n"
    "v_mfma_f32_16x16x32_bf16 v[192:195], v[0:3], a[64:67], v[192:195]\n"
    "v_exp_f32 v224, v224\n"
    "v_mfma_f32_16x16x32_bf16 v[192:195], v[4:7], a[68:71], v[192:195]\n"
    "buffer_store_dwordx4 v[120:123], v24, s[52:55], s56 offen sc0 sc1\n"
    "v_mfma_f32_16x16x32_bf16 v[200:203], v[0:3], a[88:91], v[200:203]\n"
    "v_fma_f32 v226, v226, s62, v27\n"
    "v_fma_f32 v227, v227, s62, v27\n"
    "v_mfma_f32_16x16x32_bf16 v[200:203], v[4:7], a[92:95], v[200:203]\n"
    "s_waitcnt lgkmcnt(0)\n"
    "ds_read_b128 v[0:3], v16 offset:19456\n"
    "v_exp_f32 v225, v225\n"
    "v_cvt_pk_bf16_f32 v222, v222, v223\n"
    "v_mfma_f32_16x16x32_bf16 v[164:167], v[8:11], a[96:99], 0\n"
    "v_exp_f32 v226, v226\n"
    "v_mfma_f32_16x16x32_bf16 v[164:167], v[12:15], a[100:103], v[164:167]\n"
    "ds_read_b128 v[4:7], v17 offset:19456\n"
    "v_accvgpr_write_b32 a174, v220\n"
    "v_fma_f32 v228, v228, s62, v27\n"
    "v_fma_f32 v229, v229, s62, v27\n"
    "v_mfma_f32_16x16x32_bf16 v[172:175], v[8:11], a[120:123], 0\n"
    "v_cvt_pk_bf16_f32 v224, v224, v225\n"
    "v_accvgpr_write_b32 a175, v222\n"
    "v_mfma_f32_16x16x32_bf16 v[172:175], v[12:15], a[124:127], v[172:175]\n"
    "buffer_load_dwordx4 v23, s[44:47], s36 offen offset:2048 nt lds\n"
    "v_exp_f32 v227, v227\n"
    "v_mfma_f32_16x16x32_bf16 v[180:183], v[8:11], a[0:3], 0\n"
    "v_exp_f32 v228, v228\n"
    "v_mfma_f32_16x16x32_bf16 v[180:183], v[12:15], a[4:7], v[180:183]\n"
    "v_fma_f32 v230, v230, s62, v27\n"
    "v_fma_f32 v231, v231, s62, v27\n"
    "v_exp_f32 v229, v229\n"
    "v_mfma_f32_16x16x32_bf16 v[188:191], v[8:11], a[24:27], 0\n"
    "v_cvt_pk_bf16_f32 v226, v226, v227\n"
    "v_accvgpr_write_b32 a176, v224\n"
    "v_mfma_f32_16x16x32_bf16 v[188:191], v[12:15], a[28:31], v[188:191]\n"
    "v_fma_f32 v232, v232, s62, v28\n"
    "v_exp_f32 v230, v230\n"
    "v_fma_f32 v233, v233, s62, v28\n"
    "v_mfma_f32_16x16x32_bf16 v[196:199], v[8:11], a[48:51], 0\n"
    "v_exp_f32 v231, v231\n"
    "v_mfma_f32_16x16x32_bf16 v[196:199], v[12:15], a[52:55], v[196:199]\n"
    "v_cvt_pk_bf16_f32 v228, v228, v229\n"
    "v_accvgpr_write_b32 a177, v226\n"
    "v_fma_f32 v234, v234, s62, v28\n"
    "v_exp_f32 v232, v232\n"
    "v_mfma_f32_16x16x32_bf16 v[204:207], v[8:11], a[72:75], 0\n"
    "v_fma_f32 v235, v235, s62, v28\n"
    "v_cvt_pk_bf16_f32 v230, v230, v231\n"
    "v_mfma_f32_16x16x32_bf16 v[204:207], v[12:15], a[76:79], v[204:207]\n"
    "s_waitcnt lgkmcnt(0)\n"
    "ds_read_b128 v[8:11], v16 offset:20480\n"
    "s_add_u32 m0, s60, 86016\n"
    "v_exp_f32 v233, v233\n"
    "v_mfma_f32_16x16x32_bf16 v[164:167], v[0:3], a[104:107], v[164:167]\n"
    "v_exp_f32 v234, v234\n"
    "v_mfma_f32_16x16x32_bf16 v[164:167], v[4:7], a[108:111], v[164:167]\n"
    "ds_read_b128 v[12:15], v17 offset:20480\n"
    "s_add_u32 s36, s61, 98304\n"
    "v_accvgpr_write_b32 a178, v228\n"
    "v_fma_f32 v236, v236, s62, v28\n"
    "v_mfma_f32_16x16x32_bf16 v[172:175], v[0:3], a[128:131], v[172:175]\n"
    "v_fma_f32 v237, v237, s62, v28\n"
    "v_cvt_pk_bf16_f32 v232, v232, v233\n"
    "v_mfma_f32_16x16x32_bf16 v[172:175], v[4:7], a[132:135], v[172:175]\n"
    "buffer_load_dwordx4 v20, s[44:47], s36 offen nt lds\n"
    "v_exp_f32 v235, v235\n"
    "v_mfma_f32_16x16x32_bf16 v[180:183], v[0:3], a[8:11], v[180:183]\n"
    "v_exp_f32 v236, v236\n"
    "v_mfma_f32_16x16x32_bf16 v[180:183], v[4:7], a[12:15], v[180:183]\n"
    "v_accvgpr_write_b32 a179, v230\n"
    "v_fma_f32 v238, v238, s62, v28\n"
    "v_fma_f32 v239, v239, s62, v28\n"
    "v_exp_f32 v237, v237\n"
    "v_mfma_f32_16x16x32_bf16 v[188:191], v[0:3], a[32:35], v[188:191]\n"
    "v_cvt_pk_bf16_f32 v234, v234, v235\n"
    "v_accvgpr_write_b32 a180, v232\n"
    "v_mfma_f32_16x16x32_bf16 v[188:191], v[4:7], a[36:39], v[188:191]\n"
    "v_fma_f32 v240, v240, s62, v29\n"
    "v_exp_f32 v238, v238\n"
    "v_fma_f32 v241, v241, s62, v29\n"
    "v_mfma_f32_16x16x32_bf16 v[196:199], v[0:3], a[56:59], v[196:199]\n"
    "v_exp_f32 v239, v239\n"
    "v_mfma_f32_16x16x32_bf16 v[196:199], v[4:7], a[60:63], v[196:199]\n"
    "buffer_store_dwordx4 v[96:99], v24, s[52:55], s56 offen offset:3072 sc0 sc1\n"
    "v_mfma_f32_16x16x32_bf16 v[204:207], v[0:3], a[80:83], v[204:207]\n"
    "v_cvt_pk_bf16_f32 v236, v236, v237\n"
    "v_accvgpr_write_b32 a181, v234\n"
    "v_mfma_f32_16x16x32_bf16 v[204:207], v[4:7], a[84:87], v[204:207]\n"
    "s_waitcnt lgkmcnt(0)\n"
    "ds_read_b128 v[0:3], v16 offset:24576\n"
    "v_fma_f32 v242, v242, s62, v29\n"
    "v_exp_f32 v240, v240\n"
    "v_mfma_f32_16x16x32_bf16 v[164:167], v[8:11], a[112:115], v[164:167]\n"
    "v_exp_f32 v241, v241\n"
    "v_mfma_f32_16x16x32_bf16 v[164:167], v[12:15], a[116:119], v[164:167]\n"
    "ds_read_b128 v[4:7], v17 offset:24576\n"
    "v_fma_f32 v243, v243, s62, v29\n"
    "v_cvt_pk_bf16_f32 v238, v238, v239\n"
    "v_accvgpr_write_b32 a182, v236\n"
    "v_mfma_f32_16x16x32_bf16 v[172:175], v[8:11], a[136:139], v[172:175]\n"
    "v_fma_f32 v244, v244, s62, v29\n"
    "v_fma_f32 v245, v245, s62, v29\n"
    "v_mfma_f32_16x16x32_bf16 v[172:175], v[12:15], a[140:143], v[172:175]\n"
    "v_exp_f32 v242, v242\n"
    "v_exp_f32 v243, v243\n"
    "v_mfma_f32_16x16x32_bf16 v[180:183], v[8:11], a[16:19], v[180:183]\n"
    "v_exp_f32 v244, v244\n"
    "v_mfma_f32_16x16x32_bf16 v[180:183], v[12:15], a[20:23], v[180:183]\n"
    "v_cvt_pk_bf16_f32 v240, v240, v241\n"
    "v_accvgpr_write_b32 a183, v238\n"
    "v_fma_f32 v246, v246, s62, v29\n"
    "v_fma_f32 v247, v247, s62, v29\n"
    "v_mfma_f32_16x16x32_bf16 v[188:191], v[8:11], a[40:43], v[188:191]\n"
    "v_cvt_pk_bf16_f32 v242, v242, v243\n"
    "v_accvgpr_write_b32 a184, v240\n"
    "v_mfma_f32_16x16x32_bf16 v[188:191], v[12:15], a[44:47], v[188:191]\n"
    "v_exp_f32 v245, v245\n"
    "v_fma_f32 v248, v248, s62, v30\n"
    "v_exp_f32 v246, v246\n"
    "v_mfma_f32_16x16x32_bf16 v[196:199], v[8:11], a[64:67], v[196:199]\n"
    "v_exp_f32 v247, v247\n"
    "v_mfma_f32_16x16x32_bf16 v[196:199], v[12:15], a[68:71], v[196:199]\n"
    "buffer_load_dwordx4 v22, s[44:47], s36 offen offset:1024 nt lds\n"
    "v_fma_f32 v249, v249, s62, v30\n"
    "v_cvt_pk_bf16_f32 v244, v244, v245\n"
    "v_mfma_f32_16x16x32_bf16 v[204:207], v[8:11], a[88:91], v[204:207]\n"
    "v_accvgpr_write_b32 a185, v242\n"
    "v_fma_f32 v250, v250, s62, v30\n"
    "v_mfma_f32_16x16x32_bf16 v[204:207], v[12:15], a[92:95], v[204:207]\n"
    "s_waitcnt lgkmcnt(0)\n"
    "ds_read_b128 v[8:11], v16 offset:25600\n"
    "v_exp_f32 v248, v248\n"
    "v_fma_f32 v251, v251, s62, v30\n"
    "v_mfma_f32_16x16x32_bf16 v[208:211], v[0:3], a[96:99], 0\n"
    "v_exp_f32 v249, v249\n"
    "v_mfma_f32_16x16x32_bf16 v[208:211], v[4:7], a[100:103], v[208:211]\n"
    "ds_read_b128 v[12:15], v17 offset:25600\n"
    "v_cvt_pk_bf16_f32 v246, v246, v247\n"
    "v_accvgpr_write_b32 a186, v244\n"
    "v_fma_f32 v252, v252, s62, v30\n"
    "v_mfma_f32_16x16x32_bf16 v[216:219], v[0:3], a[120:123], 0\n"
    "v_fma_f32 v253, v253, s62, v30\n"
    "v_cvt_pk_bf16_f32 v248, v248, v249\n"
    "v_mfma_f32_16x16x32_bf16 v[216:219], v[4:7], a[124:127], v[216:219]\n"
    "v_exp_f32 v250, v250\n"
    "v_exp_f32 v251, v251\n"
    "v_accvgpr_write_b32 a187, v246\n"
    "v_mfma_f32_16x16x32_bf16 v[224:227], v[0:3], a[0:3], 0\n"
    "v_exp_f32 v252, v252\n"
    "v_mfma_f32_16x16x32_bf16 v[224:227], v[4:7], a[4:7], v[224:227]\n"
    "v_fma_f32 v254, v254, s62, v30\n"
    "v_fma_f32 v255, v255, s62, v30\n"
    "v_exp_f32 v253, v253\n"
    "v_cvt_pk_bf16_f32 v250, v250, v251\n"
    "v_mfma_f32_16x16x32_bf16 v[232:235], v[0:3], a[24:27], 0\n"
    "v_accvgpr_write_b32 a188, v248\n"
    "v_cvt_pk_bf16_f32 v252, v252, v253\n"
    "v_mfma_f32_16x16x32_bf16 v[232:235], v[4:7], a[28:31], v[232:235]\n"
    "s_mov_b32 s57, 18688\n"
    "v_exp_f32 v254, v254\n"
    "v_exp_f32 v255, v255\n"
    "v_mfma_f32_16x16x32_bf16 v[240:243], v[0:3], a[48:51], 0\n"
    "v_accvgpr_write_b32 a189, v250\n"
    "v_cvt_pk_bf16_f32 v254, v254, v255\n"
    "v_mfma_f32_16x16x32_bf16 v[240:243], v[4:7], a[52:55], v[240:243]\n"
    "buffer_store_dwordx4 v[124:127], v24, s[52:55], s57 offen sc0 sc1\n"
    "v_accvgpr_write_b32 a190, v252\n"
    "v_accvgpr_write_b32 a191, v254\n"
    "v_mfma_f32_16x16x32_bf16 v[248:251], v[0:3], a[72:75], 0\n"
    "v_fma_f32 v160, v160, s62, v25\n"
    "v_fma_f32 v161, v161, s62, v25\n"
    "v_mfma_f32_16x16x32_bf16 v[248:251], v[4:7], a[76:79], v[248:251]\n"
    "s_waitcnt lgkmcnt(0)\n"
    "ds_read_b128 v[0:3], v16 offset:26624\n"
    "v_fma_f32 v162, v162, s62, v25\n"
    "v_exp_f32 v160, v160\n"
    "v_mfma_f32_16x16x32_bf16 v[208:211], v[8:11], a[104:107], v[208:211]\n"
    "v_exp_f32 v161, v161\n"
    "v_mfma_f32_16x16x32_bf16 v[208:211], v[12:15], a[108:111], v[208:211]\n"
    "ds_read_b128 v[4:7], v17 offset:26624\n"
    "v_fma_f32 v163, v163, s62, v25\n"
    "v_fma_f32 v164, v164, s62, v25\n"
    "v_exp_f32 v162, v162\n"
    "v_mfma_f32_16x16x32_bf16 v[216:219], v[8:11], a[128:131], v[216:219]\n"
    "v_fma_f32 v165, v165, s62, v25\n"
    "v_cvt_pk_bf16_f32 v160, v160, v161\n"
    "v_mfma_f32_16x16x32_bf16 v[216:219], v[12:15], a[132:135], v[216:219]\n"
    "buffer_load_dwordx4 v23, s[44:47], s36 offen offset:2048 nt lds\n"
    "v_exp_f32 v163, v163\n"
    "v_mfma_f32_16x16x32_bf16 v[224:227], v[8:11], a[8:11], v[224:227]\n"
    "v_exp_f32 v164, v164\n"
    "v_mfma_f32_16x16x32_bf16 v[224:227], v[12:15], a[12:15], v[224:227]\n"
    "v_fma_f32 v166, v166, s62, v25\n"
    "v_fma_f32 v167, v167, s62, v25\n"
    "v_exp_f32 v165, v165\n"
    "v_mfma_f32_16x16x32_bf16 v[232:235], v[8:11], a[32:35], v[232:235]\n"
    "v_cvt_pk_bf16_f32 v162, v162, v163\n"
    "v_accvgpr_write_b32 a192, v160\n"
    "v_mfma_f32_16x16x32_bf16 v[232:235], v[12:15], a[36:39], v[232:235]\n"
    "v_fma_f32 v168, v168, s62, v26\n"
    "v_exp_f32 v166, v166\n"
    "v_fma_f32 v169, v169, s62, v26\n"
    "v_mfma_f32_16x16x32_bf16 v[240:243], v[8:11], a[56:59], v[240:243]\n"
    "v_exp_f32 v167, v167\n"
    "v_mfma_f32_16x16x32_bf16 v[240:243], v[12:15], a[60:63], v[240:243]\n"
    "v_cvt_pk_bf16_f32 v164, v164, v165\n"
    "v_accvgpr_write_b32 a193, v162\n"
    "v_fma_f32 v170, v170, s62, v26\n"
    "v_exp_f32 v168, v168\n"
    "v_mfma_f32_16x16x32_bf16 v[248:251], v[8:11], a[80:83], v[248:251]\n"
    "v_fma_f32 v171, v171, s62, v26\n"
    "v_cvt_pk_bf16_f32 v166, v166, v167\n"
    "v_mfma_f32_16x16x32_bf16 v[248:251], v[12:15], a[84:87], v[248:251]\n"
    "s_waitcnt lgkmcnt(0)\n"
    "ds_read_b128 v[8:11], v16 offset:30720\n"
    "v_exp_f32 v169, v169\n"
    "v_accvgpr_write_b32 a194, v164\n"
    "v_mfma_f32_16x16x32_bf16 v[208:211], v[0:3], a[112:115], v[208:211]\n"
    "v_exp_f32 v170, v170\n"
    "v_mfma_f32_16x16x32_bf16 v[208:211], v[4:7], a[116:119], v[208:211]\n"
    "ds_read_b128 v[12:15], v17 offset:30720\n"
    "v_fma_f32 v172, v172, s62, v26\n"
    "v_fma_f32 v173, v173, s62, v26\n"
    "v_exp_f32 v171, v171\n"
    "v_mfma_f32_16x16x32_bf16 v[216:219], v[0:3], a[136:139], v[216:219]\n"
    "v_cvt_pk_bf16_f32 v168, v168, v169\n"
    "v_accvgpr_write_b32 a195, v166\n"
    "v_mfma_f32_16x16x32_bf16 v[216:219], v[4:7], a[140:143], v[216:219]\n"
    "v_fma_f32 v174, v174, s62, v26\n"
    "v_exp_f32 v172, v172\n"
    "v_fma_f32 v175, v175, s62, v26\n"
    "v_mfma_f32_16x16x32_bf16 v[224:227], v[0:3], a[16:19], v[224:227]\n"
    "v_exp_f32 v173, v173\n"
    "v_mfma_f32_16x16x32_bf16 v[224:227], v[4:7], a[20:23], v[224:227]\n"
    "v_cvt_pk_bf16_f32 v170, v170, v171\n"
    "v_accvgpr_write_b32 a196, v168\n"
    "v_fma_f32 v176, v176, s62, v27\n"
    "v_exp_f32 v174, v174\n"
    "v_mfma_f32_16x16x32_bf16 v[232:235], v[0:3], a[40:43], v[232:235]\n"
    "v_fma_f32 v177, v177, s62, v27\n"
    "v_cvt_pk_bf16_f32 v172, v172, v173\n"
    "v_mfma_f32_16x16x32_bf16 v[232:235], v[4:7], a[44:47], v[232:235]\n"
    "s_memrealtime s[92:93]\n"
    "v_exp_f32 v175, v175\n"
    "v_accvgpr_write_b32 a197, v170\n"
    "v_mfma_f32_16x16x32_bf16 v[240:243], v[0:3], a[64:67], v[240:243]\n"
    "v_exp_f32 v176, v176\n"
    "v_mfma_f32_16x16x32_bf16 v[240:243], v[4:7], a[68:71], v[240:243]\n"
    "buffer_store_dwordx4 v[100:103], v24, s[52:55], s57 offen offset:3072 sc0 sc1\n"
    "v_mfma_f32_16x16x32_bf16 v[248:251], v[0:3], a[88:91], v[248:251]\n"
    "v_fma_f32 v178, v178, s62, v27\n"
    "v_fma_f32 v179, v179, s62, v27\n"
    "v_mfma_f32_16x16x32_bf16 v[248:251], v[4:7], a[92:95], v[248:251]\n"
    "s_waitcnt lgkmcnt(0)\n"
    "ds_read_b128 v[0:3], v16 offset:31744\n"
    "s_add_u32 m0, s60, 98304\n"
    "v_exp_f32 v177, v177\n"
    "v_mfma_f32_16x16x32_bf16 v[212:215], v[8:11], a[96:99], 0\n"
    "v_exp_f32 v178, v178\n"
    "v_mfma_f32_16x16x32_bf16 v[212:215], v[12:15], a[100:103], v[212:215]\n"
    "s_add_u32 s94, s94, 190\n"
    "s_sub_u32 s95, s94, s92\n"
    "s_cmp_gt_i32 s95, 3\n"
    "s_cbranch_scc1 L_pc1_%=\n"
    "L_pr1_%=:\n"
    "ds_read_b128 v[4:7], v17 offset:31744\n"
    "s_add_u32 s36, s61, 110592\n"
    "v_mfma_f32_16x16x32_bf16 v[220:223], v[8:11], a[120:123], 0\n"
    "v_cvt_pk_bf16_f32 v174, v174, v175\n"
    "v_accvgpr_write_b32 a198, v172\n"
    "v_mfma_f32_16x16x32_bf16 v[220:223], v[12:15], a[124:127], v[220:223]\n"
    "buffer_load_dwordx4 v20, s[44:47], s36 offen nt lds\n"
    "v_fma_f32 v180, v180, s62, v27\n"
    "v_fma_f32 v181, v181, s62, v27\n"
    "v_mfma_f32_16x16x32_bf16 v[228:231], v[8:11], a[0:3], 0\n"
    "v_exp_f32 v179, v179\n"
    "v_mfma_f32_16x16x32_bf16 v[228:231], v[12:15], a[4:7], v[228:231]\n"
    "v_cvt_pk_bf16_f32 v176, v176, v177\n"
    "v_accvgpr_write_b32 a199, v174\n"
    "v_fma_f32 v182, v182, s62, v27\n"
    "v_exp_f32 v180, v180\n"
    "v_mfma_f32_16x16x32_bf16 v[236:239], v[8:11], a[24:27], 0\n"
    "v_fma_f32 v183, v183, s62, v27\n"
    "v_cvt_pk_bf16_f32 v178, v178, v179\n"
    "v_mfma_f32_16x16x32_bf16 v[236:239], v[12:15], a[28:31], v[236:239]\n"
    "v_exp_f32 v181, v181\n"
    "v_accvgpr_write_b32 a200, v176\n"
    "v_fma_f32 v184, v184, s62, v28\n"
    "v_mfma_f32_16x16x32_bf16 v[244:247], v[8:11], a[48:51], 0\n"
    "v_exp_f32 v182, v182\n"
    "v_mfma_f32_16x16x32_bf16 v[244:247], v[12:15], a[52:55], v[244:247]\n"
    "v_fma_f32 v185, v185, s62, v28\n"
    "v_exp_f32 v183, v183\n"
    "v_cvt_pk_bf16_f32 v180, v180, v181\n"
    "v_mfma_f32_16x16x32_bf16 v[252:255], v[8:11], a[72:75], 0\n"
    "v_accvgpr_write_b32 a201, v178\n"
    "v_fma_f32 v186, v186, s62, v28\n"
    "v_mfma_f32_16x16x32_bf16 v[252:255], v[12:15], a[76:79], v[252:255]\n"
    "s_waitcnt lgkmcnt(0)\n"
    "ds_read_b128 v[8:11], v16 offset:32768\n"
    "v_exp_f32 v184, v184\n"
    "v_fma_f32 v187, v187, s62, v28\n"
    "v_mfma_f32_16x16x32_bf16 v[212:215], v[0:3], a[104:107], v[212:215]\n"
    "v_exp_f32 v185, v185\n"
    "v_mfma_f32_16x16x32_bf16 v[212:215], v[4:7], a[108:111], v[212:215]\n"
    "ds_read_b128 v[12:15], v17 offset:32768\n"
    "v_cvt_pk_bf16_f32 v182, v182, v183\n"
    "v_accvgpr_write_b32 a202, v180\n"
    "v_fma_f32 v188, v188, s62, v28\n"
    "v_mfma_f32_16x16x32_bf16 v[220:223], v[0:3], a[128:131], v[220:223]\n"
    "v_fma_f32 v189, v189, s62, v28\n"
    "v_cvt_pk_bf16_f32 v184, v184, v185\n"
    "v_mfma_f32_16x16x32_bf16 v[220:223], v[4:7], a[132:135], v[220:223]\n"
    "buffer_load_dwordx4 v22, s[44:47], s36 offen offset:1024 nt lds\n"
    "v_exp_f32 v186, v186\n"
    "v_mfma_f32_16x16x32_bf16 v[228:231], v[0:3], a[8:11], v[228:231]\n"
    "v_exp_f32 v187, v187\n"
    "v_mfma_f32_16x16x32_bf16 v[228:231], v[4:7], a[12:15], v[228:231]\n"
    "v_accvgpr_write_b32 a203, v182\n"
    "v_fma_f32 v190, v190, s62, v28\n"
    "v_exp_f32 v188, v188\n"
    "v_mfma_f32_16x16x32_bf16 v[236:239], v[0:3], a[32:35], v[236:239]\n"
    "v_fma_f32 v191, v191, s62, v28\n"
    "v_cvt_pk_bf16_f32 v186, v186, v187\n"
    "v_mfma_f32_16x16x32_bf16 v[236:239], v[4:7], a[36:39], v[236:239]\n"
    "s_mov_b32 s56, 24832\n"
    "v_exp_f32 v189, v189\n"
    "v_accvgpr_write_b32 a204, v184\n"
    "v_fma_f32 v192, v192, s62, v29\n"
    "v_mfma_f32_16x16x32_bf16 v[244:247], v[0:3], a[56:59], v[244:247]\n"
    "v_exp_f32 v190, v190\n"
    "v_mfma_f32_16x16x32_bf16 v[244:247], v[4:7], a[60:63], v[244:247]\n"
    "s_waitcnt vmcnt(19) lgkmcnt(0)\n"
    "s_barrier\n"
    "buffer_store_dwordx4 v[128:131], v24, s[52:55], s56 offen sc0 sc1\n"
    "v_mfma_f32_16x16x32_bf16 v[252:255], v[0:3], a[80:83], v[252:255]\n"
    "v_fma_f32 v193, v193, s62, v29\n"
    "v_cvt_pk_bf16_f32 v188, v188, v189\n"
    "v_mfma_f32_16x16x32_bf16 v[252:255], v[4:7], a[84:87], v[252:255]\n"
    "ds_read_b128 v[0:3], v16 offset:36864\n"
    "v_exp_f32 v191, v191\n"
    "v_accvgpr_write_b32 a205, v186\n"
    "v_fma_f32 v194, v194, s62, v29\n"
    "v_mfma_f32_16x16x32_bf16 v[212:215], v[8:11], a[112:115], v[212:215]\n"
    "v_exp_f32 v192, v192\n"
    "v_mfma_f32_16x16x32_bf16 v[212:215], v[12:15], a[116:119], v[212:215]\n"
    "ds_read_b128 v[4:7], v17 offset:36864\n"
    "v_fma_f32 v195, v195, s62, v29\n"
    "v_exp_f32 v193, v193\n"
    "v_mfma_f32_16x16x32_bf16 v[220:223], v[8:11], a[136:139], v[220:223]\n"
    "v_cvt_pk_bf16_f32 v190, v190, v191\n"
    "v_accvgpr_write_b32 a206, v188\n"
    "v_mfma_f32_16x16x32_bf16 v[220:223], v[12:15], a[140:143], v[220:223]\n"
    "v_fma_f32 v196, v196, s62, v29\n"
    "v_exp_f32 v194, v194\n"
    "v_fma_f32 v197, v197, s62, v29\n"
    "v_mfma_f32_16x16x32_bf16 v[228:231], v[8:11], a[16:19], v[228:231]\n"
    "v_exp_f32 v195, v195\n"
    "v_mfma_f32_16x16x32_bf16 v[228:231], v[12:15], a[20:23], v[228:231]\n"
    "v_cvt_pk_bf16_f32 v192, v192, v193\n"
    "v_accvgpr_write_b32 a207, v190\n"
    "v_fma_f32 v198, v198, s62, v29\n"
    "v_exp_f32 v196, v196\n"
    "v_mfma_f32_16x16x32_bf16 v[236:239], v[8:11], a[40:43], v[236:239]\n"
    "v_fma_f32 v199, v199, s62, v29\n"
    "v_cvt_pk_bf16_f32 v194, v194, v195\n"
    "v_mfma_f32_16x16x32_bf16 v[236:239], v[12:15], a[44:47], v[236:239]\n"
    "v_exp_f32 v197, v197\n"
    "v_accvgpr_write_b32 a208, v192\n"
    "v_fma_f32 v200, v200, s62, v30\n"
    "v_mfma_f32_16x16x32_bf16 v[244:247], v[8:11], a[64:67], v[244:247]\n"
    "v_exp_f32 v198, v198\n"
    "v_mfma_f32_16x16x32_bf16 v[244:247], v[12:15], a[68:71], v[244:247]\n"
    "buffer_load_dwordx4 v23, s[44:47], s36 offen offset:2048 nt lds\n"
    "v_fma_f32 v201, v201, s62, v30\n"
    "v_exp_f32 v199, v199\n"
    "v_mfma_f32_16x16x32_bf16 v[252:255], v[8:11], a[88:91], v[252:255]\n"
    "v_cvt_pk_bf16_f32 v196, v196, v197\n"
    "v_accvgpr_write_b32 a209, v194\n"
    "v_mfma_f32_16x16x32_bf16 v[252:255], v[12:15], a[92:95], v[252:255]\n"
    "s_waitcnt lgkmcnt(0)\n"
    "ds_read_b128 v[8:11], v16 offset:37888\n"
    "v_fma_f32 v202, v202, s62, v30\n"
    "v_exp_f32 v200, v200\n"
    "v_fma_f32 v203, v203, s62, v30\n"
    "v_mfma_f32_16x16x32_bf16 v[160:163], v[0:3], a[96:99], 0\n"
    "v_exp_f32 v201, v201\n"
    "v_mfma_f32_16x16x32_bf16 v[160:163], v[4:7], a[100:103], v[160:163]\n"
    "ds_read_b128 v[12:15], v17 offset:37888\n"
    "v_cvt_pk_bf16_f32 v198, v198, v199\n"
    "v_accvgpr_write_b32 a210, v196\n"
    "v_fma_f32 v204, v204, s62, v30\n"
    "v_mfma_f32_16x16x32_bf16 v[168:171], v[0:3], a[120:123], 0\n"
    "v_fma_f32 v205, v205, s62, v30\n"
    "v_cvt_pk_bf16_f32 v200, v200, v201\n"
    "v_mfma_f32_16x16x32_bf16 v[168:171], v[4:7], a[124:127], v[168:171]\n"
    "v_exp_f32 v202, v202\n"
    "v_exp_f32 v203, v203\n"
    "v_accvgpr_write_b32 a211, v198\n"
    "v_mfma_f32_16x16x32_bf16 v[176:179], v[0:3], a[0:3], 0\n"
    "v_exp_f32 v204, v204\n"
    "v_mfma_f32_16x16x32_bf16 v[176:179], v[4:7], a[4:7], v[176:179]\n"
    "v_fma_f32 v206, v206, s62, v30\n"
    "v_fma_f32 v207, v207, s62, v30\n"
    "v_exp_f32 v205, v205\n"
    "v_mfma_f32_16x16x32_bf16 v[184:187], v[0:3], a[24:27], 0\n"
    "v_cvt_pk_bf16_f32 v202, v202, v203\n"
    "v_accvgpr_write_b32 a212, v200\n"
    "v_mfma_f32_16x16x32_bf16 v[184:187], v[4:7], a[28:31], v[184:187]\n"
    "v_exp_f32 v206, v206\n"
    "v_exp_f32 v207, v207\n"
    "v_cvt_pk_bf16_f32 v204, v204, v205\n"
    "v_mfma_f32_16x16x32_bf16 v[192:195], v[0:3], a[48:51], 0\n"
    "v_accvgpr_write_b32 a213, v202\n"
    "v_cvt_pk_bf16_f32 v206, v206, v207\n"
    "v_mfma_f32_16x16x32_bf16 v[192:195], v[4:7], a[52:55], v[192:195]\n"
    "buffer_store_dwordx4 v[104:107], v24, s[52:55], s56 offen offset:3072 sc0 sc1\n"
    "v_accvgpr_write_b32 a214, v204\n"
    "v_accvgpr_write_b32 a215, v206\n"
    "v_mfma_f32_16x16x32_bf16 v[200:203], v[0:3], a[72:75], 0\n"
    "v_fma_f32 v208, v208, s62, v25\n"
    "v_fma_f32 v209, v209, s62, v25\n"
    "v_mfma_f32_16x16x32_bf16 v[200:203], v[4:7], a[76:79], v[200:203]\n"
    "s_waitcnt lgkmcnt(0)\n"
    "ds_read_b128 v[0:3], v16 offset:38912\n"
    "s_add_u32 m0, s60, 110592\n"
    "v_fma_f32 v210, v210, s62, v25\n"
    "v_exp_f32 v208, v208\n"
    "v_mfma_f32_16x16x32_bf16 v[160:163], v[8:11], a[104:107], v[160:163]\n"
    "v_exp_f32 v209, v209\n"
    "v_mfma_f32_16x16x32_bf16 v[160:163], v[12:15], a[108:111], v[160:163]\n"
    "ds_read_b128 v[4:7], v17 offset:38912\n"
    "s_add_u32 s36, s61, 122880\n"
    "v_fma_f32 v211, v211, s62, v25\n"
    "v_fma_f32 v212, v212, s62, v25\n"
    "v_mfma_f32_16x16x32_bf16 v[168:171], v[8:11], a[128:131], v[168:171]\n"
    "v_fma_f32 v213, v213, s62, v25\n"
    "v_cvt_pk_bf16_f32 v208, v208, v209\n"
    "v_mfma_f32_16x16x32_bf16 v[168:171], v[12:15], a[132:135], v[168:171]\n"
    "buffer_load_dwordx4 v20, s[44:47], s36 offen nt lds\n"
    "v_exp_f32 v210, v210\n"
    "v_mfma_f32_16x16x32_bf16 v[176:179], v[8:11], a[8:11], v[176:179]\n"
    "v_exp_f32 v211, v211\n"
    "v_mfma_f32_16x16x32_bf16 v[176:179], v[12:15], a[12:15], v[176:179]\n"
    "v_fma_f32 v214, v214, s62, v25\n"
    "v_exp_f32 v212, v212\n"
    "v_fma_f32 v215, v215, s62, v25\n"
    "v_mfma_f32_16x16x32_bf16 v[184:187], v[8:11], a[32:35], v[184:187]\n"
    "v_cvt_pk_bf16_f32 v210, v210, v211\n"
    "v_accvgpr_write_b32 a216, v208\n"
    "v_mfma_f32_16x16x32_bf16 v[184:187], v[12:15], a[36:39], v[184:187]\n"
    "v_exp_f32 v213, v213\n"
    "v_fma_f32 v216, v216, s62, v26\n"
    "v_exp_f32 v214, v214\n"
    "v_mfma_f32_16x16x32_bf16 v[192:195], v[8:11], a[56:59], v[192:195]\n"
    "v_exp_f32 v215, v215\n"
    "v_mfma_f32_16x16x32_bf16 v[192:195], v[12:15], a[60:63], v[192:195]\n"
    "v_fma_f32 v217, v217, s62, v26\n"
    "v_cvt_pk_bf16_f32 v212, v212, v213\n"
    "v_accvgpr_write_b32 a217, v210\n"
    "v_fma_f32 v218, v218, s62, v26\n"
    "v_mfma_f32_16x16x32_bf16 v[200:203], v[8:11], a[80:83], v[200:203]\n"
    "v_fma_f32 v219, v219, s62, v26\n"
    "v_cvt_pk_bf16_f32 v214, v214, v215\n"
    "v_mfma_f32_16x16x32_bf16 v[200:203], v[12:15], a[84:87], v[200:203]\n"
    "s_waitcnt lgkmcnt(0)\n"
    "ds_read_b128 v[8:11], v16 offset:43008\n"
    "v_exp_f32 v216, v216\n"
    "v_exp_f32 v217, v217\n"
    "v_mfma_f32_16x16x32_bf16 v[160:163], v[0:3], a[112:115], v[160:163]\n"
    "v_exp_f32 v218, v218\n"
    "v_mfma_f32_16x16x32_bf16 v[160:163], v[4:7], a[116:119], v[160:163]\n"
    "ds_read_b128 v[12:15], v17 offset:43008\n"
    "v_accvgpr_write_b32 a218, v212\n"
    "v_fma_f32 v220, v220, s62, v26\n"
    "v_fma_f32 v221, v221, s62, v26\n"
    "v_mfma_f32_16x16x32_bf16 v[168:171], v[0:3], a[136:139], v[168:171]\n"
    "v_cvt_pk_bf16_f32 v216, v216, v217\n"
    "v_accvgpr_write_b32 a219, v214\n"
    "v_mfma_f32_16x16x32_bf16 v[168:171], v[4:7], a[140:143], v[168:171]\n"
    "v_exp_f32 v219, v219\n"
    "v_fma_f32 v222, v222, s62, v26\n"
    "v_exp_f32 v220, v220\n"
    "v_mfma_f32_16x16x32_bf16 v[176:179], v[0:3], a[16:19], v[176:179]\n"
    "v_exp_f32 v221, v221\n"
    "v_mfma_f32_16x16x32_bf16 v[176:179], v[4:7], a[20:23], v[176:179]\n"
    "v_fma_f32 v223, v223, s62, v26\n"
    "v_cvt_pk_bf16_f32 v218, v218, v219\n"
    "v_accvgpr_write_b32 a220, v216\n"
    "v_fma_f32 v224, v224, s62, v27\n"
    "v_mfma_f32_16x16x32_bf16 v[184:187], v[0:3], a[40:43], v[184:187]\n"
    "v_fma_f32 v225, v225, s62, v27\n"
    "v_cvt_pk_bf16_f32 v220, v220, v221\n"
    "v_mfma_f32_16x16x32_bf16 v[184:187], v[4:7], a[44:47], v[184:187]\n"
    "buffer_load_dwordx4 v22, s[44:47], s36 offen offset:1024 nt lds\n"
    "s_mov_b32 s57, 30976\n"
    "v_mfma_f32_16x16x32_bf16 v[192:195], v[0:3], a[64:67], v[192:195]\n"
    "v_exp_f32 v222, v222\n"
    "v_mfma_f32_16x16x32_bf16 v[192:195], v[4:7], a[68:71], v[192:195]\n"
    "buffer_store_dwordx4 v[132:135], v24, s[52:55], s57 offen sc0 sc1\n"
    "v_mfma_f32_16x16x32_bf16 v[200:203], v[0:3], a[88:91], v[200:203]\n"
    "v_accvgpr_write_b32 a221, v218\n"
    "v_fma_f32 v226, v226, s62, v27\n"
    "v_mfma_f32_16x16x32_bf16 v[200:203], v[4:7], a[92:95], v[200:203]\n"
    "s_waitcnt lgkmcnt(0)\n"
    "ds_read_b128 v[0:3], v16 offset:44032\n"
    "v_exp_f32 v223, v223\n"
    "v_exp_f32 v224, v224\n"
    "v_mfma_f32_16x16x32_bf16 v[164:167], v[8:11], a[96:99], 0\n"
    "v_exp_f32 v225, v225\n"
    "v_mfma_f32_16x16x32_bf16 v[164:167], v[12:15], a[100:103], v[164:167]\n"
    "ds_read_b128 v[4:7], v17 offset:44032\n"
    "v_fma_f32 v227, v227, s62, v27\n"
    "v_cvt_pk_bf16_f32 v222, v222, v223\n"
    "v_accvgpr_write_b32 a222, v220\n"
    "v_mfma_f32_16x16x32_bf16 v[172:175], v[8:11], a[120:123], 0\n"
    "v_fma_f32 v228, v228, s62, v27\n"
    "v_fma_f32 v229, v229, s62, v27\n"
    "v_mfma_f32_16x16x32_bf16 v[172:175], v[12:15], a[124:127], v[172:175]\n"
    "v_exp_f32 v226, v226\n"
    "v_exp_f32 v227, v227\n"
    "v_mfma_f32_16x16x32_bf16 v[180:183], v[8:11], a[0:3], 0\n"
    "v_exp_f32 v228, v228\n"
    "v_mfma_f32_16x16x32_bf16 v[180:183], v[12:15], a[4:7], v[180:183]\n"
    "v_cvt_pk_bf16_f32 v224, v224, v225\n"
    "v_accvgpr_write_b32 a223, v222\n"
    "v_fma_f32 v230, v230, s62, v27\n"
    "v_fma_f32 v231, v231, s62, v27\n"
    "v_mfma_f32_16x16x32_bf16 v[188:191], v[8:11], a[24:27], 0\n"
    "v_cvt_pk_bf16_f32 v226, v226, v227\n"
    "v_accvgpr_write_b32 a224, v224\n"
    "v_mfma_f32_16x16x32_bf16 v[188:191], v[12:15], a[28:31], v[188:191]\n"
    "v_exp_f32 v229, v229\n"
    "v_fma_f32 v232, v232, s62, v28\n"
    "v_exp_f32 v230, v230\n"
    "v_mfma_f32_16x16x32_bf16 v[196:199], v[8:11], a[48:51], 0\n"
    "v_exp_f32 v231, v231\n"
    "v_mfma_f32_16x16x32_bf16 v[196:199], v[12:15], a[52:55], v[196:199]\n"
    "v_fma_f32 v233, v233, s62, v28\n"
    "v_cvt_pk_bf16_f32 v228, v228, v229\n"
    "v_accvgpr_write_b32 a225, v226\n"
    "v_fma_f32 v234, v234, s62, v28\n"
    "v_mfma_f32_16x16x32_bf16 v[204:207], v[8:11], a[72:75], 0\n"
    "v_fma_f32 v235, v235, s62, v28\n"
    "v_cvt_pk_bf16_f32 v230, v230, v231\n"
    "v_mfma_f32_16x16x32_bf16 v[204:207], v[12:15], a[76:79], v[204:207]\n"
    "s_waitcnt lgkmcnt(0)\n"
    "ds_read_b128 v[8:11], v16 offset:45056\n"
    "v_exp_f32 v232, v232\n"
    "v_exp_f32 v233, v233\n"
    "v_mfma_f32_16x16x32_bf16 v[164:167], v[0:3], a[104:107], v[164:167]\n"
    "v_exp_f32 v234, v234\n"
    "v_mfma_f32_16x16x32_bf16 v[164:167], v[4:7], a[108:111], v[164:167]\n"
    "ds_read_b128 v[12:15], v17 offset:45056\n"
    "v_accvgpr_write_b32 a226, v228\n"
    "v_fma_f32 v236, v236, s62, v28\n"
    "v_fma_f32 v237, v237, s62, v28\n"
    "v_mfma_f32_16x16x32_bf16 v[172:175], v[0:3], a[128:131], v[172:175]\n"
    "v_cvt_pk_bf16_f32 v232, v232, v233\n"
    "v_accvgpr_write_b32 a227, v230\n"
    "v_mfma_f32_16x16x32_bf16 v[172:175], v[4:7], a[132:135], v[172:175]\n"
    "buffer_load_dwordx4 v23, s[44:47], s36 offen offset:2048 nt lds\n"
    "v_exp_f32 v235, v235\n"
    "v_mfma_f32_16x16x32_bf16 v[180:183], v[0:3], a[8:11], v[180:183]\n"
    "v_exp_f32 v236, v236\n"
    "v_mfma_f32_16x16x32_bf16 v[180:183], v[4:7], a[12:15], v[180:183]\n"
    "v_fma_f32 v238, v238, s62, v28\n"
    "v_fma_f32 v239, v239, s62, v28\n"
    "v_exp_f32 v237, v237\n"
    "v_mfma_f32_16x16x32_bf16 v[188:191], v[0:3], a[32:35], v[188:191]\n"
    "v_cvt_pk_bf16_f32 v234, v234, v235\n"
    "v_accvgpr_write_b32 a228, v232\n"
    "v_mfma_f32_16x16x32_bf16 v[188:191], v[4:7], a[36:39], v[188:191]\n"
    "v_fma_f32 v240, v240, s62, v29\n"
    "v_exp_f32 v238, v238\n"
    "v_fma_f32 v241, v241, s62, v29\n"
    "v_mfma_f32_16x16x32_bf16 v[196:199], v[0:3], a[56:59], v[196:199]\n"
    "v_exp_f32 v239, v239\n"
    "v_mfma_f32_16x16x32_bf16 v[196:199], v[4:7], a[60:63], v[196:199]\n"
    "buffer_store_dwordx4 v[108:111], v24, s[52:55], s57 offen offset:3072 sc0 sc1\n"
    "v_mfma_f32_16x16x32_bf16 v[204:207], v[0:3], a[80:83], v[204:207]\n"
    "v_cvt_pk_bf16_f32 v236, v236, v237\n"
    "v_accvgpr_write_b32 a229, v234\n"
    "v_mfma_f32_16x16x32_bf16 v[204:207], v[4:7], a[84:87], v[204:207]\n"
    "s_waitcnt lgkmcnt(0)\n"
    "ds_read_b128 v[0:3], v16 offset:49152\n"
    "s_add_u32 m0, s60, 122880\n"
    "v_fma_f32 v242, v242, s62, v29\n"
    "v_exp_f32 v240, v240\n"
    "v_mfma_f32_16x16x32_bf16 v[164:167], v[8:11], a[112:115], v[164:167]\n"
    "v_exp_f32 v241, v241\n"
    "v_mfma_f32_16x16x32_bf16 v[164:167], v[12:15], a[116:119], v[164:167]\n"
    "ds_read_b128 v[4:7], v17 offset:49152\n"
    "s_add_u32 s36, s61, 135168\n"
    "v_fma_f32 v243, v243, s62, v29\n"
    "v_cvt_pk_bf16_f32 v238, v238, v239\n"
    "v_accvgpr_write_b32 a230, v236\n"
    "v_mfma_f32_16x16x32_bf16 v[172:175], v[8:11], a[136:139], v[172:175]\n"
    "v_fma_f32 v244, v244, s62, v29\n"
    "v_fma_f32 v245, v245, s62, v29\n"
    "v_mfma_f32_16x16x32_bf16 v[172:175], v[12:15], a[140:143], v[172:175]\n"
    "buffer_load_dwordx4 v20, s[44:47], s36 offen nt lds\n"
    "v_exp_f32 v242, v242\n"
    "v_mfma_f32_16x16x32_bf16 v[180:183], v[8:11], a[16:19], v[180:183]\n"
    "v_exp_f32 v243, v243\n"
    "v_mfma_f32_16x16x32_bf16 v[180:183], v[12:15], a[20:23], v[180:183]\n"
    "v_cvt_pk_bf16_f32 v240, v240, v241\n"
    "v_accvgpr_write_b32 a231, v238\n"
    "v_fma_f32 v246, v246, s62, v29\n"
    "v_exp_f32 v244, v244\n"
    "v_mfma_f32_16x16x32_bf16 v[188:191], v[8:11], a[40:43], v[188:191]\n"
    "v_fma_f32 v247, v247, s62, v29\n"
    "v_cvt_pk_bf16_f32 v242, v242, v243\n"
    "v_mfma_f32_16x16x32_bf16 v[188:191], v[12:15], a[44:47], v[188:191]\n"
    "v_exp_f32 v245, v245\n"
    "v_accvgpr_write_b32 a232, v240\n"
    "v_fma_f32 v248, v248, s62, v30\n"
    "v_mfma_f32_16x16x32_bf16 v[196:199], v[8:11], a[64:67], v[196:199]\n"
    "v_exp_f32 v246, v246\n"
    "v_mfma_f32_16x16x32_bf16 v[196:199], v[12:15], a[68:71], v[196:199]\n"
    "v_fma_f32 v249, v249, s62, v30\n"
    "v_exp_f32 v247, v247\n"
    "v_cvt_pk_bf16_f32 v244, v244, v245\n"
    "v_mfma_f32_16x16x32_bf16 v[204:207], v[8:11], a[88:91], v[204:207]\n"
    "v_accvgpr_write_b32 a233, v242\n"
    "v_fma_f32 v250, v250, s62, v30\n"
    "v_mfma_f32_16x16x32_bf16 v[204:207], v[12:15], a[92:95], v[204:207]\n"
    "s_waitcnt lgkmcnt(0)\n"
    "ds_read_b128 v[8:11], v16 offset:50176\n"
    "v_exp_f32 v248, v248\n"
    "v_fma_f32 v251, v251, s62, v30\n"
    "v_mfma_f32_16x16x32_bf16 v[208:211], v[0:3], a[96:99], 0\n"
    "v_exp_f32 v249, v249\n"
    "v_mfma_f32_16x16x32_bf16 v[208:211], v[4:7], a[100:103], v[208:211]\n"
    "ds_read_b128 v[12:15], v17 offset:50176\n"
    "s_add_u32 s52, s52, 4718592\n"
    "s_addc_u32 s53, s53, 0\n"
    "s_mov_b32 s54, 147456\n"
    "v_cvt_pk_bf16_f32 v246, v246, v247\n"
    "v_mfma_f32_16x16x32_bf16 v[216:219], v[0:3], a[120:123], 0\n"
    "v_accvgpr_write_b32 a234, v244\n"
    "v_fma_f32 v252, v252, s62, v30\n"
    "v_mfma_f32_16x16x32_bf16 v[216:219], v[4:7], a[124:127], v[216:219]\n"
    "buffer_load_dwordx4 v22, s[44:47], s36 offen offset:1024 nt lds\n"
    "v_exp_f32 v250, v250\n"
    "v_fma_f32 v253, v253, s62, v30\n"
    "v_mfma_f32_16x16x32_bf16 v[224:227], v[0:3], a[0:3], 0\n"
    "v_exp_f32 v251, v251\n"
    "v_mfma_f32_16x16x32_bf16 v[224:227], v[4:7], a[4:7], v[224:227]\n"
    "v_cvt_pk_bf16_f32 v248, v248, v249\n"
    "v_accvgpr_write_b32 a235, v246\n"
    "v_fma_f32 v254, v254, s62, v30\n"
    "v_exp_f32 v252, v252\n"
    "v_mfma_f32_16x16x32_bf16 v[232:235], v[0:3], a[24:27], 0\n"
    "v_fma_f32 v255, v255, s62, v30\n"
    "v_cvt_pk_bf16_f32 v250, v250, v251\n"
    "v_mfma_f32_16x16x32_bf16 v[232:235], v[4:7], a[28:31], v[232:235]\n"
    "v_exp_f32 v253, v253\n"
    "v_accvgpr_write_b32 a236, v248\n"
    "v_exp_f32 v254, v254\n"
    "v_mfma_f32_16x16x32_bf16 v[240:243], v[0:3], a[48:51], 0\n"
    "v_exp_f32 v255, v255\n"
    "v_mfma_f32_16x16x32_bf16 v[240:243], v[4:7], a[52:55], v[240:243]\n"
    "v_cvt_pk_bf16_f32 v252, v252, v253\n"
    "v_accvgpr_write_b32 a237, v250\n"
    "v_cvt_pk_bf16_f32 v254, v254, v255\n"
    "v_accvgpr_write_b32 a238, v252\n"
    "v_accvgpr_write_b32 a239, v254\n"
    "v_mfma_f32_16x16x32_bf16 v[248:251], v[0:3], a[72:75], 0\n"
    "v_fma_f32 v160, v160, s62, v25\n"
    "v_fma_f32 v161, v161, s62, v25\n"
    "v_mfma_f32_16x16x32_bf16 v[248:251], v[4:7], a[76:79], v[248:251]\n"
    "s_waitcnt lgkmcnt(0)\n"
    "ds_read_b128 v[0:3], v16 offset:51200\n"
    "v_fma_f32 v162, v162, s62, v25\n"
    "v_exp_f32 v160, v160\n"
    "v_mfma_f32_16x16x32_bf16 v[208:211], v[8:11], a[104:107], v[208:211]\n"
    "v_exp_f32 v161, v161\n"
    "v_mfma_f32_16x16x32_bf16 v[208:211], v[12:15], a[108:111], v[208:211]\n"
    "ds_read_b128 v[4:7], v17 offset:51200\n"
    "v_fma_f32 v163, v163, s62, v25\n"
    "v_fma_f32 v164, v164, s62, v25\n"
    "v_mfma_f32_16x16x32_bf16 v[216:219], v[8:11], a[128:131], v[216:219]\n"
    "v_fma_f32 v165, v165, s62, v25\n"
    "v_cvt_pk_bf16_f32 v160, v160, v161\n"
    "v_mfma_f32_16x16x32_bf16 v[216:219], v[12:15], a[132:135], v[216:219]\n"
    "buffer_load_dwordx4 v23, s[44:47], s36 offen offset:2048 nt lds\n"
    "v_exp_f32 v162, v162\n"
    "v_mfma_f32_16x16x32_bf16 v[224:227], v[8:11], a[8:11], v[224:227]\n"
    "v_exp_f32 v163, v163\n"
    "v_mfma_f32_16x16x32_bf16 v[224:227], v[12:15], a[12:15], v[224:227]\n"
    "v_fma_f32 v166, v166, s62, v25\n"
    "v_exp_f32 v164, v164\n"
    "v_fma_f32 v167, v167, s62, v25\n"
    "v_mfma_f32_16x16x32_bf16 v[232:235], v[8:11], a[32:35], v[232:235]\n"
    "v_cvt_pk_bf16_f32 v162, v162, v163\n"
    "v_accvgpr_write_b32 a240, v160\n"
    "v_mfma_f32_16x16x32_bf16 v[232:235], v[12:15], a[36:39], v[232:235]\n"
    "v_exp_f32 v165, v165\n"
    "v_fma_f32 v168, v168, s62, v26\n"
    "v_exp_f32 v166, v166\n"
    "v_mfma_f32_16x16x32_bf16 v[240:243], v[8:11], a[56:59], v[240:243]\n"
    "v_exp_f32 v167, v167\n"
    "v_mfma_f32_16x16x32_bf16 v[240:243], v[12:15], a[60:63], v[240:243]\n"
    "v_fma_f32 v169, v169, s62, v26\n"
    "v_cvt_pk_bf16_f32 v164, v164, v165\n"
    "v_accvgpr_write_b32 a241, v162\n"
    "v_fma_f32 v170, v170, s62, v26\n"
    "v_mfma_f32_16x16x32_bf16 v[248:251], v[8:11], a[80:83], v[248:251]\n"
    "v_fma_f32 v171, v171, s62, v26\n"
    "v_cvt_pk_bf16_f32 v166, v166, v167\n"
    "v_mfma_f32_16x16x32_bf16 v[248:251], v[12:15], a[84:87], v[248:251]\n"
    "s_waitcnt lgkmcnt(0)\n"
    "ds_read_b128 v[8:11], v16 offset:55296\n"
    "s_add_u32 m0, s60, 135168\n"
    "v_exp_f32 v168, v168\n"
    "v_mfma_f32_16x16x32_bf16 v[208:211], v[0:3], a[112:115], v[208:211]\n"
    "v_exp_f32 v169, v169\n"
    "v_mfma_f32_16x16x32_bf16 v[208:211], v[4:7], a[116:119], v[208:211]\n"
    "ds_read_b128 v[12:15], v17 offset:55296\n"
    "s_add_u32 s36, s71, 128\n"
    "v_accvgpr_write_b32 a242, v164\n"
    "v_fma_f32 v172, v172, s62, v26\n"
    "v_mfma_f32_16x16x32_bf16 v[216:219], v[0:3], a[136:139], v[216:219]\n"
    "v_fma_f32 v173, v173, s62, v26\n"
    "v_cvt_pk_bf16_f32 v168, v168, v169\n"
    "v_mfma_f32_16x16x32_bf16 v[216:219], v[4:7], a[140:143], v[216:219]\n"
    "buffer_load_dwordx4 v20, s[48:51], s36 offen nt lds\n"
    "v_exp_f32 v170, v170\n"
    "v_mfma_f32_16x16x32_bf16 v[224:227], v[0:3], a[16:19], v[224:227]\n"
    "v_exp_f32 v171, v171\n"
    "v_mfma_f32_16x16x32_bf16 v[224:227], v[4:7], a[20:23], v[224:227]\n"
    "v_accvgpr_write_b32 a243, v166\n"
    "v_fma_f32 v174, v174, s62, v26\n"
    "v_exp_f32 v172, v172\n"
    "v_mfma_f32_16x16x32_bf16 v[232:235], v[0:3], a[40:43], v[232:235]\n"
    "v_fma_f32 v175, v175, s62, v26\n"
    "v_cvt_pk_bf16_f32 v170, v170, v171\n"
    "v_mfma_f32_16x16x32_bf16 v[232:235], v[4:7], a[44:47], v[232:235]\n"
    "s_memrealtime s[92:93]\n"
    "v_exp_f32 v173, v173\n"
    "v_accvgpr_write_b32 a244, v168\n"
    "v_mfma_f32_16x16x32_bf16 v[240:243], v[0:3], a[64:67], v[240:243]\n"
    "v_exp_f32 v174, v174\n"
    "v_mfma_f32_16x16x32_bf16 v[240:243], v[4:7], a[68:71], v[240:243]\n"
    "v_fma_f32 v176, v176, s62, v27\n"
    "v_fma_f32 v177, v177, s62, v27\n"
    "v_exp_f32 v175, v175\n"
    "v_mfma_f32_16x16x32_bf16 v[248:251], v[0:3], a[88:91], v[248:251]\n"
    "v_cvt_pk_bf16_f32 v172, v172, v173\n"
    "v_accvgpr_write_b32 a245, v170\n"
    "v_mfma_f32_16x16x32_bf16 v[248:251], v[4:7], a[92:95], v[248:251]\n"
    "s_waitcnt lgkmcnt(0)\n"
    "ds_read_b128 v[0:3], v16 offset:56320\n"
    "v_fma_f32 v178, v178, s62, v27\n"
    "v_exp_f32 v176, v176\n"
    "v_mfma_f32_16x16x32_bf16 v[212:215], v[8:11], a[96:99], 0\n"
    "v_exp_f32 v177, v177\n"
    "v_mfma_f32_16x16x32_bf16 v[212:215], v[12:15], a[100:103], v[212:215]\n"
    "s_add_u32 s94, s94, 190\n"
    "s_sub_u32 s95, s94, s92\n"
    "s_cmp_gt_i32 s95, 3\n"
    "s_cbranch_scc1 L_pc2_%=\n"
    "L_pr2_%=:\n"
    "ds_read_b128 v[4:7], v17 offset:56320\n"
    "v_mfma_f32_16x16x32_bf16 v[220:223], v[8:11], a[120:123], 0\n"
    "v_fma_f32 v179, v179, s62, v27\n"
    "v_cvt_pk_bf16_f32 v174, v174, v175\n"
    "v_mfma_f32_16x16x32_bf16 v[220:223], v[12:15], a[124:127], v[220:223]\n"
    "buffer_load_dwordx4 v21, s[48:51], s36 offen offset:1024 nt lds\n"
    "v_accvgpr_write_b32 a246, v172\n"
    "v_fma_f32 v180, v180, s62, v27\n"
    "v_mfma_f32_16x16x32_bf16 v[228:231], v[8:11], a[0:3], 0\n"
    "v_exp_f32 v178, v178\n"
    "v_mfma_f32_16x16x32_bf16 v[228:231], v[12:15], a[4:7], v[228:231]\n"
    "v_fma_f32 v181, v181, s62, v27\n"
    "v_exp_f32 v179, v179\n"
    "v_cvt_pk_bf16_f32 v176, v176, v177\n"
    "v_mfma_f32_16x16x32_bf16 v[236:239], v[8:11], a[24:27], 0\n"
    "v_accvgpr_write_b32 a247, v174\n"
    "v_fma_f32 v182, v182, s62, v27\n"
    "v_mfma_f32_16x16x32_bf16 v[236:239], v[12:15], a[28:31], v[236:239]\n"
    "v_exp_f32 v180, v180\n"
    "v_fma_f32 v183, v183, s62, v27\n"
    "v_exp_f32 v181, v181\n"
    "v_mfma_f32_16x16x32_bf16 v[244:247], v[8:11], a[48:51], 0\n"
    "v_exp_f32 v182, v182\n"
    "v_mfma_f32_16x16x32_bf16 v[244:247], v[12:15], a[52:55], v[244:247]\n"
    "v_cvt_pk_bf16_f32 v178, v178, v179\n"
    "v_accvgpr_write_b32 a248, v176\n"
    "v_fma_f32 v184, v184, s62, v28\n"
    "v_fma_f32 v185, v185, s62, v28\n"
    "v_mfma_f32_16x16x32_bf16 v[252:255], v[8:11], a[72:75], 0\n"
    "v_cvt_pk_bf16_f32 v180, v180, v181\n"
    "v_accvgpr_write_b32 a249, v178\n"
    "v_mfma_f32_16x16x32_bf16 v[252:255], v[12:15], a[76:79], v[252:255]\n"
    "s_waitcnt lgkmcnt(0)\n"
    "ds_read_b128 v[8:11], v16 offset:57344\n"
    "v_exp_f32 v183, v183\n"
    "v_fma_f32 v186, v186, s62, v28\n"
    "v_mfma_f32_16x16x32_bf16 v[212:215], v[0:3], a[104:107], v[212:215]\n"
    "v_exp_f32 v184, v184\n"
    "v_mfma_f32_16x16x32_bf16 v[212:215], v[4:7], a[108:111], v[212:215]\n"
    "ds_read_b128 v[12:15], v17 offset:57344\n"
    "v_fma_f32 v187, v187, s62, v28\n"
    "v_exp_f32 v185, v185\n"
    "v_mfma_f32_16x16x32_bf16 v[220:223], v[0:3], a[128:131], v[220:223]\n"
    "v_cvt_pk_bf16_f32 v182, v182, v183\n"
    "v_accvgpr_write_b32 a250, v180\n"
    "v_mfma_f32_16x16x32_bf16 v[220:223], v[4:7], a[132:135], v[220:223]\n"
    "v_add_u32 v16, 61440, v16\n"
    "buffer_load_dwordx4 v31, s[48:51], s36 offen offset:2048 nt lds\n"
    "v_fma_f32 v188, v188, s62, v28\n"
    "v_mfma_f32_16x16x32_bf16 v[228:231], v[0:3], a[8:11], v[228:231]\n"
    "v_exp_f32 v186, v186\n"
    "v_mfma_f32_16x16x32_bf16 v[228:231], v[4:7], a[12:15], v[228:231]\n"
    "v_add_u32 v17, 61440, v17\n"
    "v_fma_f32 v189, v189, s62, v28\n"
    "v_exp_f32 v187, v187\n"
    "v_mfma_f32_16x16x32_bf16 v[236:239], v[0:3], a[32:35], v[236:239]\n"
    "v_cvt_pk_bf16_f32 v184, v184, v185\n"
    "v_accvgpr_write_b32 a251, v182\n"
    "v_mfma_f32_16x16x32_bf16 v[236:239], v[4:7], a[36:39], v[236:239]\n"
    "v_fma_f32 v190, v190, s62, v28\n"
    "v_exp_f32 v188, v188\n"
    "v_fma_f32 v191, v191, s62, v28\n"
    "v_mfma_f32_16x16x32_bf16 v[244:247], v[0:3], a[56:59], v[244:247]\n"
    "v_exp_f32 v189, v189\n"
    "v_mfma_f32_16x16x32_bf16 v[244:247], v[4:7], a[60:63], v[244:247]\n"
    "s_waitcnt vmcnt(22) lgkmcnt(0)\n"
    "s_barrier\n"
    "v_cvt_pk_bf16_f32 v186, v186, v187\n"
    "v_accvgpr_write_b32 a252, v184\n"
    "v_fma_f32 v192, v192, s62, v29\n"
    "v_mfma_f32_16x16x32_bf16 v[252:255], v[0:3], a[80:83], v[252:255]\n"
    "v_fma_f32 v193, v193, s62, v29\n"
    "v_cvt_pk_bf16_f32 v188, v188, v189\n"
    "v_mfma_f32_16x16x32_bf16 v[252:255], v[4:7], a[84:87], v[252:255]\n"
    "ds_read_b128 v[0:3], v16 offset:0\n"
    "s_add_u32 m0, s60, 24576\n"
    "v_exp_f32 v190, v190\n"
    "v_mfma_f32_16x16x32_bf16 v[212:215], v[8:11], a[112:115], v[212:215]\n"
    "v_exp_f32 v191, v191\n"
    "v_mfma_f32_16x16x32_bf16 v[212:215], v[12:15], a[116:119], v[212:215]\n"
    "ds_read_b128 v[4:7], v17 offset:0\n"
    "s_add_u32 s36, s71, 0\n"
    "v_accvgpr_write_b32 a253, v186\n"
    "v_fma_f32 v194, v194, s62, v29\n"
    "v_mfma_f32_16x16x32_bf16 v[220:223], v[8:11], a[136:139], v[220:223]\n"
    "v_fma_f32 v195, v195, s62, v29\n"
    "v_cvt_pk_bf16_f32 v190, v190, v191\n"
    "v_mfma_f32_16x16x32_bf16 v[220:223], v[12:15], a[140:143], v[220:223]\n"
    "buffer_load_dwordx4 v20, s[48:51], s36 offen nt lds\n"
    "v_exp_f32 v192, v192\n"
    "v_mfma_f32_16x16x32_bf16 v[228:231], v[8:11], a[16:19], v[228:231]\n"
    "v_exp_f32 v193, v193\n"
    "v_mfma_f32_16x16x32_bf16 v[228:231], v[12:15], a[20:23], v[228:231]\n"
    "v_accvgpr_write_b32 a254, v188\n"
    "v_fma_f32 v196, v196, s62, v29\n"
    "v_exp_f32 v194, v194\n"
    "v_mfma_f32_16x16x32_bf16 v[236:239], v[8:11], a[40:43], v[236:239]\n"
    "v_fma_f32 v197, v197, s62, v29\n"
    "v_cvt_pk_bf16_f32 v32, v192, v193\n"
    "v_mfma_f32_16x16x32_bf16 v[236:239], v[12:15], a[44:47], v[236:239]\n"
    "v_exp_f32 v195, v195\n"
    "v_accvgpr_write_b32 a255, v190\n"
    "v_fma_f32 v198, v198, s62, v29\n"
    "v_mfma_f32_16x16x32_bf16 v[244:247], v[8:11], a[64:67], v[244:247]\n"
    "v_exp_f32 v196, v196\n"
    "v_mfma_f32_16x16x32_bf16 v[244:247], v[12:15], a[68:71], v[244:247]\n"
    "v_fma_f32 v199, v199, s62, v29\n"
    "v_exp_f32 v197, v197\n"
    "v_cvt_pk_bf16_f32 v33, v194, v195\n"
    "v_mfma_f32_16x16x32_bf16 v[252:255], v[8:11], a[88:91], v[252:255]\n"
    "v_fma_f32 v200, v200, s62, v30\n"
    "v_fma_f32 v201, v201, s62, v30\n"
    "v_mfma_f32_16x16x32_bf16 v[252:255], v[12:15], a[92:95], v[252:255]\n"
    "s_waitcnt lgkmcnt(0)\n"
    "ds_read_b128 v[8:11], v16 offset:1024\n"
    "v_exp_f32 v198, v198\n"
    "v_exp_f32 v199, v199\n"
    "v_mfma_f32_16x16x32_bf16 v[160:163], v[0:3], a[96:99], 0\n"
    "v_exp_f32 v200, v200\n"
    "v_mfma_f32_16x16x32_bf16 v[160:163], v[4:7], a[100:103], v[160:163]\n"
    "ds_read_b128 v[12:15], v17 offset:1024\n"
    "s_add_u32 s44, s44, 4718592\n"
    "s_addc_u32 s45, s45, 0\n"
    "s_cmp_eq_u32 s64, 3\n"
    "s_cselect_b32 s46, 0, s46\n"
    "v_mfma_f32_16x16x32_bf16 v[168:171], v[0:3], a[120:123], 0\n"
    "v_cvt_pk_bf16_f32 v34, v196, v197\n"
    "v_fma_f32 v202, v202, s62, v30\n"
    "v_mfma_f32_16x16x32_bf16 v[168:171], v[4:7], a[124:127], v[168:171]\n"
    "buffer_load_dwordx4 v21, s[48:51], s36 offen offset:1024 nt lds\n"
    "v_fma_f32 v203, v203, s62, v30\n"
    "v_exp_f32 v201, v201\n"
    "v_mfma_f32_16x16x32_bf16 v[176:179], v[0:3], a[0:3], 0\n"
    "v_exp_f32 v202, v202\n"
    "v_mfma_f32_16x16x32_bf16 v[176:179], v[4:7], a[4:7], v[176:179]\n"
    "v_cvt_pk_bf16_f32 v35, v198, v199\n"
    "v_fma_f32 v204, v204, s62, v30\n"
    "v_fma_f32 v205, v205, s62, v30\n"
    "v_exp_f32 v203, v203\n"
    "v_mfma_f32_16x16x32_bf16 v[184:187], v[0:3], a[24:27], 0\n"
    "v_cvt_pk_bf16_f32 v36, v200, v201\n"
    "v_fma_f32 v206, v206, s62, v30\n"
    "v_mfma_f32_16x16x32_bf16 v[184:187], v[4:7], a[28:31], v[184:187]\n"
    "v_exp_f32 v204, v204\n"
    "v_fma_f32 v207, v207, s62, v30\n"
    "v_exp_f32 v205, v205\n"
    "v_mfma_f32_16x16x32_bf16 v[192:195], v[0:3], a[48:51], 0\n"
    "v_exp_f32 v206, v206\n"
    "v_mfma_f32_16x16x32_bf16 v[192:195], v[4:7], a[52:55], v[192:195]\n"
    "v_cvt_pk_bf16_f32 v37, v202, v203\n"
    "v_exp_f32 v207, v207\n"
    "v_cvt_pk_bf16_f32 v38, v204, v205\n"
    "v_cvt_pk_bf16_f32 v39, v206, v207\n"
    "v_mfma_f32_16x16x32_bf16 v[200:203], v[0:3], a[72:75], 0\n"
    "v_fma_f32 v208, v208, s62, v25\n"
    "v_fma_f32 v209, v209, s62, v25\n"
    "v_mfma_f32_16x16x32_bf16 v[200:203], v[4:7], a[76:79], v[200:203]\n"
    "s_waitcnt lgkmcnt(0)\n"
    "ds_read_b128 v[0:3], v16 offset:2048\n"
    "v_fma_f32 v210, v210, s62, v25\n"
    "v_exp_f32 v208, v208\n"
    "v_mfma_f32_16x16x32_bf16 v[160:163], v[8:11], a[104:107], v[160:163]\n"
    "v_exp_f32 v209, v209\n"
    "v_mfma_f32_16x16x32_bf16 v[160:163], v[12:15], a[108:111], v[160:163]\n"
    "ds_read_b128 v[4:7], v17 offset:2048\n"
    "v_fma_f32 v211, v211, s62, v25\n"
    "v_fma_f32 v212, v212, s62, v25\n"
    "v_mfma_f32_16x16x32_bf16 v[168:171], v[8:11], a[128:131], v[168:171]\n"
    "v_fma_f32 v213, v213, s62, v25\n"
    "v_cvt_pk_bf16_f32 v40, v208, v209\n"
    "v_mfma_f32_16x16x32_bf16 v[168:171], v[12:15], a[132:135], v[168:171]\n"
    "buffer_load_dwordx4 v31, s[48:51], s36 offen offset:2048 nt lds\n"
    "v_mfma_f32_16x16x32_bf16 v[176:179], v[8:11], a[8:11], v[176:179]\n"
    "v_exp_f32 v210, v210\n"
    "v_mfma_f32_16x16x32_bf16 v[176:179], v[12:15], a[12:15], v[176:179]\n"
    "v_exp_f32 v211, v211\n"
    "v_fma_f32 v214, v214, s62, v25\n"
    "v_mfma_f32_16x16x32_bf16 v[184:187], v[8:11], a[32:35], v[184:187]\n"
    "v_fma_f32 v215, v215, s62, v25\n"
    "v_cvt_pk_bf16_f32 v41, v210, v211\n"
    "v_mfma_f32_16x16x32_bf16 v[184:187], v[12:15], a[36:39], v[184:187]\n"
    "v_exp_f32 v212, v212\n"
    "v_exp_f32 v213, v213\n"
    "v_mfma_f32_16x16x32_bf16 v[192:195], v[8:11], a[56:59], v[192:195]\n"
    "v_exp_f32 v214, v214\n"
    "v_mfma_f32_16x16x32_bf16 v[192:195], v[12:15], a[60:63], v[192:195]\n"
    "v_fma_f32 v216, v216, s62, v26\n"
    "v_fma_f32 v217, v217, s62, v26\n"
    "v_exp_f32 v215, v215\n"
    "v_mfma_f32_16x16x32_bf16 v[200:203], v[8:11], a[80:83], v[200:203]\n"
    "v_cvt_pk_bf16_f32 v42, v212, v213\n"
    "v_fma_f32 v218, v218, s62, v26\n"
    "v_mfma_f32_16x16x32_bf16 v[200:203], v[12:15], a[84:87], v[200:203]\n"
    "s_waitcnt lgkmcnt(0)\n"
    "ds_read_b128 v[8:11], v16 offset:6144\n"
    "s_add_u32 m0, s60, 36864\n"
    "v_exp_f32 v216, v216\n"
    "v_mfma_f32_16x16x32_bf16 v[160:163], v[0:3], a[112:115], v[160:163]\n"
    "v_exp_f32 v217, v217\n"
    "v_mfma_f32_16x16x32_bf16 v[160:163], v[4:7], a[116:119], v[160:163]\n"
    "ds_read_b128 v[12:15], v17 offset:6144\n"
    "s_add_u32 s36, s71, 36864\n"
    "v_fma_f32 v219, v219, s62, v26\n"
    "v_cvt_pk_bf16_f32 v43, v214, v215\n"
    "v_mfma_f32_16x16x32_bf16 v[168:171], v[0:3], a[136:139], v[168:171]\n"
    "v_fma_f32 v220, v220, s62, v26\n"
    "v_fma_f32 v221, v221, s62, v26\n"
    "v_mfma_f32_16x16x32_bf16 v[168:171], v[4:7], a[140:143], v[168:171]\n"
    "buffer_load_dwordx4 v20, s[48:51], s36 offen nt lds\n"
    "v_mfma_f32_16x16x32_bf16 v[176:179], v[0:3], a[16:19], v[176:179]\n"
    "v_exp_f32 v218, v218\n"
    "v_mfma_f32_16x16x32_bf16 v[176:179], v[4:7], a[20:23], v[176:179]\n"
    "v_exp_f32 v219, v219\n"
    "v_cvt_pk_bf16_f32 v44, v216, v217\n"
    "v_mfma_f32_16x16x32_bf16 v[184:187], v[0:3], a[40:43], v[184:187]\n"
    "v_fma_f32 v222, v222, s62, v26\n"
    "v_fma_f32 v223, v223, s62, v26\n"
    "v_mfma_f32_16x16x32_bf16 v[184:187], v[4:7], a[44:47], v[184:187]\n"
    "v_exp_f32 v220, v220\n"
    "v_exp_f32 v221, v221\n"
    "v_mfma_f32_16x16x32_bf16 v[192:195], v[0:3], a[64:67], v[192:195]\n"
    "v_exp_f32 v222, v222\n"
    "v_mfma_f32_16x16x32_bf16 v[192:195], v[4:7], a[68:71], v[192:195]\n"
    "v_cvt_pk_bf16_f32 v45, v218, v219\n"
    "v_fma_f32 v224, v224, s62, v27\n"
    "v_fma_f32 v225, v225, s62, v27\n"
    "v_mfma_f32_16x16x32_bf16 v[200:203], v[0:3], a[88:91], v[200:203]\n"
    "v_cvt_pk_bf16_f32 v46, v220, v221\n"
    "v_fma_f32 v226, v226, s62, v27\n"
    "v_mfma_f32_16x16x32_bf16 v[200:203], v[4:7], a[92:95], v[200:203]\n"
    "s_waitcnt lgkmcnt(0)\n"
    "ds_read_b128 v[0:3], v16 offset:7168\n"
    "v_exp_f32 v223, v223\n"
    "v_mfma_f32_16x16x32_bf16 v[164:167], v[8:11], a[96:99], 0\n"
    "v_exp_f32 v224, v224\n"
    "v_mfma_f32_16x16x32_bf16 v[164:167], v[12:15], a[100:103], v[164:167]\n"
    "ds_read_b128 v[4:7], v17 offset:7168\n"
    "v_fma_f32 v227, v227, s62, v27\n"
    "v_exp_f32 v225, v225\n"
    "v_mfma_f32_16x16x32_bf16 v[172:175], v[8:11], a[120:123], 0\n"
    "v_cvt_pk_bf16_f32 v47, v222, v223\n"
    "v_fma_f32 v228, v228, s62, v27\n"
    "v_mfma_f32_16x16x32_bf16 v[172:175], v[12:15], a[124:127], v[172:175]\n"
    "buffer_load_dwordx4 v21, s[48:51], s36 offen offset:1024 nt lds\n"
    "v_mfma_f32_16x16x32_bf16 v[180:183], v[8:11], a[0:3], 0\n"
    "v_exp_f32 v226, v226\n"
    "v_mfma_f32_16x16x32_bf16 v[180:183], v[12:15], a[4:7], v[180:183]\n"
    "v_fma_f32 v229, v229, s62, v27\n"
    "v_exp_f32 v227, v227\n"
    "v_cvt_pk_bf16_f32 v48, v224, v225\n"
    "v_mfma_f32_16x16x32_bf16 v[188:191], v[8:11], a[24:27], 0\n"
    "v_fma_f32 v230, v230, s62, v27\n"
    "v_fma_f32 v231, v231, s62, v27\n"
    "v_mfma_f32_16x16x32_bf16 v[188:191], v[12:15], a[28:31], v[188:191]\n"
    "v_exp_f32 v228, v228\n"
    "v_exp_f32 v229, v229\n"
    "v_mfma_f32_16x16x32_bf16 v[196:199], v[8:11], a[48:51], 0\n"
    "v_exp_f32 v230, v230\n"
    "v_mfma_f32_16x16x32_bf16 v[196:199], v[12:15], a[52:55], v[196:199]\n"
    "v_cvt_pk_bf16_f32 v49, v226, v227\n"
    "v_fma_f32 v232, v232, s62, v28\n"
    "v_fma_f32 v233, v233, s62, v28\n"
    "v_mfma_f32_16x16x32_bf16 v[204:207], v[8:11], a[72:75], 0\n"
    "v_cvt_pk_bf16_f32 v50, v228, v229\n"
    "v_fma_f32 v234, v234, s62, v28\n"
    "v_mfma_f32_16x16x32_bf16 v[204:207], v[12:15], a[76:79], v[204:207]\n"
    "s_waitcnt lgkmcnt(0)\n"
    "ds_read_b128 v[8:11], v16 offset:8192\n"
    "v_exp_f32 v231, v231\n"
    "v_mfma_f32_16x16x32_bf16 v[164:167], v[0:3], a[104:107], v[164:167]\n"
    "v_exp_f32 v232, v232\n"
    "v_mfma_f32_16x16x32_bf16 v[164:167], v[4:7], a[108:111], v[164:167]\n"
    "ds_read_b128 v[12:15], v17 offset:8192\n"
    "v_fma_f32 v235, v235, s62, v28\n"
    "v_exp_f32 v233, v233\n"
    "v_mfma_f32_16x16x32_bf16 v[172:175], v[0:3], a[128:131], v[172:175]\n"
    "v_cvt_pk_bf16_f32 v51, v230, v231\n"
    "v_fma_f32 v236, v236, s62, v28\n"
    "v_mfma_f32_16x16x32_bf16 v[172:175], v[4:7], a[132:135], v[172:175]\n"
    "buffer_load_dwordx4 v31, s[48:51], s36 offen offset:2048 nt lds\n"
    "v_exp_f32 v234, v234\n"
    "v_mfma_f32_16x16x32_bf16 v[180:183], v[0:3], a[8:11], v[180:183]\n"
    "v_exp_f32 v235, v235\n"
    "v_mfma_f32_16x16x32_bf16 v[180:183], v[4:7], a[12:15], v[180:183]\n"
    "v_fma_f32 v237, v237, s62, v28\n"
    "v_cvt_pk_bf16_f32 v52, v232, v233\n"
    "v_fma_f32 v238, v238, s62, v28\n"
    "v_mfma_f32_16x16x32_bf16 v[188:191], v[0:3], a[32:35], v[188:191]\n"
    "v_fma_f32 v239, v239, s62, v28\n"
    "v_cvt_pk_bf16_f32 v53, v234, v235\n"
    "v_mfma_f32_16x16x32_bf16 v[188:191], v[4:7], a[36:39], v[188:191]\n"
    "v_exp_f32 v236, v236\n"
    "v_exp_f32 v237, v237\n"
    "v_mfma_f32_16x16x32_bf16 v[196:199], v[0:3], a[56:59], v[196:199]\n"
    "v_exp_f32 v238, v238\n"
    "v_mfma_f32_16x16x32_bf16 v[196:199], v[4:7], a[60:63], v[196:199]\n"
    "v_fma_f32 v240, v240, s62, v29\n"
    "v_fma_f32 v241, v241, s62, v29\n"
    "v_exp_f32 v239, v239\n"
    "v_mfma_f32_16x16x32_bf16 v[204:207], v[0:3], a[80:83], v[204:207]\n"
    "v_cvt_pk_bf16_f32 v54, v236, v237\n"
    "v_fma_f32 v242, v242, s62, v29\n"
    "v_mfma_f32_16x16x32_bf16 v[204:207], v[4:7], a[84:87], v[204:207]\n"
    "s_waitcnt lgkmcnt(0)\n"
    "ds_read_b128 v[0:3], v16 offset:12288\n"
    "s_add_u32 m0, s60, 49152\n"
    "v_exp_f32 v240, v240\n"
    "v_mfma_f32_16x16x32_bf16 v[164:167], v[8:11], a[112:115], v[164:167]\n"
    "v_exp_f32 v241, v241\n"
    "v_mfma_f32_16x16x32_bf16 v[164:167], v[12:15], a[116:119], v[164:167]\n"
    "ds_read_b128 v[4:7], v17 offset:12288\n"
    "s_add_u32 s36, s71, 73728\n"
    "v_fma_f32 v243, v243, s62, v29\n"
    "v_cvt_pk_bf16_f32 v55, v238, v239\n"
    "v_mfma_f32_16x16x32_bf16 v[172:175], v[8:11], a[136:139], v[172:175]\n"
    "v_fma_f32 v244, v244, s62, v29\n"
    "v_fma_f32 v245, v245, s62, v29\n"
    "v_mfma_f32_16x16x32_bf16 v[172:175], v[12:15], a[140:143], v[172:175]\n"
    "buffer_load_dwordx4 v20, s[48:51], s36 offen nt lds\n"
    "v_mfma_f32_16x16x32_bf16 v[180:183], v[8:11], a[16:19], v[180:183]\n"
    "v_exp_f32 v242, v242\n"
    "v_mfma_f32_16x16x32_bf16 v[180:183], v[12:15], a[20:23], v[180:183]\n"
    "v_exp_f32 v243, v243\n"
    "v_cvt_pk_bf16_f32 v56, v240, v241\n"
    "v_fma_f32 v246, v246, s62, v29\n"
    "v_mfma_f32_16x16x32_bf16 v[188:191], v[8:11], a[40:43], v[188:191]\n"
    "v_fma_f32 v247, v247, s62, v29\n"
    "v_cvt_pk_bf16_f32 v57, v242, v243\n"
    "v_mfma_f32_16x16x32_bf16 v[188:191], v[12:15], a[44:47], v[188:191]\n"
    "v_exp_f32 v244, v244\n"
    "v_exp_f32 v245, v245\n"
    "v_mfma_f32_16x16x32_bf16 v[196:199], v[8:11], a[64:67], v[196:199]\n"
    "v_exp_f32 v246, v246\n"
    "v_mfma_f32_16x16x32_bf16 v[196:199], v[12:15], a[68:71], v[196:199]\n"
    "v_fma_f32 v248, v248, s62, v30\n"
    "v_fma_f32 v249, v249, s62, v30\n"
    "v_exp_f32 v247, v247\n"
    "v_mfma_f32_16x16x32_bf16 v[204:207], v[8:11], a[88:91], v[204:207]\n"
    "v_cvt_pk_bf16_f32 v58, v244, v245\n"
    "v_fma_f32 v250, v250, s62, v30\n"
    "v_mfma_f32_16x16x32_bf16 v[204:207], v[12:15], a[92:95], v[204:207]\n"
    "s_waitcnt lgkmcnt(0)\n"
    "ds_read_b128 v[8:11], v16 offset:13312\n"
    "v_exp_f32 v248, v248\n"
    "v_mfma_f32_16x16x32_bf16 v[208:211], v[0:3], a[96:99], 0\n"
    "v_exp_f32 v249, v249\n"
    "v_mfma_f32_16x16x32_bf16 v[208:211], v[4:7], a[100:103], v[208:211]\n"
    "ds_read_b128 v[12:15], v17 offset:13312\n"
    "v_fma_f32 v251, v251, s62, v30\n"
    "v_cvt_pk_bf16_f32 v59, v246, v247\n"
    "v_mfma_f32_16x16x32_bf16 v[216:219], v[0:3], a[120:123], 0\n"
    "v_fma_f32 v252, v252, s62, v30\n"
    "v_fma_f32 v253, v253, s62, v30\n"
    "v_mfma_f32_16x16x32_bf16 v[216:219], v[4:7], a[124:127], v[216:219]\n"
    "buffer_load_dwordx4 v21, s[48:51], s36 offen offset:1024 nt lds\n"
    "v_exp_f32 v250, v250\n"
    "v_mfma_f32_16x16x32_bf16 v[224:227], v[0:3], a[0:3], 0\n"
    "v_exp_f32 v251, v251\n"
    "v_mfma_f32_16x16x32_bf16 v[224:227], v[4:7], a[4:7], v[224:227]\n"
    "v_cvt_pk_bf16_f32 v60, v248, v249\n"
    "v_fma_f32 v254, v254, s62, v30\n"
    "v_exp_f32 v252, v252\n"
    "v_mfma_f32_16x16x32_bf16 v[232:235], v[0:3], a[24:27], 0\n"
    "v_fma_f32 v255, v255, s62, v30\n"
    "v_cvt_pk_bf16_f32 v61, v250, v251\n"
    "v_mfma_f32_16x16x32_bf16 v[232:235], v[4:7], a[28:31], v[232:235]\n"
    "v_exp_f32 v253, v253\n"
    "v_exp_f32 v254, v254\n"
    "v_mfma_f32_16x16x32_bf16 v[240:243], v[0:3], a[48:51], 0\n"
    "v_exp_f32 v255, v255\n"
    "v_mfma_f32_16x16x32_bf16 v[240:243], v[4:7], a[52:55], v[240:243]\n"
    "v_cvt_pk_bf16_f32 v62, v252, v253\n"
    "v_cvt_pk_bf16_f32 v63, v254, v255\n"
    "v_fma_f32 v160, v160, s62, v25\n"
    "v_mfma_f32_16x16x32_bf16 v[248:251], v[0:3], a[72:75], 0\n"
    "v_fma_f32 v161, v161, s62, v25\n"
    "v_fma_f32 v162, v162, s62, v25\n"
    "v_mfma_f32_16x16x32_bf16 v[248:251], v[4:7], a[76:79], v[248:251]\n"
    "s_waitcnt lgkmcnt(0)\n"
    "ds_read_b128 v[0:3], v16 offset:14336\n"
    "v_exp_f32 v160, v160\n"
    "v_mfma_f32_16x16x32_bf16 v[208:211], v[8:11], a[104:107], v[208:211]\n"
    "v_exp_f32 v161, v161\n"
    "v_mfma_f32_16x16x32_bf16 v[208:211], v[12:15], a[108:111], v[208:211]\n"
    "ds_read_b128 v[4:7], v17 offset:14336\n"
    "v_fma_f32 v163, v163, s62, v25\n"
    "v_fma_f32 v164, v164, s62, v25\n"
    "v_mfma_f32_16x16x32_bf16 v[216:219], v[8:11], a[128:131], v[216:219]\n"
    "v_fma_f32 v165, v165, s62, v25\n"
    "v_cvt_pk_bf16_f32 v64, v160, v161\n"
    "v_mfma_f32_16x16x32_bf16 v[216:219], v[12:15], a[132:135], v[216:219]\n"
    "buffer_load_dwordx4 v31, s[48:51], s36 offen offset:2048 nt lds\n"
    "v_mfma_f32_16x16x32_bf16 v[224:227], v[8:11], a[8:11], v[224:227]\n"
    "v_exp_f32 v162, v162\n"
    "v_mfma_f32_16x16x32_bf16 v[224:227], v[12:15], a[12:15], v[224:227]\n"
    "v_exp_f32 v163, v163\n"
    "v_fma_f32 v166, v166, s62, v25\n"
    "v_mfma_f32_16x16x32_bf16 v[232:235], v[8:11], a[32:35], v[232:235]\n"
    "v_fma_f32 v167, v167, s62, v25\n"
    "v_cvt_pk_bf16_f32 v65, v162, v163\n"
    "v_mfma_f32_16x16x32_bf16 v[232:235], v[12:15], a[36:39], v[232:235]\n"
    "v_exp_f32 v164, v164\n"
    "v_exp_f32 v165, v165\n"
    "v_mfma_f32_16x16x32_bf16 v[240:243], v[8:11], a[56:59], v[240:243]\n"
    "v_exp_f32 v166, v166\n"
    "v_mfma_f32_16x16x32_bf16 v[240:243], v[12:15], a[60:63], v[240:243]\n"
    "v_fma_f32 v168, v168, s62, v26\n"
    "v_fma_f32 v169, v169, s62, v26\n"
    "v_exp_f32 v167, v167\n"
    "v_mfma_f32_16x16x32_bf16 v[248:251], v[8:11], a[80:83], v[248:251]\n"
    "v_cvt_pk_bf16_f32 v66, v164, v165\n"
    "v_fma_f32 v170, v170, s62, v26\n"
    "v_mfma_f32_16x16x32_bf16 v[248:251], v[12:15], a[84:87], v[248:251]\n"
    "s_waitcnt lgkmcnt(0)\n"
    "ds_read_b128 v[8:11], v16 offset:18432\n"
    "s_add_u32 m0, s60, 0\n"
    "v_exp_f32 v168, v168\n"
    "v_mfma_f32_16x16x32_bf16 v[208:211], v[0:3], a[112:115], v[208:211]\n"
    "v_exp_f32 v169, v169\n"
    "v_mfma_f32_16x16x32_bf16 v[208:211], v[4:7], a[116:119], v[208:211]\n"
    "ds_read_b128 v[12:15], v17 offset:18432\n"
    "s_add_u32 s36, s61, 0\n"
    "v_fma_f32 v171, v171, s62, v26\n"
    "v_cvt_pk_bf16_f32 v67, v166, v167\n"
    "v_mfma_f32_16x16x32_bf16 v[216:219], v[0:3], a[136:139], v[216:219]\n"
    "v_fma_f32 v172, v172, s62, v26\n"
    "v_fma_f32 v173, v173, s62, v26\n"
    "v_mfma_f32_16x16x32_bf16 v[216:219], v[4:7], a[140:143], v[216:219]\n"
    "buffer_load_dwordx4 v20, s[40:43], s36 offen nt lds\n"
    "v_exp_f32 v170, v170\n"
    "v_mfma_f32_16x16x32_bf16 v[224:227], v[0:3], a[16:19], v[224:227]\n"
    "v_exp_f32 v171, v171\n"
    "v_mfma_f32_16x16x32_bf16 v[224:227], v[4:7], a[20:23], v[224:227]\n"
    "v_cvt_pk_bf16_f32 v68, v168, v169\n"
    "v_fma_f32 v174, v174, s62, v26\n"
    "v_exp_f32 v172, v172\n"
    "v_mfma_f32_16x16x32_bf16 v[232:235], v[0:3], a[40:43], v[232:235]\n"
    "v_fma_f32 v175, v175, s62, v26\n"
    "v_cvt_pk_bf16_f32 v69, v170, v171\n"
    "v_mfma_f32_16x16x32_bf16 v[232:235], v[4:7], a[44:47], v[232:235]\n"
    "s_memrealtime s[92:93]\n"
    "v_exp_f32 v173, v173\n"
    "v_fma_f32 v176, v176, s62, v27\n"
    "v_mfma_f32_16x16x32_bf16 v[240:243], v[0:3], a[64:67], v[240:243]\n"
    "v_exp_f32 v174, v174\n"
    "v_mfma_f32_16x16x32_bf16 v[240:243], v[4:7], a[68:71], v[240:243]\n"
    "v_fma_f32 v177, v177, s62, v27\n"
    "v_exp_f32 v175, v175\n"
    "v_mfma_f32_16x16x32_bf16 v[248:251], v[0:3], a[88:91], v[248:251]\n"
    "v_cvt_pk_bf16_f32 v70, v172, v173\n"
    "v_fma_f32 v178, v178, s62, v27\n"
    "v_mfma_f32_16x16x32_bf16 v[248:251], v[4:7], a[92:95], v[248:251]\n"
    "s_waitcnt lgkmcnt(0)\n"
    "ds_read_b128 v[0:3], v16 offset:19456\n"
    "v_exp_f32 v176, v176\n"
    "v_mfma_f32_16x16x32_bf16 v[212:215], v[8:11], a[96:99], 0\n"
    "v_exp_f32 v177, v177\n"
    "v_mfma_f32_16x16x32_bf16 v[212:215], v[12:15], a[100:103], v[212:215]\n"
    "s_add_u32 s94, s94, 190\n"
    "s_sub_u32 s95, s94, s92\n"
    "s_cmp_gt_i32 s95, 3\n"
    "s_cbranch_scc1 L_pc3_%=\n"
    "L_pr3_%=:\n"
    "ds_read_b128 v[4:7], v17 offset:19456\n"
    "v_mfma_f32_16x16x32_bf16 v[220:223], v[8:11], a[120:123], 0\n"
    "v_fma_f32 v179, v179, s62, v27\n"
    "v_cvt_pk_bf16_f32 v71, v174, v175\n"
    "v_mfma_f32_16x16x32_bf16 v[220:223], v[12:15], a[124:127], v[220:223]\n"
    "buffer_load_dwordx4 v22, s[40:43], s36 offen offset:1024 nt lds\n"
    "v_fma_f32 v180, v180, s62, v27\n"
    "v_mfma_f32_16x16x32_bf16 v[228:231], v[8:11], a[0:3], 0\n"
    "v_exp_f32 v178, v178\n"
    "v_mfma_f32_16x16x32_bf16 v[228:231], v[12:15], a[4:7], v[228:231]\n"
    "v_fma_f32 v181, v181, s62, v27\n"
    "v_exp_f32 v179, v179\n"
    "v_cvt_pk_bf16_f32 v72, v176, v177\n"
    "v_mfma_f32_16x16x32_bf16 v[236:239], v[8:11], a[24:27], 0\n"
    "v_fma_f32 v182, v182, s62, v27\n"
    "v_fma_f32 v183, v183, s62, v27\n"
    "v_mfma_f32_16x16x32_bf16 v[236:239], v[12:15], a[28:31], v[236:239]\n"
    "v_exp_f32 v180, v180\n"
    "v_exp_f32 v181, v181\n"
    "v_mfma_f32_16x16x32_bf16 v[244:247], v[8:11], a[48:51], 0\n"
    "v_exp_f32 v182, v182\n"
    "v_mfma_f32_16x16x32_bf16 v[244:247], v[12:15], a[52:55], v[244:247]\n"
    "v_cvt_pk_bf16_f32 v73, v178, v179\n"
    "v_fma_f32 v184, v184, s62, v28\n"
    "v_fma_f32 v185, v185, s62, v28\n"
    "v_mfma_f32_16x16x32_bf16 v[252:255], v[8:11], a[72:75], 0\n"
    "v_cvt_pk_bf16_f32 v74, v180, v181\n"
    "v_fma_f32 v186, v186, s62, v28\n"
    "v_mfma_f32_16x16x32_bf16 v[252:255], v[12:15], a[76:79], v[252:255]\n"
    "s_waitcnt lgkmcnt(0)\n"
    "ds_read_b128 v[8:11], v16 offset:20480\n"
    "v_exp_f32 v183, v183\n"
    "v_mfma_f32_16x16x32_bf16 v[212:215], v[0:3], a[104:107], v[212:215]\n"
    "v_exp_f32 v184, v184\n"
    "v_mfma_f32_16x16x32_bf16 v[212:215], v[4:7], a[108:111], v[212:215]\n"
    "ds_read_b128 v[12:15], v17 offset:20480\n"
    "v_fma_f32 v187, v187, s62, v28\n"
    "v_exp_f32 v185, v185\n"
    "v_mfma_f32_16x16x32_bf16 v[220:223], v[0:3], a[128:131], v[220:223]\n"
    "v_cvt_pk_bf16_f32 v75, v182, v183\n"
    "v_fma_f32 v188, v188, s62, v28\n"
    "v_mfma_f32_16x16x32_bf16 v[220:223], v[4:7], a[132:135], v[220:223]\n"
    "buffer_load_dwordx4 v23, s[40:43], s36 offen offset:2048 nt lds\n"
    "v_exp_f32 v186, v186\n"
    "v_mfma_f32_16x16x32_bf16 v[228:231], v[0:3], a[8:11], v[228:231]\n"
    "v_exp_f32 v187, v187\n"
    "v_mfma_f32_16x16x32_bf16 v[228:231], v[4:7], a[12:15], v[228:231]\n"
    "v_fma_f32 v189, v189, s62, v28\n"
    "v_cvt_pk_bf16_f32 v76, v184, v185\n"
    "v_fma_f32 v190, v190, s62, v28\n"
    "v_mfma_f32_16x16x32_bf16 v[236:239], v[0:3], a[32:35], v[236:239]\n"
    "v_fma_f32 v191, v191, s62, v28\n"
    "v_cvt_pk_bf16_f32 v77, v186, v187\n"
    "v_mfma_f32_16x16x32_bf16 v[236:239], v[4:7], a[36:39], v[236:239]\n"
    "v_exp_f32 v188, v188\n"
    "v_exp_f32 v189, v189\n"
    "v_mfma_f32_16x16x32_bf16 v[244:247], v[0:3], a[56:59], v[244:247]\n"
    "v_exp_f32 v190, v190\n"
    "v_mfma_f32_16x16x32_bf16 v[244:247], v[4:7], a[60:63], v[244:247]\n"
    "s_waitcnt vmcnt(29) lgkmcnt(0)\n"
    "s_barrier\n"
    "v_fma_f32 v192, v192, s62, v29\n"
    "v_fma_f32 v193, v193, s62, v29\n"
    "v_mfma_f32_16x16x32_bf16 v[252:255], v[0:3], a[80:83], v[252:255]\n"
    "v_cvt_pk_bf16_f32 v78, v188, v189\n"
    "v_fma_f32 v194, v194, s62, v29\n"
    "v_mfma_f32_16x16x32_bf16 v[252:255], v[4:7], a[84:87], v[252:255]\n"
    "ds_read_b128 v[0:3], v16 offset:24576\n"
    "s_add_u32 m0, s60, 12288\n"
    "v_exp_f32 v191, v191\n"
    "v_mfma_f32_16x16x32_bf16 v[212:215], v[8:11], a[112:115], v[212:215]\n"
    "v_exp_f32 v192, v192\n"
    "v_mfma_f32_16x16x32_bf16 v[212:215], v[12:15], a[116:119], v[212:215]\n"
    "ds_read_b128 v[4:7], v17 offset:24576\n"
    "s_add_u32 s36, s61, 36864\n"
    "v_fma_f32 v195, v195, s62, v29\n"
    "v_mfma_f32_16x16x32_bf16 v[220:223], v[8:11], a[136:139], v[220:223]\n"
    "v_cvt_pk_bf16_f32 v79, v190, v191\n"
    "v_fma_f32 v196, v196, s62, v29\n"
    "v_mfma_f32_16x16x32_bf16 v[220:223], v[12:15], a[140:143], v[220:223]\n"
    "buffer_load_dwordx4 v20, s[40:43], s36 offen nt lds\n"
    "v_exp_f32 v193, v193\n"
    "v_mfma_f32_16x16x32_bf16 v[228:231], v[8:11], a[16:19], v[228:231]\n"
    "v_exp_f32 v194, v194\n"
    "v_mfma_f32_16x16x32_bf16 v[228:231], v[12:15], a[20:23], v[228:231]\n"
    "v_fma_f32 v197, v197, s62, v29\n"
    "v_exp_f32 v195, v195\n"
    "v_cvt_pk_bf16_f32 v80, v192, v193\n"
    "v_mfma_f32_16x16x32_bf16 v[236:239], v[8:11], a[40:43], v[236:239]\n"
    "v_fma_f32 v198, v198, s62, v29\n"
    "v_fma_f32 v199, v199, s62, v29\n"
    "v_mfma_f32_16x16x32_bf16 v[236:239], v[12:15], a[44:47], v[236:239]\n"
    "v_exp_f32 v196, v196\n"
    "v_exp_f32 v197, v197\n"
    "v_mfma_f32_16x16x32_bf16 v[244:247], v[8:11], a[64:67], v[244:247]\n"
    "v_exp_f32 v198, v198\n"
    "v_mfma_f32_16x16x32_bf16 v[244:247], v[12:15], a[68:71], v[244:247]\n"
    "v_cvt_pk_bf16_f32 v81, v194, v195\n"
    "v_fma_f32 v200, v200, s62, v30\n"
    "v_fma_f32 v201, v201, s62, v30\n"
    "v_mfma_f32_16x16x32_bf16 v[252:255], v[8:11], a[88:91], v[252:255]\n"
    "v_cvt_pk_bf16_f32 v82, v196, v197\n"
    "v_fma_f32 v202, v202, s62, v30\n"
    "v_mfma_f32_16x16x32_bf16 v[252:255], v[12:15], a[92:95], v[252:255]\n"
    "s_waitcnt lgkmcnt(0)\n"
    "ds_read_b128 v[8:11], v16 offset:25600\n"
    "v_exp_f32 v199, v199\n"
    "v_mfma_f32_16x16x32_bf16 v[160:163], v[0:3], a[96:99], 0\n"
    "v_exp_f32 v200, v200\n"
    "v_mfma_f32_16x16x32_bf16 v[160:163], v[4:7], a[100:103], v[160:163]\n"
    "ds_read_b128 v[12:15], v17 offset:25600\n"
    "v_fma_f32 v203, v203, s62, v30\n"
    "v_exp_f32 v201, v201\n"
    "v_mfma_f32_16x16x32_bf16 v[168:171], v[0:3], a[120:123], 0\n"
    "v_cvt_pk_bf16_f32 v83, v198, v199\n"
    "v_fma_f32 v204, v204, s62, v30\n"
    "v_mfma_f32_16x16x32_bf16 v[168:171], v[4:7], a[124:127], v[168:171]\n"
    "buffer_load_dwordx4 v22, s[40:43], s36 offen offset:1024 nt lds\n"
    "v_exp_f32 v202, v202\n"
    "v_mfma_f32_16x16x32_bf16 v[176:179], v[0:3], a[0:3], 0\n"
    "v_exp_f32 v203, v203\n"
    "v_mfma_f32_16x16x32_bf16 v[176:179], v[4:7], a[4:7], v[176:179]\n"
    "v_fma_f32 v205, v205, s62, v30\n"
    "v_cvt_pk_bf16_f32 v84, v200, v201\n"
    "v_fma_f32 v206, v206, s62, v30\n"
    "v_mfma_f32_16x16x32_bf16 v[184:187], v[0:3], a[24:27], 0\n"
    "v_fma_f32 v207, v207, s62, v30\n"
    "v_cvt_pk_bf16_f32 v85, v202, v203\n"
    "v_mfma_f32_16x16x32_bf16 v[184:187], v[4:7], a[28:31], v[184:187]\n"
    "v_exp_f32 v204, v204\n"
    "v_exp_f32 v205, v205\n"
    "v_mfma_f32_16x16x32_bf16 v[192:195], v[0:3], a[48:51], 0\n"
    "v_exp_f32 v206, v206\n"
    "v_mfma_f32_16x16x32_bf16 v[192:195], v[4:7], a[52:55], v[192:195]\n"
    "v_exp_f32 v207, v207\n"
    "v_cvt_pk_bf16_f32 v86, v204, v205\n"
    "v_cvt_pk_bf16_f32 v87, v206, v207\n"
    "v_mfma_f32_16x16x32_bf16 v[200:203], v[0:3], a[72:75], 0\n"
    "v_fma_f32 v208, v208, s62, v25\n"
    "v_fma_f32 v209, v209, s62, v25\n"
    "v_mfma_f32_16x16x32_bf16 v[200:203], v[4:7], a[76:79], v[200:203]\n"
    "s_waitcnt lgkmcnt(0)\n"
    "ds_read_b128 v[0:3], v16 offset:26624\n"
    "v_fma_f32 v210, v210, s62, v25\n"
    "v_exp_f32 v208, v208\n"
    "v_mfma_f32_16x16x32_bf16 v[160:163], v[8:11], a[104:107], v[160:163]\n"
    "v_exp_f32 v209, v209\n"
    "v_mfma_f32_16x16x32_bf16 v[160:163], v[12:15], a[108:111], v[160:163]\n"
    "ds_read_b128 v[4:7], v17 offset:26624\n"
    "v_fma_f32 v211, v211, s62, v25\n"
    "v_fma_f32 v212, v212, s62, v25\n"
    "v_mfma_f32_16x16x32_bf16 v[168:171], v[8:11], a[128:131], v[168:171]\n"
    "v_fma_f32 v213, v213, s62, v25\n"
    "v_cvt_pk_bf16_f32 v88, v208, v209\n"
    "v_mfma_f32_16x16x32_bf16 v[168:171], v[12:15], a[132:135], v[168:171]\n"
    "buffer_load_dwordx4 v23, s[40:43], s36 offen offset:2048 nt lds\n"
    "v_mfma_f32_16x16x32_bf16 v[176:179], v[8:11], a[8:11], v[176:179]\n"
    "v_exp_f32 v210, v210\n"
    "v_mfma_f32_16x16x32_bf16 v[176:179], v[12:15], a[12:15], v[176:179]\n"
    "v_exp_f32 v211, v211\n"
    "v_fma_f32 v214, v214, s62, v25\n"
    "v_mfma_f32_16x16x32_bf16 v[184:187], v[8:11], a[32:35], v[184:187]\n"
    "v_fma_f32 v215, v215, s62, v25\n"
    "v_cvt_pk_bf16_f32 v89, v210, v211\n"
    "v_mfma_f32_16x16x32_bf16 v[184:187], v[12:15], a[36:39], v[184:187]\n"
    "v_exp_f32 v212, v212\n"
    "v_exp_f32 v213, v213\n"
    "v_mfma_f32_16x16x32_bf16 v[192:195], v[8:11], a[56:59], v[192:195]\n"
    "v_exp_f32 v214, v214\n"
    "v_mfma_f32_16x16x32_bf16 v[192:195], v[12:15], a[60:63], v[192:195]\n"
    "v_fma_f32 v216, v216, s62, v26\n"
    "v_fma_f32 v217, v217, s62, v26\n"
    "v_exp_f32 v215, v215\n"
    "v_mfma_f32_16x16x32_bf16 v[200:203], v[8:11], a[80:83], v[200:203]\n"
    "v_cvt_pk_bf16_f32 v90, v212, v213\n"
    "v_fma_f32 v218, v218, s62, v26\n"
    "v_mfma_f32_16x16x32_bf16 v[200:203], v[12:15], a[84:87], v[200:203]\n"
    "s_waitcnt lgkmcnt(0)\n"
    "ds_read_b128 v[8:11], v16 offset:30720\n"
    "s_add_u32 m0, s60, 61440\n"
    "v_exp_f32 v216, v216\n"
    "v_mfma_f32_16x16x32_bf16 v[160:163], v[0:3], a[112:115], v[160:163]\n"
    "v_exp_f32 v217, v217\n"
    "v_mfma_f32_16x16x32_bf16 v[160:163], v[4:7], a[116:119], v[160:163]\n"
    "ds_read_b128 v[12:15], v17 offset:30720\n"
    "s_add_u32 s36, s71, 110592\n"
    "v_fma_f32 v219, v219, s62, v26\n"
    "v_cvt_pk_bf16_f32 v91, v214, v215\n"
    "v_mfma_f32_16x16x32_bf16 v[168:171], v[0:3], a[136:139], v[168:171]\n"
    "v_fma_f32 v220, v220, s62, v26\n"
    "v_fma_f32 v221, v221, s62, v26\n"
    "v_mfma_f32_16x16x32_bf16 v[168:171], v[4:7], a[140:143], v[168:171]\n"
    "buffer_load_dwordx4 v20, s[48:51], s36 offen nt lds\n"
    "v_mfma_f32_16x16x32_bf16 v[176:179], v[0:3], a[16:19], v[176:179]\n"
    "v_exp_f32 v218, v218\n"
    "v_mfma_f32_16x16x32_bf16 v[176:179], v[4:7], a[20:23], v[176:179]\n"
    "v_exp_f32 v219, v219\n"
    "v_cvt_pk_bf16_f32 v92, v216, v217\n"
    "v_fma_f32 v222, v222, s62, v26\n"
    "v_mfma_f32_16x16x32_bf16 v[184:187], v[0:3], a[40:43], v[184:187]\n"
    "v_fma_f32 v223, v223, s62, v26\n"
    "v_cvt_pk_bf16_f32 v93, v218, v219\n"
    "v_mfma_f32_16x16x32_bf16 v[184:187], v[4:7], a[44:47], v[184:187]\n"
    "v_exp_f32 v220, v220\n"
    "v_exp_f32 v221, v221\n"
    "v_mfma_f32_16x16x32_bf16 v[192:195], v[0:3], a[64:67], v[192:195]\n"
    "v_exp_f32 v222, v222\n"
    "v_mfma_f32_16x16x32_bf16 v[192:195], v[4:7], a[68:71], v[192:195]\n"
    "v_fma_f32 v224, v224, s62, v27\n"
    "v_fma_f32 v225, v225, s62, v27\n"
    "v_exp_f32 v223, v223\n"
    "v_mfma_f32_16x16x32_bf16 v[200:203], v[0:3], a[88:91], v[200:203]\n"
    "v_cvt_pk_bf16_f32 v94, v220, v221\n"
    "v_fma_f32 v226, v226, s62, v27\n"
    "v_mfma_f32_16x16x32_bf16 v[200:203], v[4:7], a[92:95], v[200:203]\n"
    "s_waitcnt lgkmcnt(0)\n"
    "ds_read_b128 v[0:3], v16 offset:31744\n"
    "v_exp_f32 v224, v224\n"
    "v_mfma_f32_16x16x32_bf16 v[164:167], v[8:11], a[96:99], 0\n"
    "v_exp_f32 v225, v225\n"
    "v_mfma_f32_16x16x32_bf16 v[164:167], v[12:15], a[100:103], v[164:167]\n"
    "ds_read_b128 v[4:7], v17 offset:31744\n"
    "v_fma_f32 v227, v227, s62, v27\n"
    "v_cvt_pk_bf16_f32 v95, v222, v223\n"
    "v_mfma_f32_16x16x32_bf16 v[172:175], v[8:11], a[120:123], 0\n"
    "v_fma_f32 v228, v228, s62, v27\n"
    "v_fma_f32 v229, v229, s62, v27\n"
    "v_mfma_f32_16x16x32_bf16 v[172:175], v[12:15], a[124:127], v[172:175]\n"
    "buffer_load_dwordx4 v21, s[48:51], s36 offen offset:1024 nt lds\n"
    "v_mfma_f32_16x16x32_bf16 v[180:183], v[8:11], a[0:3], 0\n"
    "v_exp_f32 v226, v226\n"
    "v_mfma_f32_16x16x32_bf16 v[180:183], v[12:15], a[4:7], v[180:183]\n"
    "v_exp_f32 v227, v227\n"
    "v_cvt_pk_bf16_f32 v96, v224, v225\n"
    "v_fma_f32 v230, v230, s62, v27\n"
    "v_mfma_f32_16x16x32_bf16 v[188:191], v[8:11], a[24:27], 0\n"
    "v_fma_f32 v231, v231, s62, v27\n"
    "v_cvt_pk_bf16_f32 v97, v226, v227\n"
    "v_mfma_f32_16x16x32_bf16 v[188:191], v[12:15], a[28:31], v[188:191]\n"
    "v_exp_f32 v228, v228\n"
    "v_exp_f32 v229, v229\n"
    "v_mfma_f32_16x16x32_bf16 v[196:199], v[8:11], a[48:51], 0\n"
    "v_exp_f32 v230, v230\n"
    "v_mfma_f32_16x16x32_bf16 v[196:199], v[12:15], a[52:55], v[196:199]\n"
    "v_fma_f32 v232, v232, s62, v28\n"
    "v_fma_f32 v233, v233, s62, v28\n"
    "v_exp_f32 v231, v231\n"
    "v_mfma_f32_16x16x32_bf16 v[204:207], v[8:11], a[72:75], 0\n"
    "v_cvt_pk_bf16_f32 v98, v228, v229\n"
    "v_fma_f32 v234, v234, s62, v28\n"
    "v_mfma_f32_16x16x32_bf16 v[204:207], v[12:15], a[76:79], v[204:207]\n"
    "s_waitcnt lgkmcnt(0)\n"
    "ds_read_b128 v[8:11], v16 offset:32768\n"
    "v_exp_f32 v232, v232\n"
    "v_mfma_f32_16x16x32_bf16 v[164:167], v[0:3], a[104:107], v[164:167]\n"
    "v_exp_f32 v233, v233\n"
    "v_mfma_f32_16x16x32_bf16 v[164:167], v[4:7], a[108:111], v[164:167]\n"
    "ds_read_b128 v[12:15], v17 offset:32768\n"
    "v_fma_f32 v235, v235, s62, v28\n"
    "v_cvt_pk_bf16_f32 v99, v230, v231\n"
    "v_fma_f32 v236, v236, s62, v28\n"
    "v_mfma_f32_16x16x32_bf16 v[172:175], v[0:3], a[128:131], v[172:175]\n"
    "v_fma_f32 v237, v237, s62, v28\n"
    "v_cvt_pk_bf16_f32 v100, v232, v233\n"
    "v_mfma_f32_16x16x32_bf16 v[172:175], v[4:7], a[132:135], v[172:175]\n"
    "buffer_load_dwordx4 v31, s[48:51], s36 offen offset:2048 nt lds\n"
    "v_mfma_f32_16x16x32_bf16 v[180:183], v[0:3], a[8:11], v[180:183]\n"
    "v_exp_f32 v234, v234\n"
    "v_mfma_f32_16x16x32_bf16 v[180:183], v[4:7], a[12:15], v[180:183]\n"
    "v_exp_f32 v235, v235\n"
    "v_fma_f32 v238, v238, s62, v28\n"
    "v_mfma_f32_16x16x32_bf16 v[188:191], v[0:3], a[32:35], v[188:191]\n"
    "v_fma_f32 v239, v239, s62, v28\n"
    "v_cvt_pk_bf16_f32 v101, v234, v235\n"
    "v_mfma_f32_16x16x32_bf16 v[188:191], v[4:7], a[36:39], v[188:191]\n"
    "v_exp_f32 v236, v236\n"
    "v_exp_f32 v237, v237\n"
    "v_mfma_f32_16x16x32_bf16 v[196:199], v[0:3], a[56:59], v[196:199]\n"
    "v_exp_f32 v238, v238\n"
    "v_mfma_f32_16x16x32_bf16 v[196:199], v[4:7], a[60:63], v[196:199]\n"
    "s_waitcnt vmcnt(25) lgkmcnt(0)\n"
    "s_barrier\n"
    "v_fma_f32 v240, v240, s62, v29\n"
    "v_fma_f32 v241, v241, s62, v29\n"
    "v_mfma_f32_16x16x32_bf16 v[204:207], v[0:3], a[80:83], v[204:207]\n"
    "v_cvt_pk_bf16_f32 v102, v236, v237\n"
    "v_fma_f32 v242, v242, s62, v29\n"
    "v_mfma_f32_16x16x32_bf16 v[204:207], v[4:7], a[84:87], v[204:207]\n"
    "ds_read_b128 v[0:3], v16 offset:36864\n"
    "s_add_u32 m0, s60, 73728\n"
    "v_exp_f32 v239, v239\n"
    "v_mfma_f32_16x16x32_bf16 v[164:167], v[8:11], a[112:115], v[164:167]\n"
    "v_exp_f32 v240, v240\n"
    "v_mfma_f32_16x16x32_bf16 v[164:167], v[12:15], a[116:119], v[164:167]\n"
    "ds_read_b128 v[4:7], v17 offset:36864\n"
    "s_add_u32 s36, s61, 73728\n"
    "v_fma_f32 v243, v243, s62, v29\n"
    "v_mfma_f32_16x16x32_bf16 v[172:175], v[8:11], a[136:139], v[172:175]\n"
    "v_cvt_pk_bf16_f32 v103, v238, v239\n"
    "v_fma_f32 v244, v244, s62, v29\n"
    "v_mfma_f32_16x16x32_bf16 v[172:175], v[12:15], a[140:143], v[172:175]\n"
    "buffer_load_dwordx4 v20, s[40:43], s36 offen nt lds\n"
    "v_exp_f32 v241, v241\n"
    "v_mfma_f32_16x16x32_bf16 v[180:183], v[8:11], a[16:19], v[180:183]\n"
    "v_exp_f32 v242, v242\n"
    "v_mfma_f32_16x16x32_bf16 v[180:183], v[12:15], a[20:23], v[180:183]\n"
    "v_fma_f32 v245, v245, s62, v29\n"
    "v_exp_f32 v243, v243\n"
    "v_cvt_pk_bf16_f32 v104, v240, v241\n"
    "v_mfma_f32_16x16x32_bf16 v[188:191], v[8:11], a[40:43], v[188:191]\n"
    "v_fma_f32 v246, v246, s62, v29\n"
    "v_fma_f32 v247, v247, s62, v29\n"
    "v_mfma_f32_16x16x32_bf16 v[188:191], v[12:15], a[44:47], v[188:191]\n"
    "v_exp_f32 v244, v244\n"
    "v_exp_f32 v245, v245\n"
    "v_mfma_f32_16x16x32_bf16 v[196:199], v[8:11], a[64:67], v[196:199]\n"
    "v_exp_f32 v246, v246\n"
    "v_mfma_f32_16x16x32_bf16 v[196:199], v[12:15], a[68:71], v[196:199]\n"
    "v_cvt_pk_bf16_f32 v105, v242, v243\n"
    "v_fma_f32 v248, v248, s62, v30\n"
    "v_fma_f32 v249, v249, s62, v30\n"
    "v_mfma_f32_16x16x32_bf16 v[204:207], v[8:11], a[88:91], v[204:207]\n"
    "v_cvt_pk_bf16_f32 v106, v244, v245\n"
    "v_fma_f32 v250, v250, s62, v30\n"
    "v_mfma_f32_16x16x32_bf16 v[204:207], v[12:15], a[92:95], v[204:207]\n"
    "s_waitcnt lgkmcnt(0)\n"
    "ds_read_b128 v[8:11], v16 offset:37888\n"
    "v_exp_f32 v247, v247\n"
    "v_mfma_f32_16x16x32_bf16 v[208:211], v[0:3], a[96:99], 0\n"
    "v_exp_f32 v248, v248\n"
    "v_mfma_f32_16x16x32_bf16 v[208:211], v[4:7], a[100:103], v[208:211]\n"
    "ds_read_b128 v[12:15], v17 offset:37888\n"
    "v_fma_f32 v251, v251, s62, v30\n"
    "v_exp_f32 v249, v249\n"
    "v_mfma_f32_16x16x32_bf16 v[216:219], v[0:3], a[120:123], 0\n"
    "v_cvt_pk_bf16_f32 v107, v246, v247\n"
    "v_fma_f32 v252, v252, s62, v30\n"
    "v_mfma_f32_16x16x32_bf16 v[216:219], v[4:7], a[124:127], v[216:219]\n"
    "buffer_load_dwordx4 v22, s[40:43], s36 offen offset:1024 nt lds\n"
    "v_exp_f32 v250, v250\n"
    "v_mfma_f32_16x16x32_bf16 v[224:227], v[0:3], a[0:3], 0\n"
    "v_exp_f32 v251, v251\n"
    "v_mfma_f32_16x16x32_bf16 v[224:227], v[4:7], a[4:7], v[224:227]\n"
    "v_fma_f32 v253, v253, s62, v30\n"
    "v_cvt_pk_bf16_f32 v108, v248, v249\n"
    "v_fma_f32 v254, v254, s62, v30\n"
    "v_mfma_f32_16x16x32_bf16 v[232:235], v[0:3], a[24:27], 0\n"
    "v_fma_f32 v255, v255, s62, v30\n"
    "v_cvt_pk_bf16_f32 v109, v250, v251\n"
    "v_mfma_f32_16x16x32_bf16 v[232:235], v[4:7], a[28:31], v[232:235]\n"
    "v_exp_f32 v252, v252\n"
    "v_exp_f32 v253, v253\n"
    "v_mfma_f32_16x16x32_bf16 v[240:243], v[0:3], a[48:51], 0\n"
    "v_exp_f32 v254, v254\n"
    "v_mfma_f32_16x16x32_bf16 v[240:243], v[4:7], a[52:55], v[240:243]\n"
    "v_exp_f32 v255, v255\n"
    "v_cvt_pk_bf16_f32 v110, v252, v253\n"
    "v_cvt_pk_bf16_f32 v111, v254, v255\n"
    "v_mfma_f32_16x16x32_bf16 v[248:251], v[0:3], a[72:75], 0\n"
    "v_fma_f32 v160, v160, s62, v25\n"
    "v_fma_f32 v161, v161, s62, v25\n"
    "v_mfma_f32_16x16x32_bf16 v[248:251], v[4:7], a[76:79], v[248:251]\n"
    "s_waitcnt lgkmcnt(0)\n"
    "ds_read_b128 v[0:3], v16 offset:38912\n"
    "v_fma_f32 v162, v162, s62, v25\n"
    "v_exp_f32 v160, v160\n"
    "v_mfma_f32_16x16x32_bf16 v[208:211], v[8:11], a[104:107], v[208:211]\n"
    "v_exp_f32 v161, v161\n"
    "v_mfma_f32_16x16x32_bf16 v[208:211], v[12:15], a[108:111], v[208:211]\n"
    "ds_read_b128 v[4:7], v17 offset:38912\n"
    "v_fma_f32 v163, v163, s62, v25\n"
    "v_fma_f32 v164, v164, s62, v25\n"
    "v_mfma_f32_16x16x32_bf16 v[216:219], v[8:11], a[128:131], v[216:219]\n"
    "v_fma_f32 v165, v165, s62, v25\n"
    "v_cvt_pk_bf16_f32 v112, v160, v161\n"
    "v_mfma_f32_16x16x32_bf16 v[216:219], v[12:15], a[132:135], v[216:219]\n"
    "buffer_load_dwordx4 v23, s[40:43], s36 offen offset:2048 nt lds\n"
    "v_mfma_f32_16x16x32_bf16 v[224:227], v[8:11], a[8:11], v[224:227]\n"
    "v_exp_f32 v162, v162\n"
    "v_mfma_f32_16x16x32_bf16 v[224:227], v[12:15], a[12:15], v[224:227]\n"
    "v_exp_f32 v163, v163\n"
    "v_fma_f32 v166, v166, s62, v25\n"
    "v_mfma_f32_16x16x32_bf16 v[232:235], v[8:11], a[32:35], v[232:235]\n"
    "v_fma_f32 v167, v167, s62, v25\n"
    "v_cvt_pk_bf16_f32 v113, v162, v163\n"
    "v_mfma_f32_16x16x32_bf16 v[232:235], v[12:15], a[36:39], v[232:235]\n"
    "v_exp_f32 v164, v164\n"
    "v_exp_f32 v165, v165\n"
    "v_mfma_f32_16x16x32_bf16 v[240:243], v[8:11], a[56:59], v[240:243]\n"
    "v_exp_f32 v166, v166\n"
    "v_mfma_f32_16x16x32_bf16 v[240:243], v[12:15], a[60:63], v[240:243]\n"
    "v_fma_f32 v168, v168, s62, v26\n"
    "v_fma_f32 v169, v169, s62, v26\n"
    "v_exp_f32 v167, v167\n"
    "v_mfma_f32_16x16x32_bf16 v[248:251], v[8:11], a[80:83], v[248:251]\n"
    "v_cvt_pk_bf16_f32 v114, v164, v165\n"
    "v_fma_f32 v170, v170, s62, v26\n"
    "v_mfma_f32_16x16x32_bf16 v[248:251], v[12:15], a[84:87], v[248:251]\n"
    "s_waitcnt lgkmcnt(0)\n"
    "ds_read_b128 v[8:11], v16 offset:43008\n"
    "s_add_u32 m0, s60, 86016\n"
    "v_exp_f32 v168, v168\n"
    "v_mfma_f32_16x16x32_bf16 v[208:211], v[0:3], a[112:115], v[208:211]\n"
    "v_exp_f32 v169, v169\n"
    "v_mfma_f32_16x16x32_bf16 v[208:211], v[4:7], a[116:119], v[208:211]\n"
    "ds_read_b128 v[12:15], v17 offset:43008\n"
    "s_add_u32 s36, s61, 110592\n"
    "v_fma_f32 v171, v171, s62, v26\n"
    "v_cvt_pk_bf16_f32 v115, v166, v167\n"
    "v_mfma_f32_16x16x32_bf16 v[216:219], v[0:3], a[136:139], v[216:219]\n"
    "v_fma_f32 v172, v172, s62, v26\n"
    "v_fma_f32 v173, v173, s62, v26\n"
    "v_mfma_f32_16x16x32_bf16 v[216:219], v[4:7], a[140:143], v[216:219]\n"
    "buffer_load_dwordx4 v20, s[40:43], s36 offen nt lds\n"
    "v_mfma_f32_16x16x32_bf16 v[224:227], v[0:3], a[16:19], v[224:227]\n"
    "v_exp_f32 v170, v170\n"
    "v_mfma_f32_16x16x32_bf16 v[224:227], v[4:7], a[20:23], v[224:227]\n"
    "v_exp_f32 v171, v171\n"
    "v_cvt_pk_bf16_f32 v116, v168, v169\n"
    "v_mfma_f32_16x16x32_bf16 v[232:235], v[0:3], a[40:43], v[232:235]\n"
    "v_fma_f32 v174, v174, s62, v26\n"
    "v_fma_f32 v175, v175, s62, v26\n"
    "v_mfma_f32_16x16x32_bf16 v[232:235], v[4:7], a[44:47], v[232:235]\n"
    "s_memrealtime s[92:93]\n"
    "v_exp_f32 v172, v172\n"
    "v_mfma_f32_16x16x32_bf16 v[240:243], v[0:3], a[64:67], v[240:243]\n"
    "v_exp_f32 v173, v173\n"
    "v_mfma_f32_16x16x32_bf16 v[240:243], v[4:7], a[68:71], v[240:243]\n"
    "v_cvt_pk_bf16_f32 v117, v170, v171\n"
    "v_fma_f32 v176, v176, s62, v27\n"
    "v_exp_f32 v174, v174\n"
    "v_mfma_f32_16x16x32_bf16 v[248:251], v[0:3], a[88:91], v[248:251]\n"
    "v_fma_f32 v177, v177, s62, v27\n"
    "v_cvt_pk_bf16_f32 v118, v172, v173\n"
    "v_mfma_f32_16x16x32_bf16 v[248:251], v[4:7], a[92:95], v[248:251]\n"
    "s_waitcnt lgkmcnt(0)\n"
    "ds_read_b128 v[0:3], v16 offset:44032\n"
    "v_exp_f32 v175, v175\n"
    "v_mfma_f32_16x16x32_bf16 v[212:215], v[8:11], a[96:99], 0\n"
    "v_exp_f32 v176, v176\n"
    "v_mfma_f32_16x16x32_bf16 v[212:215], v[12:15], a[100:103], v[212:215]\n"
    "s_add_u32 s94, s94, 190\n"
    "s_sub_u32 s95, s94, s92\n"
    "s_cmp_gt_i32 s95, 3\n"
    "s_cbranch_scc1 L_pc4_%=\n"
    "L_pr4_%=:\n"
    "ds_read_b128 v[4:7], v17 offset:44032\n"
    "v_mfma_f32_16x16x32_bf16 v[220:223], v[8:11], a[120:123], 0\n"
    "v_fma_f32 v178, v178, s62, v27\n"
    "v_fma_f32 v179, v179, s62, v27\n"
    "v_mfma_f32_16x16x32_bf16 v[220:223], v[12:15], a[124:127], v[220:223]\n"
    "v_exp_f32 v177, v177\n"
    "v_cvt_pk_bf16_f32 v119, v174, v175\n"
    "v_fma_f32 v180, v180, s62, v27\n"
    "v_mfma_f32_16x16x32_bf16 v[228:231], v[8:11], a[0:3], 0\n"
    "v_exp_f32 v178, v178\n"
    "v_mfma_f32_16x16x32_bf16 v[228:231], v[12:15], a[4:7], v[228:231]\n"
    "v_fma_f32 v181, v181, s62, v27\n"
    "v_exp_f32 v179, v179\n"
    "v_cvt_pk_bf16_f32 v120, v176, v177\n"
    "v_mfma_f32_16x16x32_bf16 v[236:239], v[8:11], a[24:27], 0\n"
    "v_fma_f32 v182, v182, s62, v27\n"
    "v_fma_f32 v183, v183, s62, v27\n"
    "v_mfma_f32_16x16x32_bf16 v[236:239], v[12:15], a[28:31], v[236:239]\n"
    "buffer_load_dwordx4 v22, s[40:43], s36 offen offset:1024 nt lds\n"
    "v_exp_f32 v180, v180\n"
    "v_mfma_f32_16x16x32_bf16 v[244:247], v[8:11], a[48:51], 0\n"
    "v_exp_f32 v181, v181\n"
    "v_mfma_f32_16x16x32_bf16 v[244:247], v[12:15], a[52:55], v[244:247]\n"
    "v_cvt_pk_bf16_f32 v121, v178, v179\n"
    "v_fma_f32 v184, v184, s62, v28\n"
    "v_exp_f32 v182, v182\n"
    "v_mfma_f32_16x16x32_bf16 v[252:255], v[8:11], a[72:75], 0\n"
    "v_fma_f32 v185, v185, s62, v28\n"
    "v_cvt_pk_bf16_f32 v122, v180, v181\n"
    "v_mfma_f32_16x16x32_bf16 v[252:255], v[12:15], a[76:79], v[252:255]\n"
    "s_waitcnt lgkmcnt(0)\n"
    "ds_read_b128 v[8:11], v16 offset:45056\n"
    "v_exp_f32 v183, v183\n"
    "v_mfma_f32_16x16x32_bf16 v[212:215], v[0:3], a[104:107], v[212:215]\n"
    "v_exp_f32 v184, v184\n"
    "v_mfma_f32_16x16x32_bf16 v[212:215], v[4:7], a[108:111], v[212:215]\n"
    "ds_read_b128 v[12:15], v17 offset:45056\n"
    "v_fma_f32 v186, v186, s62, v28\n"
    "v_fma_f32 v187, v187, s62, v28\n"
    "v_mfma_f32_16x16x32_bf16 v[220:223], v[0:3], a[128:131], v[220:223]\n"
    "v_cvt_pk_bf16_f32 v123, v182, v183\n"
    "v_fma_f32 v188, v188, s62, v28\n"
    "v_mfma_f32_16x16x32_bf16 v[220:223], v[4:7], a[132:135], v[220:223]\n"
    "v_exp_f32 v185, v185\n"
    "v_exp_f32 v186, v186\n"
    "v_mfma_f32_16x16x32_bf16 v[228:231], v[0:3], a[8:11], v[228:231]\n"
    "v_exp_f32 v187, v187\n"
    "v_mfma_f32_16x16x32_bf16 v[228:231], v[4:7], a[12:15], v[228:231]\n"
    "v_fma_f32 v189, v189, s62, v28\n"
    "v_cvt_pk_bf16_f32 v124, v184, v185\n"
    "v_fma_f32 v190, v190, s62, v28\n"
    "v_mfma_f32_16x16x32_bf16 v[236:239], v[0:3], a[32:35], v[236:239]\n"
    "v_fma_f32 v191, v191, s62, v28\n"
    "v_cvt_pk_bf16_f32 v125, v186, v187\n"
    "v_mfma_f32_16x16x32_bf16 v[236:239], v[4:7], a[36:39], v[236:239]\n"
    "v_exp_f32 v188, v188\n"
    "v_exp_f32 v189, v189\n"
    "v_mfma_f32_16x16x32_bf16 v[244:247], v[0:3], a[56:59], v[244:247]\n"
    "v_exp_f32 v190, v190\n"
    "v_mfma_f32_16x16x32_bf16 v[244:247], v[4:7], a[60:63], v[244:247]\n"
    "v_fma_f32 v192, v192, s62, v29\n"
    "v_fma_f32 v193, v193, s62, v29\n"
    "v_exp_f32 v191, v191\n"
    "v_mfma_f32_16x16x32_bf16 v[252:255], v[0:3], a[80:83], v[252:255]\n"
    "v_cvt_pk_bf16_f32 v126, v188, v189\n"
    "v_fma_f32 v194, v194, s62, v29\n"
    "v_mfma_f32_16x16x32_bf16 v[252:255], v[4:7], a[84:87], v[252:255]\n"
    "s_waitcnt lgkmcnt(0)\n"
    "ds_read_b128 v[0:3], v16 offset:49152\n"
    "v_exp_f32 v192, v192\n"
    "v_mfma_f32_16x16x32_bf16 v[212:215], v[8:11], a[112:115], v[212:215]\n"
    "v_exp_f32 v193, v193\n"
    "v_mfma_f32_16x16x32_bf16 v[212:215], v[12:15], a[116:119], v[212:215]\n"
    "ds_read_b128 v[4:7], v17 offset:49152\n"
    "v_fma_f32 v195, v195, s62, v29\n"
    "v_cvt_pk_bf16_f32 v127, v190, v191\n"
    "v_fma_f32 v196, v196, s62, v29\n"
    "v_mfma_f32_16x16x32_bf16 v[220:223], v[8:11], a[136:139], v[220:223]\n"
    "v_fma_f32 v197, v197, s62, v29\n"
    "v_cvt_pk_bf16_f32 v128, v192, v193\n"
    "v_mfma_f32_16x16x32_bf16 v[220:223], v[12:15], a[140:143], v[220:223]\n"
    "buffer_load_dwordx4 v23, s[40:43], s36 offen offset:2048 nt lds\n"
    "v_mfma_f32_16x16x32_bf16 v[228:231], v[8:11], a[16:19], v[228:231]\n"
    "v_exp_f32 v194, v194\n"
    "v_mfma_f32_16x16x32_bf16 v[228:231], v[12:15], a[20:23], v[228:231]\n"
    "v_exp_f32 v195, v195\n"
    "v_fma_f32 v198, v198, s62, v29\n"
    "v_mfma_f32_16x16x32_bf16 v[236:239], v[8:11], a[40:43], v[236:239]\n"
    "v_fma_f32 v199, v199, s62, v29\n"
    "v_cvt_pk_bf16_f32 v129, v194, v195\n"
    "v_mfma_f32_16x16x32_bf16 v[236:239], v[12:15], a[44:47], v[236:239]\n"
    "v_exp_f32 v196, v196\n"
    "v_exp_f32 v197, v197\n"
    "v_mfma_f32_16x16x32_bf16 v[244:247], v[8:11], a[64:67], v[244:247]\n"
    "v_exp_f32 v198, v198\n"
    "v_mfma_f32_16x16x32_bf16 v[244:247], v[12:15], a[68:71], v[244:247]\n"
    "v_fma_f32 v200, v200, s62, v30\n"
    "v_fma_f32 v201, v201, s62, v30\n"
    "v_exp_f32 v199, v199\n"
    "v_mfma_f32_16x16x32_bf16 v[252:255], v[8:11], a[88:91], v[252:255]\n"
    "v_cvt_pk_bf16_f32 v130, v196, v197\n"
    "v_fma_f32 v202, v202, s62, v30\n"
    "v_mfma_f32_16x16x32_bf16 v[252:255], v[12:15], a[92:95], v[252:255]\n"
    "s_waitcnt lgkmcnt(0)\n"
    "ds_read_b128 v[8:11], v16 offset:50176\n"
    "v_exp_f32 v200, v200\n"
    "v_fma_f32 v203, v203, s62, v30\n"
    "v_mfma_f32_16x16x32_bf16 v[160:163], v[0:3], a[96:99], 0\n"
    "v_exp_f32 v201, v201\n"
    "v_mfma_f32_16x16x32_bf16 v[160:163], v[4:7], a[100:103], v[160:163]\n"
    "ds_read_b128 v[12:15], v17 offset:50176\n"
    "s_add_u32 m0, s60, 147456\n"
    "v_cvt_pk_bf16_f32 v131, v198, v199\n"
    "v_fma_f32 v204, v204, s62, v30\n"
    "v_mfma_f32_16x16x32_bf16 v[168:171], v[0:3], a[120:123], 0\n"
    "v_fma_f32 v205, v205, s62, v30\n"
    "v_cvt_pk_bf16_f32 v132, v200, v201\n"
    "v_mfma_f32_16x16x32_bf16 v[168:171], v[4:7], a[124:127], v[168:171]\n"
    "s_add_u32 s36, s61, 0\n"
    "v_exp_f32 v202, v202\n"
    "v_mfma_f32_16x16x32_bf16 v[176:179], v[0:3], a[0:3], 0\n"
    "v_exp_f32 v203, v203\n"
    "v_mfma_f32_16x16x32_bf16 v[176:179], v[4:7], a[4:7], v[176:179]\n"
    "buffer_load_dwordx4 v20, s[44:47], s36 offen nt lds\n"
    "v_fma_f32 v206, v206, s62, v30\n"
    "v_mfma_f32_16x16x32_bf16 v[184:187], v[0:3], a[24:27], 0\n"
    "v_fma_f32 v207, v207, s62, v30\n"
    "v_cvt_pk_bf16_f32 v133, v202, v203\n"
    "v_mfma_f32_16x16x32_bf16 v[184:187], v[4:7], a[28:31], v[184:187]\n"
    "v_exp_f32 v204, v204\n"
    "v_exp_f32 v205, v205\n"
    "v_mfma_f32_16x16x32_bf16 v[192:195], v[0:3], a[48:51], 0\n"
    "v_exp_f32 v206, v206\n"
    "v_mfma_f32_16x16x32_bf16 v[192:195], v[4:7], a[52:55], v[192:195]\n"
    "v_exp_f32 v207, v207\n"
    "v_cvt_pk_bf16_f32 v134, v204, v205\n"
    "v_cvt_pk_bf16_f32 v135, v206, v207\n"
    "v_mfma_f32_16x16x32_bf16 v[200:203], v[0:3], a[72:75], 0\n"
    "v_fma_f32 v208, v208, s62, v25\n"
    "v_fma_f32 v209, v209, s62, v25\n"
    "v_mfma_f32_16x16x32_bf16 v[200:203], v[4:7], a[76:79], v[200:203]\n"
    "s_waitcnt lgkmcnt(0)\n"
    "ds_read_b128 v[0:3], v16 offset:51200\n"
    "v_fma_f32 v210, v210, s62, v25\n"
    "v_exp_f32 v208, v208\n"
    "v_mfma_f32_16x16x32_bf16 v[160:163], v[8:11], a[104:107], v[160:163]\n"
    "v_exp_f32 v209, v209\n"
    "v_mfma_f32_16x16x32_bf16 v[160:163], v[12:15], a[108:111], v[160:163]\n"
    "ds_read_b128 v[4:7], v17 offset:51200\n"
    "v_fma_f32 v211, v211, s62, v25\n"
    "v_fma_f32 v212, v212, s62, v25\n"
    "v_mfma_f32_16x16x32_bf16 v[168:171], v[8:11], a[128:131], v[168:171]\n"
    "v_fma_f32 v213, v213, s62, v25\n"
    "v_cvt_pk_bf16_f32 v136, v208, v209\n"
    "v_mfma_f32_16x16x32_bf16 v[168:171], v[12:15], a[132:135], v[168:171]\n"
    "v_exp_f32 v210, v210\n"
    "v_exp_f32 v211, v211\n"
    "v_mfma_f32_16x16x32_bf16 v[176:179], v[8:11], a[8:11], v[176:179]\n"
    "v_exp_f32 v212, v212\n"
    "v_mfma_f32_16x16x32_bf16 v[176:179], v[12:15], a[12:15], v[176:179]\n"
    "v_fma_f32 v214, v214, s62, v25\n"
    "v_fma_f32 v215, v215, s62, v25\n"
    "v_exp_f32 v213, v213\n"
    "v_mfma_f32_16x16x32_bf16 v[184:187], v[8:11], a[32:35], v[184:187]\n"
    "v_cvt_pk_bf16_f32 v137, v210, v211\n"
    "v_fma_f32 v216, v216, s62, v26\n"
    "v_mfma_f32_16x16x32_bf16 v[184:187], v[12:15], a[36:39], v[184:187]\n"
    "v_exp_f32 v214, v214\n"
    "v_fma_f32 v217, v217, s62, v26\n"
    "v_mfma_f32_16x16x32_bf16 v[192:195], v[8:11], a[56:59], v[192:195]\n"
    "v_exp_f32 v215, v215\n"
    "v_mfma_f32_16x16x32_bf16 v[192:195], v[12:15], a[60:63], v[192:195]\n"
    "v_cvt_pk_bf16_f32 v138, v212, v213\n"
    "v_fma_f32 v218, v218, s62, v26\n"
    "v_exp_f32 v216, v216\n"
    "v_mfma_f32_16x16x32_bf16 v[200:203], v[8:11], a[80:83], v[200:203]\n"
    "v_fma_f32 v219, v219, s62, v26\n"
    "v_cvt_pk_bf16_f32 v139, v214, v215\n"
    "v_mfma_f32_16x16x32_bf16 v[200:203], v[12:15], a[84:87], v[200:203]\n"
    "s_waitcnt lgkmcnt(0)\n"
    "ds_read_b128 v[8:11], v16 offset:55296\n"
    "v_exp_f32 v217, v217\n"
    "v_mfma_f32_16x16x32_bf16 v[160:163], v[0:3], a[112:115], v[160:163]\n"
    "v_exp_f32 v218, v218\n"
    "v_mfma_f32_16x16x32_bf16 v[160:163], v[4:7], a[116:119], v[160:163]\n"
    "ds_read_b128 v[12:15], v17 offset:55296\n"
    "v_fma_f32 v220, v220, s62, v26\n"
    "v_fma_f32 v221, v221, s62, v26\n"
    "v_mfma_f32_16x16x32_bf16 v[168:171], v[0:3], a[136:139], v[168:171]\n"
    "v_cvt_pk_bf16_f32 v140, v216, v217\n"
    "v_fma_f32 v222, v222, s62, v26\n"
    "v_mfma_f32_16x16x32_bf16 v[168:171], v[4:7], a[140:143], v[168:171]\n"
    "buffer_load_dwordx4 v22, s[44:47], s36 offen offset:1024 nt lds\n"
    "v_mfma_f32_16x16x32_bf16 v[176:179], v[0:3], a[16:19], v[176:179]\n"
    "v_exp_f32 v219, v219\n"
    "v_mfma_f32_16x16x32_bf16 v[176:179], v[4:7], a[20:23], v[176:179]\n"
    "v_exp_f32 v220, v220\n"
    "v_fma_f32 v223, v223, s62, v26\n"
    "v_mfma_f32_16x16x32_bf16 v[184:187], v[0:3], a[40:43], v[184:187]\n"
    "v_cvt_pk_bf16_f32 v141, v218, v219\n"
    "v_fma_f32 v224, v224, s62, v27\n"
    "v_mfma_f32_16x16x32_bf16 v[184:187], v[4:7], a[44:47], v[184:187]\n"
    "v_exp_f32 v221, v221\n"
    "v_exp_f32 v222, v222\n"
    "v_mfma_f32_16x16x32_bf16 v[192:195], v[0:3], a[64:67], v[192:195]\n"
    "v_exp_f32 v223, v223\n"
    "v_mfma_f32_16x16x32_bf16 v[192:195], v[4:7], a[68:71], v[192:195]\n"
    "v_fma_f32 v225, v225, s62, v27\n"
    "v_cvt_pk_bf16_f32 v142, v220, v221\n"
    "v_fma_f32 v226, v226, s62, v27\n"
    "v_mfma_f32_16x16x32_bf16 v[200:203], v[0:3], a[88:91], v[200:203]\n"
    "v_fma_f32 v227, v227, s62, v27\n"
    "v_cvt_pk_bf16_f32 v143, v222, v223\n"
    "v_mfma_f32_16x16x32_bf16 v[200:203], v[4:7], a[92:95], v[200:203]\n"
    "s_waitcnt lgkmcnt(0)\n"
    "ds_read_b128 v[0:3], v16 offset:56320\n"
    "v_exp_f32 v224, v224\n"
    "v_mfma_f32_16x16x32_bf16 v[164:167], v[8:11], a[96:99], 0\n"
    "v_exp_f32 v225, v225\n"
    "v_mfma_f32_16x16x32_bf16 v[164:167], v[12:15], a[100:103], v[164:167]\n"
    "ds_read_b128 v[4:7], v17 offset:56320\n"
    "v_fma_f32 v228, v228, s62, v27\n"
    "v_exp_f32 v226, v226\n"
    "v_mfma_f32_16x16x32_bf16 v[172:175], v[8:11], a[120:123], 0\n"
    "v_fma_f32 v229, v229, s62, v27\n"
    "v_cvt_pk_bf16_f32 v144, v224, v225\n"
    "v_mfma_f32_16x16x32_bf16 v[172:175], v[12:15], a[124:127], v[172:175]\n"
    "v_exp_f32 v227, v227\n"
    "v_fma_f32 v230, v230, s62, v27\n"
    "v_mfma_f32_16x16x32_bf16 v[180:183], v[8:11], a[0:3], 0\n"
    "v_exp_f32 v228, v228\n"
    "v_mfma_f32_16x16x32_bf16 v[180:183], v[12:15], a[4:7], v[180:183]\n"
    "buffer_load_dwordx4 v23, s[44:47], s36 offen offset:2048 nt lds\n"
    "v_fma_f32 v231, v231, s62, v27\n"
    "v_mfma_f32_16x16x32_bf16 v[188:191], v[8:11], a[24:27], 0\n"
    "v_cvt_pk_bf16_f32 v145, v226, v227\n"
    "v_fma_f32 v232, v232, s62, v28\n"
    "v_mfma_f32_16x16x32_bf16 v[188:191], v[12:15], a[28:31], v[188:191]\n"
    "v_exp_f32 v229, v229\n"
    "v_exp_f32 v230, v230\n"
    "v_mfma_f32_16x16x32_bf16 v[196:199], v[8:11], a[48:51], 0\n"
    "v_exp_f32 v231, v231\n"
    "v_mfma_f32_16x16x32_bf16 v[196:199], v[12:15], a[52:55], v[196:199]\n"
    "v_fma_f32 v233, v233, s62, v28\n"
    "v_cvt_pk_bf16_f32 v146, v228, v229\n"
    "v_fma_f32 v234, v234, s62, v28\n"
    "v_mfma_f32_16x16x32_bf16 v[204:207], v[8:11], a[72:75], 0\n"
    "v_fma_f32 v235, v235, s62, v28\n"
    "v_cvt_pk_bf16_f32 v147, v230, v231\n"
    "v_mfma_f32_16x16x32_bf16 v[204:207], v[12:15], a[76:79], v[204:207]\n"
    "s_waitcnt lgkmcnt(0)\n"
    "ds_read_b128 v[8:11], v16 offset:57344\n"
    "v_exp_f32 v232, v232\n"
    "v_mfma_f32_16x16x32_bf16 v[164:167], v[0:3], a[104:107], v[164:167]\n"
    "v_exp_f32 v233, v233\n"
    "v_mfma_f32_16x16x32_bf16 v[164:167], v[4:7], a[108:111], v[164:167]\n"
    "ds_read_b128 v[12:15], v17 offset:57344\n"
    "v_fma_f32 v236, v236, s62, v28\n"
    "v_exp_f32 v234, v234\n"
    "v_mfma_f32_16x16x32_bf16 v[172:175], v[0:3], a[128:131], v[172:175]\n"
    "v_fma_f32 v237, v237, s62, v28\n"
    "v_cvt_pk_bf16_f32 v148, v232, v233\n"
    "v_mfma_f32_16x16x32_bf16 v[172:175], v[4:7], a[132:135], v[172:175]\n"
    "v_add_u32 v16, 61440, v16\n"
    "v_exp_f32 v235, v235\n"
    "v_mfma_f32_16x16x32_bf16 v[180:183], v[0:3], a[8:11], v[180:183]\n"
    "v_exp_f32 v236, v236\n"
    "v_mfma_f32_16x16x32_bf16 v[180:183], v[4:7], a[12:15], v[180:183]\n"
    "v_add_u32 v17, 61440, v17\n"
    "v_fma_f32 v238, v238, s62, v28\n"
    "v_fma_f32 v239, v239, s62, v28\n"
    "v_mfma_f32_16x16x32_bf16 v[188:191], v[0:3], a[32:35], v[188:191]\n"
    "v_cvt_pk_bf16_f32 v149, v234, v235\n"
    "v_fma_f32 v240, v240, s62, v29\n"
    "v_mfma_f32_16x16x32_bf16 v[188:191], v[4:7], a[36:39], v[188:191]\n"
    "v_exp_f32 v237, v237\n"
    "v_exp_f32 v238, v238\n"
    "v_mfma_f32_16x16x32_bf16 v[196:199], v[0:3], a[56:59], v[196:199]\n"
    "v_exp_f32 v239, v239\n"
    "v_mfma_f32_16x16x32_bf16 v[196:199], v[4:7], a[60:63], v[196:199]\n"
    "s_waitcnt vmcnt(30) lgkmcnt(0)\n"
    "s_barrier\n"
    "v_fma_f32 v241, v241, s62, v29\n"
    "v_cvt_pk_bf16_f32 v150, v236, v237\n"
    "v_fma_f32 v242, v242, s62, v29\n"
    "v_mfma_f32_16x16x32_bf16 v[204:207], v[0:3], a[80:83], v[204:207]\n"
    "v_fma_f32 v243, v243, s62, v29\n"
    "v_cvt_pk_bf16_f32 v151, v238, v239\n"
    "v_mfma_f32_16x16x32_bf16 v[204:207], v[4:7], a[84:87], v[204:207]\n"
    "ds_read_b128 v[0:3], v16 offset:0\n"
    "s_add_u32 m0, s60, 98304\n"
    "v_exp_f32 v240, v240\n"
    "v_mfma_f32_16x16x32_bf16 v[164:167], v[8:11], a[112:115], v[164:167]\n"
    "v_exp_f32 v241, v241\n"
    "v_mfma_f32_16x16x32_bf16 v[164:167], v[12:15], a[116:119], v[164:167]\n"
    "ds_read_b128 v[4:7], v17 offset:0\n"
    "s_add_u32 s36, s71, 36992\n"
    "v_fma_f32 v244, v244, s62, v29\n"
    "v_mfma_f32_16x16x32_bf16 v[172:175], v[8:11], a[136:139], v[172:175]\n"
    "v_fma_f32 v245, v245, s62, v29\n"
    "v_cvt_pk_bf16_f32 v152, v240, v241\n"
    "v_mfma_f32_16x16x32_bf16 v[172:175], v[12:15], a[140:143], v[172:175]\n"
    "buffer_load_dwordx4 v20, s[48:51], s36 offen nt lds\n"
    "v_exp_f32 v242, v242\n"
    "v_mfma_f32_16x16x32_bf16 v[180:183], v[8:11], a[16:19], v[180:183]\n"
    "v_exp_f32 v243, v243\n"
    "v_mfma_f32_16x16x32_bf16 v[180:183], v[12:15], a[20:23], v[180:183]\n"
    "v_fma_f32 v246, v246, s62, v29\n"
    "v_exp_f32 v244, v244\n"
    "v_mfma_f32_16x16x32_bf16 v[188:191], v[8:11], a[40:43], v[188:191]\n"
    "v_fma_f32 v247, v247, s62, v29\n"
    "v_cvt_pk_bf16_f32 v153, v242, v243\n"
    "v_mfma_f32_16x16x32_bf16 v[188:191], v[12:15], a[44:47], v[188:191]\n"
    "v_exp_f32 v245, v245\n"
    "v_fma_f32 v248, v248, s62, v30\n"
    "v_mfma_f32_16x16x32_bf16 v[196:199], v[8:11], a[64:67], v[196:199]\n"
    "v_exp_f32 v246, v246\n"
    "v_mfma_f32_16x16x32_bf16 v[196:199], v[12:15], a[68:71], v[196:199]\n"
    "v_fma_f32 v249, v249, s62, v30\n"
    "v_exp_f32 v247, v247\n"
    "v_cvt_pk_bf16_f32 v154, v244, v245\n"
    "v_mfma_f32_16x16x32_bf16 v[204:207], v[8:11], a[88:91], v[204:207]\n"
    "v_fma_f32 v250, v250, s62, v30\n"
    "v_fma_f32 v251, v251, s62, v30\n"
    "v_mfma_f32_16x16x32_bf16 v[204:207], v[12:15], a[92:95], v[204:207]\n"
    "s_waitcnt lgkmcnt(0)\n"
    "ds_read_b128 v[8:11], v16 offset:1024\n"
    "v_exp_f32 v248, v248\n"
    "v_mfma_f32_16x16x32_bf16 v[208:211], v[0:3], a[96:99], 0\n"
    "v_exp_f32 v249, v249\n"
    "v_mfma_f32_16x16x32_bf16 v[208:211], v[4:7], a[100:103], v[208:211]\n"
    "ds_read_b128 v[12:15], v17 offset:1024\n"
    "v_cvt_pk_bf16_f32 v155, v246, v247\n"
    "v_fma_f32 v252, v252, s62, v30\n"
    "v_mfma_f32_16x16x32_bf16 v[216:219], v[0:3], a[120:123], 0\n"
    "v_fma_f32 v253, v253, s62, v30\n"
    "v_cvt_pk_bf16_f32 v156, v248, v249\n"
    "v_mfma_f32_16x16x32_bf16 v[216:219], v[4:7], a[124:127], v[216:219]\n"
    "buffer_load_dwordx4 v21, s[48:51], s36 offen offset:1024 nt lds\n"
    "v_exp_f32 v250, v250\n"
    "v_mfma_f32_16x16x32_bf16 v[224:227], v[0:3], a[0:3], 0\n"
    "v_exp_f32 v251, v251\n"
    "v_mfma_f32_16x16x32_bf16 v[224:227], v[4:7], a[4:7], v[224:227]\n"
    "v_fma_f32 v254, v254, s62, v30\n"
    "v_exp_f32 v252, v252\n"
    "v_fma_f32 v255, v255, s62, v30\n"
    "v_mfma_f32_16x16x32_bf16 v[232:235], v[0:3], a[24:27], 0\n"
    "v_exp_f32 v253, v253\n"
    "v_mfma_f32_16x16x32_bf16 v[232:235], v[4:7], a[28:31], v[232:235]\n"
    "v_cvt_pk_bf16_f32 v157, v250, v251\n"
    "v_exp_f32 v254, v254\n"
    "v_mfma_f32_16x16x32_bf16 v[240:243], v[0:3], a[48:51], 0\n"
    "v_exp_f32 v255, v255\n"
    "v_mfma_f32_16x16x32_bf16 v[240:243], v[4:7], a[52:55], v[240:243]\n"
    "buffer_load_dwordx4 v31, s[48:51], s36 offen offset:2048 nt lds\n"
    "v_cvt_pk_bf16_f32 v158, v252, v253\n"
    "v_cvt_pk_bf16_f32 v159, v254, v255\n"
    "v_mfma_f32_16x16x32_bf16 v[248:251], v[0:3], a[72:75], 0\n"
    "v_fma_f32 v160, v160, s62, v25\n"
    "v_fma_f32 v161, v161, s62, v25\n"
    "v_mfma_f32_16x16x32_bf16 v[248:251], v[4:7], a[76:79], v[248:251]\n"
    "s_waitcnt lgkmcnt(0)\n"
    "ds_read_b128 v[0:3], v16 offset:2048\n"
    "v_fma_f32 v162, v162, s62, v25\n"
    "v_mfma_f32_16x16x32_bf16 v[208:211], v[8:11], a[104:107], v[208:211]\n"
    "v_exp_f32 v160, v160\n"
    "v_mfma_f32_16x16x32_bf16 v[208:211], v[12:15], a[108:111], v[208:211]\n"
    "ds_read_b128 v[4:7], v17 offset:2048\n"
    "v_fma_f32 v163, v163, s62, v25\n"
    "v_mfma_f32_16x16x32_bf16 v[216:219], v[8:11], a[128:131], v[216:219]\n"
    "v_fma_f32 v164, v164, s62, v25\n"
    "v_fma_f32 v165, v165, s62, v25\n"
    "v_mfma_f32_16x16x32_bf16 v[216:219], v[12:15], a[132:135], v[216:219]\n"
    "v_exp_f32 v161, v161\n"
    "v_mfma_f32_16x16x32_bf16 v[224:227], v[8:11], a[8:11], v[224:227]\n"
    "v_exp_f32 v162, v162\n"
    "v_mfma_f32_16x16x32_bf16 v[224:227], v[12:15], a[12:15], v[224:227]\n"
    "s_add_u32 m0, s60, 110592\n"
    "v_mfma_f32_16x16x32_bf16 v[232:235], v[8:11], a[32:35], v[232:235]\n"
    "v_cvt_pk_bf16_f32 v160, v160, v161\n"
    "v_fma_f32 v166, v166, s62, v25\n"
    "v_mfma_f32_16x16x32_bf16 v[232:235], v[12:15], a[36:39], v[232:235]\n"
    "s_add_u32 s36, s71, 73856\n"
    "v_mfma_f32_16x16x32_bf16 v[240:243], v[8:11], a[56:59], v[240:243]\n"
    "v_exp_f32 v163, v163\n"
    "v_mfma_f32_16x16x32_bf16 v[240:243], v[12:15], a[60:63], v[240:243]\n"
    "buffer_load_dwordx4 v20, s[48:51], s36 offen nt lds\n"
    "v_mfma_f32_16x16x32_bf16 v[248:251], v[8:11], a[80:83], v[248:251]\n"
    "v_fma_f32 v167, v167, s62, v25\n"
    "v_cvt_pk_bf16_f32 v161, v162, v163\n"
    "v_mfma_f32_16x16x32_bf16 v[248:251], v[12:15], a[84:87], v[248:251]\n"
    "s_waitcnt lgkmcnt(0)\n"
    "ds_read_b128 v[8:11], v16 offset:6144\n"
    "v_mfma_f32_16x16x32_bf16 v[208:211], v[0:3], a[112:115], v[208:211]\n"
    "v_exp_f32 v164, v164\n"
    "v_mfma_f32_16x16x32_bf16 v[208:211], v[4:7], a[116:119], v[208:211]\n"
    "ds_read_b128 v[12:15], v17 offset:6144\n"
    "v_mfma_f32_16x16x32_bf16 v[216:219], v[0:3], a[136:139], v[216:219]\n"
    "v_fma_f32 v168, v168, s62, v26\n"
    "v_fma_f32 v169, v169, s62, v26\n"
    "v_mfma_f32_16x16x32_bf16 v[216:219], v[4:7], a[140:143], v[216:219]\n"
    "v_exp_f32 v165, v165\n"
    "v_mfma_f32_16x16x32_bf16 v[224:227], v[0:3], a[16:19], v[224:227]\n"
    "v_exp_f32 v166, v166\n"
    "v_mfma_f32_16x16x32_bf16 v[224:227], v[4:7], a[20:23], v[224:227]\n"
    "v_exp_f32 v167, v167\n"
    "v_mfma_f32_16x16x32_bf16 v[232:235], v[0:3], a[40:43], v[232:235]\n"
    "v_cvt_pk_bf16_f32 v162, v164, v165\n"
    "v_fma_f32 v170, v170, s62, v26\n"
    "v_mfma_f32_16x16x32_bf16 v[232:235], v[4:7], a[44:47], v[232:235]\n"
    "buffer_load_dwordx4 v21, s[48:51], s36 offen offset:1024 nt lds\n"
    "s_memrealtime s[92:93]\n"
    "v_mfma_f32_16x16x32_bf16 v[240:243], v[0:3], a[64:67], v[240:243]\n"
    "v_exp_f32 v168, v168\n"
    "v_mfma_f32_16x16x32_bf16 v[240:243], v[4:7], a[68:71], v[240:243]\n"
    "v_fma_f32 v171, v171, s62, v26\n"
    "v_mfma_f32_16x16x32_bf16 v[248:251], v[0:3], a[88:91], v[248:251]\n"
    "v_cvt_pk_bf16_f32 v163, v166, v167\n"
    "v_fma_f32 v172, v172, s62, v26\n"
    "v_mfma_f32_16x16x32_bf16 v[248:251], v[4:7], a[92:95], v[248:251]\n"
    "s_waitcnt lgkmcnt(0)\n"
    "ds_read_b128 v[0:3], v16 offset:7168\n"
    "v_mfma_f32_16x16x32_bf16 v[212:215], v[8:11], a[96:99], 0\n"
    "v_exp_f32 v169, v169\n"
    "v_mfma_f32_16x16x32_bf16 v[212:215], v[12:15], a[100:103], v[212:215]\n"
    "s_add_u32 s94, s94, 190\n"
    "s_sub_u32 s95, s94, s92\n"
    "s_cmp_gt_i32 s95, 3\n"
    "s_cbranch_scc1 L_pc5_%=\n"
    "L_pr5_%=:\n"
    "ds_read_b128 v[4:7], v17 offset:7168\n"
    "v_mfma_f32_16x16x32_bf16 v[220:223], v[8:11], a[120:123], 0\n"
    "v_mov_b32 v164, 0x3f803f80\n"
    "v_mov_b32 v165, 0x3f803f80\n"
    "v_mfma_f32_16x16x32_bf16 v[220:223], v[12:15], a[124:127], v[220:223]\n"
    "v_exp_f32 v170, v170\n"
    "v_mfma_f32_16x16x32_bf16 v[228:231], v[8:11], a[0:3], 0\n"
    "v_exp_f32 v171, v171\n"
    "v_mfma_f32_16x16x32_bf16 v[228:231], v[12:15], a[4:7], v[228:231]\n"
    "buffer_load_dwordx4 v31, s[48:51], s36 offen offset:2048 nt lds\n"
    "v_mfma_f32_16x16x32_bf16 v[236:239], v[8:11], a[24:27], 0\n"
    "v_mov_b32 v166, 0x3f803f80\n"
    "v_mov_b32 v167, 0x3f803f80\n"
    "v_mfma_f32_16x16x32_bf16 v[236:239], v[12:15], a[28:31], v[236:239]\n"
    "v_fma_f32 v173, v173, s62, v26\n"
    "v_cvt_pk_bf16_f32 v168, v168, v169\n"
    "v_mfma_f32_16x16x32_bf16 v[244:247], v[8:11], a[48:51], 0\n"
    "v_exp_f32 v172, v172\n"
    "v_mfma_f32_16x16x32_bf16 v[244:247], v[12:15], a[52:55], v[244:247]\n"
    "v_fma_f32 v174, v174, s62, v26\n"
    "v_fma_f32 v175, v175, s62, v26\n"
    "v_mfma_f32_16x16x32_bf16 v[252:255], v[8:11], a[72:75], 0\n"
    "v_cvt_pk_bf16_f32 v169, v170, v171\n"
    "v_fma_f32 v176, v176, s62, v27\n"
    "v_mfma_f32_16x16x32_bf16 v[252:255], v[12:15], a[76:79], v[252:255]\n"
    "s_waitcnt lgkmcnt(0)\n"
    "ds_read_b128 v[8:11], v16 offset:8192\n"
    "v_mfma_f32_16x16x32_bf16 v[212:215], v[0:3], a[104:107], v[212:215]\n"
    "v_exp_f32 v173, v173\n"
    "v_mfma_f32_16x16x32_bf16 v[212:215], v[4:7], a[108:111], v[212:215]\n"
    "ds_read_b128 v[12:15], v17 offset:8192\n"
    "v_mfma_f32_16x16x32_bf16 v[220:223], v[0:3], a[128:131], v[220:223]\n"
    "v_fma_f32 v177, v177, s62, v27\n"
    "v_cvt_pk_bf16_f32 v170, v172, v173\n"
    "v_mfma_f32_16x16x32_bf16 v[220:223], v[4:7], a[132:135], v[220:223]\n"
    "v_exp_f32 v174, v174\n"
    "v_mfma_f32_16x16x32_bf16 v[228:231], v[0:3], a[8:11], v[228:231]\n"
    "v_exp_f32 v175, v175\n"
    "v_mfma_f32_16x16x32_bf16 v[228:231], v[4:7], a[12:15], v[228:231]\n"
    "v_fma_f32 v178, v178, s62, v27\n"
    "v_mfma_f32_16x16x32_bf16 v[236:239], v[0:3], a[32:35], v[236:239]\n"
    "v_fma_f32 v179, v179, s62, v27\n"
    "v_cvt_pk_bf16_f32 v171, v174, v175\n"
    "v_mfma_f32_16x16x32_bf16 v[236:239], v[4:7], a[36:39], v[236:239]\n"
    "v_exp_f32 v176, v176\n"
    "v_mfma_f32_16x16x32_bf16 v[244:247], v[0:3], a[56:59], v[244:247]\n"
    "v_exp_f32 v177, v177\n"
    "v_mfma_f32_16x16x32_bf16 v[244:247], v[4:7], a[60:63], v[244:247]\n"
    "s_waitcnt vmcnt(9) lgkmcnt(0)\n"
    "s_barrier\n"
    "v_fma_f32 v180, v180, s62, v27\n"
    "v_mfma_f32_16x16x32_bf16 v[252:255], v[0:3], a[80:83], v[252:255]\n"
    "v_fma_f32 v181, v181, s62, v27\n"
    "v_cvt_pk_bf16_f32 v176, v176, v177\n"
    "v_mfma_f32_16x16x32_bf16 v[252:255], v[4:7], a[84:87], v[252:255]\n"
    "ds_read_b64_tr_b16 v[0:1], v18 offset:0\n"
    "ds_read_b64_tr_b16 v[2:3], v18 offset:2048\n"
    "s_add_u32 m0, s60, 122880\n"
    "v_mfma_f32_16x16x32_bf16 v[212:215], v[8:11], a[112:115], v[212:215]\n"
    "v_exp_f32 v178, v178\n"
    "v_mfma_f32_16x16x32_bf16 v[212:215], v[12:15], a[116:119], v[212:215]\n"
    "ds_read_b64_tr_b16 v[4:5], v18 offset:4096\n"
    "ds_read_b64_tr_b16 v[6:7], v18 offset:6144\n"
    "s_add_u32 s36, s71, 110720\n"
    "v_mfma_f32_16x16x32_bf16 v[220:223], v[8:11], a[136:139], v[220:223]\n"
    "v_fma_f32 v182, v182, s62, v27\n"
    "v_fma_f32 v183, v183, s62, v27\n"
    "v_mfma_f32_16x16x32_bf16 v[220:223], v[12:15], a[140:143], v[220:223]\n"
    "buffer_load_dwordx4 v20, s[48:51], s36 offen nt lds\n"
    "v_mfma_f32_16x16x32_bf16 v[228:231], v[8:11], a[16:19], v[228:231]\n"
    "v_exp_f32 v179, v179\n"
    "v_mfma_f32_16x16x32_bf16 v[228:231], v[12:15], a[20:23], v[228:231]\n"
    "v_exp_f32 v180, v180\n"
    "v_mfma_f32_16x16x32_bf16 v[236:239], v[8:11], a[40:43], v[236:239]\n"
    "v_cvt_pk_bf16_f32 v177, v178, v179\n"
    "v_fma_f32 v184, v184, s62, v28\n"
    "v_mfma_f32_16x16x32_bf16 v[236:239], v[12:15], a[44:47], v[236:239]\n"
    "v_exp_f32 v181, v181\n"
    "v_mfma_f32_16x16x32_bf16 v[244:247], v[8:11], a[64:67], v[244:247]\n"
    "v_exp_f32 v182, v182\n"
    "v_mfma_f32_16x16x32_bf16 v[244:247], v[12:15], a[68:71], v[244:247]\n"
    "v_fma_f32 v185, v185, s62, v28\n"
    "v_mfma_f32_16x16x32_bf16 v[252:255], v[8:11], a[88:91], v[252:255]\n"
    "v_cvt_pk_bf16_f32 v178, v180, v181\n"
    "v_fma_f32 v186, v186, s62, v28\n"
    "v_mfma_f32_16x16x32_bf16 v[252:255], v[12:15], a[92:95], v[252:255]\n"
    "s_waitcnt lgkmcnt(0)\n"
    "ds_read_b64_tr_b16 v[8:9], v18 offset:8\n"
    "ds_read_b64_tr_b16 v[10:11], v18 offset:2056\n"
    "v_mfma_f32_16x16x32_bf16 a[0:3], v[0:3], a[144:147], 0\n"
    "v_add_u32 v16, s75, v16\n"
    "v_add_u32 v17, s75, v17\n"
    "v_mfma_f32_16x16x32_bf16 a[0:3], v[4:7], a[168:171], a[0:3]\n"
    "ds_read_b64_tr_b16 v[12:13], v18 offset:4104\n"
    "ds_read_b64_tr_b16 v[14:15], v18 offset:6152\n"
    "v_mfma_f32_16x16x32_bf16 a[8:11], v[0:3], a[148:151], 0\n"
    "v_fma_f32 v187, v187, s62, v28\n"
    "v_fma_f32 v188, v188, s62, v28\n"
    "v_mfma_f32_16x16x32_bf16 a[8:11], v[4:7], a[172:175], a[8:11]\n"
    "ds_read_b128 a[96:99], v16 offset:0\n"
    "v_mfma_f32_16x16x32_bf16 a[16:19], v[0:3], a[152:155], 0\n"
    "v_exp_f32 v183, v183\n"
    "v_mfma_f32_16x16x32_bf16 a[16:19], v[4:7], a[176:179], a[16:19]\n"
    "ds_read_b128 a[100:103], v17 offset:0\n"
    "v_mfma_f32_16x16x32_bf16 a[24:27], v[0:3], a[156:159], 0\n"
    "v_cvt_pk_bf16_f32 v179, v182, v183\n"
    "v_fma_f32 v189, v189, s62, v28\n"
    "v_mfma_f32_16x16x32_bf16 a[24:27], v[4:7], a[180:183], a[24:27]\n"
    "ds_read_b128 a[104:107], v16 offset:1024\n"
    "v_mfma_f32_16x16x32_bf16 a[32:35], v[0:3], a[160:163], 0\n"
    "v_exp_f32 v184, v184\n"
    "v_mfma_f32_16x16x32_bf16 a[32:35], v[4:7], a[184:187], a[32:35]\n"
    "ds_read_b128 a[108:111], v17 offset:1024\n"
    "v_mfma_f32_16x16x32_bf16 a[40:43], v[0:3], a[164:167], 0\n"
    "v_exp_f32 v185, v185\n"
    "v_mfma_f32_16x16x32_bf16 a[40:43], v[4:7], a[188:191], a[40:43]\n"
    "s_waitcnt lgkmcnt(4)\n"
    "ds_read_b128 a[112:115], v16 offset:2048\n"
    "v_mfma_f32_16x16x32_bf16 a[4:7], v[8:11], a[144:147], 0\n"
    "v_exp_f32 v186, v186\n"
    "v_mfma_f32_16x16x32_bf16 a[4:7], v[12:15], a[168:171], a[4:7]\n"
    "ds_read_b128 a[116:119], v17 offset:2048\n"
    "v_mfma_f32_16x16x32_bf16 a[12:15], v[8:11], a[148:151], 0\n"
    "v_cvt_pk_bf16_f32 v184, v184, v185\n"
    "v_fma_f32 v190, v190, s62, v28\n"
    "v_mfma_f32_16x16x32_bf16 a[12:15], v[12:15], a[172:175], a[12:15]\n"
    "ds_read_b128 a[120:123], v16 offset:6144\n"
    "v_mfma_f32_16x16x32_bf16 a[20:23], v[8:11], a[152:155], 0\n"
    "v_exp_f32 v187, v187\n"
    "v_mfma_f32_16x16x32_bf16 a[20:23], v[12:15], a[176:179], a[20:23]\n"
    "ds_read_b128 a[124:127], v17 offset:6144\n"
    "v_mfma_f32_16x16x32_bf16 a[28:31], v[8:11], a[156:159], 0\n"
    "v_fma_f32 v191, v191, s62, v28\n"
    "v_cvt_pk_bf16_f32 v185, v186, v187\n"
    "v_mfma_f32_16x16x32_bf16 a[28:31], v[12:15], a[180:183], a[28:31]\n"
    "ds_read_b128 a[128:131], v16 offset:7168\n"
    "v_mfma_f32_16x16x32_bf16 a[36:39], v[8:11], a[160:163], 0\n"
    "v_exp_f32 v188, v188\n"
    "v_mfma_f32_16x16x32_bf16 a[36:39], v[12:15], a[184:187], a[36:39]\n"
    "ds_read_b128 a[132:135], v17 offset:7168\n"
    "v_mfma_f32_16x16x32_bf16 a[44:47], v[8:11], a[164:167], 0\n"
    "v_fma_f32 v192, v192, s62, v29\n"
    "v_fma_f32 v193, v193, s62, v29\n"
    "v_mfma_f32_16x16x32_bf16 a[44:47], v[12:15], a[188:191], a[44:47]\n"
    "ds_read_b64_tr_b16 v[0:1], v18 offset:8192\n"
    "ds_read_b64_tr_b16 v[2:3], v18 offset:10240\n"
    "v_mfma_f32_16x16x32_bf16 a[48:51], v[164:167], a[144:147], 0\n"
    "v_exp_f32 v189, v189\n"
    "v_mfma_f32_16x16x32_bf16 a[48:51], v[164:167], a[168:171], a[48:51]\n"
    "ds_read_b64_tr_b16 v[4:5], v18 offset:12288\n"
    "ds_read_b64_tr_b16 v[6:7], v18 offset:14336\n"
    "v_mfma_f32_16x16x32_bf16 a[56:59], v[164:167], a[148:151], 0\n"
    "v_cvt_pk_bf16_f32 v186, v188, v189\n"
    "v_fma_f32 v194, v194, s62, v29\n"
    "v_mfma_f32_16x16x32_bf16 a[56:59], v[164:167], a[172:175], a[56:59]\n"
    "ds_read_b128 a[136:139], v16 offset:8192\n"
    "v_mfma_f32_16x16x32_bf16 a[64:67], v[164:167], a[152:155], 0\n"
    "v_exp_f32 v190, v190\n"
    "v_mfma_f32_16x16x32_bf16 a[64:67], v[164:167], a[176:179], a[64:67]\n"
    "ds_read_b128 a[140:143], v17 offset:8192\n"
    "v_mfma_f32_16x16x32_bf16 a[72:75], v[164:167], a[156:159], 0\n"
    "v_fma_f32 v195, v195, s62, v29\n"
    "v_fma_f32 v196, v196, s62, v29\n"
    "v_mfma_f32_16x16x32_bf16 a[72:75], v[164:167], a[180:183], a[72:75]\n"
    "buffer_load_dwordx4 v21, s[48:51], s36 offen offset:1024 nt lds\n"
    "v_mfma_f32_16x16x32_bf16 a[80:83], v[164:167], a[160:163], 0\n"
    "v_exp_f32 v191, v191\n"
    "v_mfma_f32_16x16x32_bf16 a[80:83], v[164:167], a[184:187], a[80:83]\n"
    "v_exp_f32 v192, v192\n"
    "v_mfma_f32_16x16x32_bf16 a[88:91], v[164:167], a[164:167], 0\n"
    "v_cvt_pk_bf16_f32 v187, v190, v191\n"
    "v_fma_f32 v197, v197, s62, v29\n"
    "v_mfma_f32_16x16x32_bf16 a[88:91], v[164:167], a[188:191], a[88:91]\n"
    "s_waitcnt lgkmcnt(2)\n"
    "ds_read_b64_tr_b16 v[8:9], v18 offset:8200\n"
    "ds_read_b64_tr_b16 v[10:11], v18 offset:10248\n"
    "v_mfma_f32_16x16x32_bf16 a[0:3], v[0:3], a[192:195], a[0:3]\n"
    "v_exp_f32 v193, v193\n"
    "v_mfma_f32_16x16x32_bf16 a[0:3], v[4:7], a[216:219], a[0:3]\n"
    "ds_read_b64_tr_b16 v[12:13], v18 offset:12296\n"
    "ds_read_b64_tr_b16 v[14:15], v18 offset:14344\n"
    "v_mfma_f32_16x16x32_bf16 a[8:11], v[0:3], a[196:199], a[8:11]\n"
    "v_cvt_pk_bf16_f32 v192, v192, v193\n"
    "v_fma_f32 v198, v198, s62, v29\n"
    "v_mfma_f32_16x16x32_bf16 a[8:11], v[4:7], a[220:223], a[8:11]\n"
    "s_waitcnt lgkmcnt(4)\n"
    "s_barrier\n"
    "v_mfma_f32_16x16x32_bf16 a[16:19], v[0:3], a[200:203], a[16:19]\n"
    "v_exp_f32 v194, v194\n"
    "v_mfma_f32_16x16x32_bf16 a[16:19], v[4:7], a[224:227], a[16:19]\n"
    "buffer_load_dwordx4 v31, s[48:51], s36 offen offset:2048 nt lds\n"
    "v_mfma_f32_16x16x32_bf16 a[24:27], v[0:3], a[204:207], a[24:27]\n"
    "v_add_u32 v16, s38, v16\n"
    "v_add_u32 v17, s38, v17\n"
    "v_mfma_f32_16x16x32_bf16 a[24:27], v[4:7], a[228:231], a[24:27]\n"
    "s_add_u32 m0, s60, 0\n"
    "v_mfma_f32_16x16x32_bf16 a[32:35], v[0:3], a[208:211], a[32:35]\n"
    "v_exp_f32 v195, v195\n"
    "v_mfma_f32_16x16x32_bf16 a[32:35], v[4:7], a[232:235], a[32:35]\n"
    "s_add_u32 s36, s71, 256\n"
    "v_mfma_f32_16x16x32_bf16 a[40:43], v[0:3], a[212:215], a[40:43]\n"
    "v_fma_f32 v199, v199, s62, v29\n"
    "v_cvt_pk_bf16_f32 v193, v194, v195\n"
    "v_mfma_f32_16x16x32_bf16 a[40:43], v[4:7], a[236:239], a[40:43]\n"
    "s_waitcnt lgkmcnt(0)\n"
    "buffer_load_dwordx4 v20, s[48:51], s36 offen nt lds\n"
    "v_mfma_f32_16x16x32_bf16 a[4:7], v[8:11], a[192:195], a[4:7]\n"
    "v_exp_f32 v196, v196\n"
    "v_mfma_f32_16x16x32_bf16 a[4:7], v[12:15], a[216:219], a[4:7]\n"
    "v_exp_f32 v197, v197\n"
    "v_mfma_f32_16x16x32_bf16 a[12:15], v[8:11], a[196:199], a[12:15]\n"
    "v_fma_f32 v200, v200, s62, v30\n"
    "v_fma_f32 v201, v201, s62, v30\n"
    "v_mfma_f32_16x16x32_bf16 a[12:15], v[12:15], a[220:223], a[12:15]\n"
    "v_exp_f32 v198, v198\n"
    "v_mfma_f32_16x16x32_bf16 a[20:23], v[8:11], a[200:203], a[20:23]\n"
    "v_exp_f32 v199, v199\n"
    "v_mfma_f32_16x16x32_bf16 a[20:23], v[12:15], a[224:227], a[20:23]\n"
    "v_cvt_pk_bf16_f32 v194, v196, v197\n"
    "v_fma_f32 v202, v202, s62, v30\n"
    "v_mfma_f32_16x16x32_bf16 a[28:31], v[8:11], a[204:207], a[28:31]\n"
    "v_fma_f32 v203, v203, s62, v30\n"
    "v_cvt_pk_bf16_f32 v195, v198, v199\n"
    "v_mfma_f32_16x16x32_bf16 a[28:31], v[12:15], a[228:231], a[28:31]\n"
    "v_exp_f32 v200, v200\n"
    "v_mfma_f32_16x16x32_bf16 a[36:39], v[8:11], a[208:211], a[36:39]\n"
    "v_exp_f32 v201, v201\n"
    "v_mfma_f32_16x16x32_bf16 a[36:39], v[12:15], a[232:235], a[36:39]\n"
    "v_fma_f32 v204, v204, s62, v30\n"
    "v_mfma_f32_16x16x32_bf16 a[44:47], v[8:11], a[212:215], a[44:47]\n"
    "v_fma_f32 v205, v205, s62, v30\n"
    "v_cvt_pk_bf16_f32 v200, v200, v201\n"
    "v_mfma_f32_16x16x32_bf16 a[44:47], v[12:15], a[236:239], a[44:47]\n"
    "ds_read_b64_tr_b16 v[0:1], v18 offset:16384\n"
    "ds_read_b64_tr_b16 v[2:3], v18 offset:18432\n"
    "v_mfma_f32_16x16x32_bf16 a[48:51], v[164:167], a[192:195], a[48:51]\n"
    "v_exp_f32 v202, v202\n"
    "v_mfma_f32_16x16x32_bf16 a[48:51], v[164:167], a[216:219], a[48:51]\n"
    "ds_read_b64_tr_b16 v[4:5], v18 offset:20480\n"
    "ds_read_b64_tr_b16 v[6:7], v18 offset:22528\n"
    "v_mfma_f32_16x16x32_bf16 a[56:59], v[164:167], a[196:199], a[56:59]\n"
    "v_fma_f32 v206, v206, s62, v30\n"
    "v_fma_f32 v207, v207, s62, v30\n"
    "v_mfma_f32_16x16x32_bf16 a[56:59], v[164:167], a[220:223], a[56:59]\n"
    "buffer_load_dwordx4 v21, s[48:51], s36 offen offset:1024 nt lds\n"
    "v_mfma_f32_16x16x32_bf16 a[64:67], v[164:167], a[200:203], a[64:67]\n"
    "v_exp_f32 v203, v203\n"
    "v_mfma_f32_16x16x32_bf16 a[64:67], v[164:167], a[224:227], a[64:67]\n"
    "v_exp_f32 v204, v204\n"
    "v_mfma_f32_16x16x32_bf16 a[72:75], v[164:167], a[204:207], a[72:75]\n"
    "v_exp_f32 v205, v205\n"
    "v_mfma_f32_16x16x32_bf16 a[72:75], v[164:167], a[228:231], a[72:75]\n"
    "v_cvt_pk_bf16_f32 v201, v202, v203\n"
    "v_mfma_f32_16x16x32_bf16 a[80:83], v[164:167], a[208:211], a[80:83]\n"
    "v_exp_f32 v206, v206\n"
    "v_mfma_f32_16x16x32_bf16 a[80:83], v[164:167], a[232:235], a[80:83]\n"
    "v_exp_f32 v207, v207\n"
    "v_mfma_f32_16x16x32_bf16 a[88:91], v[164:167], a[212:215], a[88:91]\n"
    "v_cvt_pk_bf16_f32 v202, v204, v205\n"
    "v_cvt_pk_bf16_f32 v203, v206, v207\n"
    "v_mfma_f32_16x16x32_bf16 a[88:91], v[164:167], a[236:239], a[88:91]\n"
    "s_waitcnt lgkmcnt(0)\n"
    "ds_read_b64_tr_b16 v[8:9], v18 offset:16392\n"
    "ds_read_b64_tr_b16 v[10:11], v18 offset:18440\n"
    "v_mfma_f32_16x16x32_bf16 a[0:3], v[0:3], a[240:243], a[0:3]\n"
    "v_fma_f32 v208, v208, s62, v25\n"
    "v_fma_f32 v209, v209, s62, v25\n"
    "v_mfma_f32_16x16x32_bf16 a[0:3], v[4:7], v[40:43], a[0:3]\n"
    "ds_read_b64_tr_b16 v[12:13], v18 offset:20488\n"
    "ds_read_b64_tr_b16 v[14:15], v18 offset:22536\n"
    "v_mfma_f32_16x16x32_bf16 a[8:11], v[0:3], a[244:247], a[8:11]\n"
    "v_fma_f32 v210, v210, s62, v25\n"
    "v_fma_f32 v211, v211, s62, v25\n"
    "v_mfma_f32_16x16x32_bf16 a[8:11], v[4:7], v[44:47], a[8:11]\n"
    "v_exp_f32 v208, v208\n"
    "v_mfma_f32_16x16x32_bf16 a[16:19], v[0:3], a[248:251], a[16:19]\n"
    "v_exp_f32 v209, v209\n"
    "v_mfma_f32_16x16x32_bf16 a[16:19], v[4:7], v[48:51], a[16:19]\n"
    "v_fma_f32 v212, v212, s62, v25\n"
    "v_mfma_f32_16x16x32_bf16 a[24:27], v[0:3], a[252:255], a[24:27]\n"
    "v_fma_f32 v213, v213, s62, v25\n"
    "v_cvt_pk_bf16_f32 v208, v208, v209\n"
    "v_mfma_f32_16x16x32_bf16 a[24:27], v[4:7], v[52:55], a[24:27]\n"
    "buffer_load_dwordx4 v31, s[48:51], s36 offen offset:2048 nt lds\n"
    "v_mfma_f32_16x16x32_bf16 a[32:35], v[0:3], v[32:35], a[32:35]\n"
    "v_exp_f32 v210, v210\n"
    "v_mfma_f32_16x16x32_bf16 a[32:35], v[4:7], v[56:59], a[32:35]\n"
    "v_exp_f32 v211, v211\n"
    "v_mfma_f32_16x16x32_bf16 a[40:43], v[0:3], v[36:39], a[40:43]\n"
    "v_fma_f32 v214, v214, s62, v25\n"
    "v_fma_f32 v215, v215, s62, v25\n"
    "v_mfma_f32_16x16x32_bf16 a[40:43], v[4:7], v[60:63], a[40:43]\n"
    "s_waitcnt lgkmcnt(0)\n"
    "v_exp_f32 v212, v212\n"
    "v_mfma_f32_16x16x32_bf16 a[4:7], v[8:11], a[240:243], a[4:7]\n"
    "v_exp_f32 v213, v213\n"
    "v_mfma_f32_16x16x32_bf16 a[4:7], v[12:15], v[40:43], a[4:7]\n"
    "v_cvt_pk_bf16_f32 v209, v210, v211\n"
    "v_fma_f32 v216, v216, s62, v26\n"
    "v_mfma_f32_16x16x32_bf16 a[12:15], v[8:11], a[244:247], a[12:15]\n"
    "v_fma_f32 v217, v217, s62, v26\n"
    "v_cvt_pk_bf16_f32 v210, v212, v213\n"
    "v_mfma_f32_16x16x32_bf16 a[12:15], v[12:15], v[44:47], a[12:15]\n"
    "v_exp_f32 v214, v214\n"
    "v_mfma_f32_16x16x32_bf16 a[20:23], v[8:11], a[248:251], a[20:23]\n"
    "v_exp_f32 v215, v215\n"
    "v_mfma_f32_16x16x32_bf16 a[20:23], v[12:15], v[48:51], a[20:23]\n"
    "v_fma_f32 v218, v218, s62, v26\n"
    "v_mfma_f32_16x16x32_bf16 a[28:31], v[8:11], a[252:255], a[28:31]\n"
    "v_fma_f32 v219, v219, s62, v26\n"
    "v_cvt_pk_bf16_f32 v211, v214, v215\n"
    "v_mfma_f32_16x16x32_bf16 a[28:31], v[12:15], v[52:55], a[28:31]\n"
    "v_exp_f32 v216, v216\n"
    "v_mfma_f32_16x16x32_bf16 a[36:39], v[8:11], v[32:35], a[36:39]\n"
    "v_exp_f32 v217, v217\n"
    "v_mfma_f32_16x16x32_bf16 a[36:39], v[12:15], v[56:59], a[36:39]\n"
    "v_fma_f32 v220, v220, s62, v26\n"
    "v_mfma_f32_16x16x32_bf16 a[44:47], v[8:11], v[36:39], a[44:47]\n"
    "v_fma_f32 v221, v221, s62, v26\n"
    "v_cvt_pk_bf16_f32 v216, v216, v217\n"
    "v_mfma_f32_16x16x32_bf16 a[44:47], v[12:15], v[60:63], a[44:47]\n"
    "ds_read_b64_tr_b16 v[0:1], v18 offset:24576\n"
    "ds_read_b64_tr_b16 v[2:3], v18 offset:26624\n"
    "s_add_u32 m0, s60, 12288\n"
    "v_mfma_f32_16x16x32_bf16 a[48:51], v[164:167], a[240:243], a[48:51]\n"
    "v_exp_f32 v218, v218\n"
    "v_mfma_f32_16x16x32_bf16 a[48:51], v[164:167], v[40:43], a[48:51]\n"
    "ds_read_b64_tr_b16 v[4:5], v18 offset:28672\n"
    "ds_read_b64_tr_b16 v[6:7], v18 offset:30720\n"
    "s_add_u32 s36, s71, 37120\n"
    "v_mfma_f32_16x16x32_bf16 a[56:59], v[164:167], a[244:247], a[56:59]\n"
    "v_fma_f32 v222, v222, s62, v26\n"
    "v_fma_f32 v223, v223, s62, v26\n"
    "v_mfma_f32_16x16x32_bf16 a[56:59], v[164:167], v[44:47], a[56:59]\n"
    "buffer_load_dwordx4 v20, s[48:51], s36 offen nt lds\n"
    "v_mfma_f32_16x16x32_bf16 a[64:67], v[164:167], a[248:251], a[64:67]\n"
    "v_exp_f32 v219, v219\n"
    "v_mfma_f32_16x16x32_bf16 a[64:67], v[164:167], v[48:51], a[64:67]\n"
    "v_exp_f32 v220, v220\n"
    "v_mfma_f32_16x16x32_bf16 a[72:75], v[164:167], a[252:255], a[72:75]\n"
    "v_cvt_pk_bf16_f32 v217, v218, v219\n"
    "v_fma_f32 v224, v224, s62, v27\n"
    "v_mfma_f32_16x16x32_bf16 a[72:75], v[164:167], v[52:55], a[72:75]\n"
    "v_exp_f32 v221, v221\n"
    "v_mfma_f32_16x16x32_bf16 a[80:83], v[164:167], v[32:35], a[80:83]\n"
    "v_exp_f32 v222, v222\n"
    "v_mfma_f32_16x16x32_bf16 a[80:83], v[164:167], v[56:59], a[80:83]\n"
    "v_fma_f32 v225, v225, s62, v27\n"
    "v_mfma_f32_16x16x32_bf16 a[88:91], v[164:167], v[36:39], a[88:91]\n"
    "v_cvt_pk_bf16_f32 v218, v220, v221\n"
    "v_fma_f32 v226, v226, s62, v27\n"
    "v_mfma_f32_16x16x32_bf16 a[88:91], v[164:167], v[60:63], a[88:91]\n"
    "s_waitcnt lgkmcnt(0)\n"
    "ds_read_b64_tr_b16 v[8:9], v18 offset:24584\n"
    "ds_read_b64_tr_b16 v[10:11], v18 offset:26632\n"
    "v_mfma_f32_16x16x32_bf16 a[0:3], v[0:3], v[64:67], a[0:3]\n"
    "v_exp_f32 v223, v223\n"
    "v_mfma_f32_16x16x32_bf16 a[0:3], v[4:7], v[88:91], a[0:3]\n"
    "ds_read_b64_tr_b16 v[12:13], v18 offset:28680\n"
    "ds_read_b64_tr_b16 v[14:15], v18 offset:30728\n"
    "v_mfma_f32_16x16x32_bf16 a[8:11], v[0:3], v[68:71], a[8:11]\n"
    "v_fma_f32 v227, v227, s62, v27\n"
    "v_cvt_pk_bf16_f32 v219, v222, v223\n"
    "v_mfma_f32_16x16x32_bf16 a[8:11], v[4:7], v[92:95], a[8:11]\n"
    "v_exp_f32 v224, v224\n"
    "v_mfma_f32_16x16x32_bf16 a[16:19], v[0:3], v[72:75], a[16:19]\n"
    "v_exp_f32 v225, v225\n"
    "v_mfma_f32_16x16x32_bf16 a[16:19], v[4:7], v[96:99], a[16:19]\n"
    "v_fma_f32 v228, v228, s62, v27\n"
    "v_mfma_f32_16x16x32_bf16 a[24:27], v[0:3], v[76:79], a[24:27]\n"
    "v_fma_f32 v229, v229, s62, v27\n"
    "v_cvt_pk_bf16_f32 v224, v224, v225\n"
    "v_mfma_f32_16x16x32_bf16 a[24:27], v[4:7], v[100:103], a[24:27]\n"
    "buffer_load_dwordx4 v21, s[48:51], s36 offen offset:1024 nt lds\n"
    "v_mfma_f32_16x16x32_bf16 a[32:35], v[0:3], v[80:83], a[32:35]\n"
    "v_exp_f32 v226, v226\n"
    "v_mfma_f32_16x16x32_bf16 a[32:35], v[4:7], v[104:107], a[32:35]\n"
    "v_exp_f32 v227, v227\n"
    "v_mfma_f32_16x16x32_bf16 a[40:43], v[0:3], v[84:87], a[40:43]\n"
    "v_fma_f32 v230, v230, s62, v27\n"
    "v_fma_f32 v231, v231, s62, v27\n"
    "v_mfma_f32_16x16x32_bf16 a[40:43], v[4:7], v[108:111], a[40:43]\n"
    "s_waitcnt lgkmcnt(0)\n"
    "v_exp_f32 v228, v228\n"
    "v_mfma_f32_16x16x32_bf16 a[4:7], v[8:11], v[64:67], a[4:7]\n"
    "v_exp_f32 v229, v229\n"
    "v_mfma_f32_16x16x32_bf16 a[4:7], v[12:15], v[88:91], a[4:7]\n"
    "v_cvt_pk_bf16_f32 v225, v226, v227\n"
    "v_fma_f32 v232, v232, s62, v28\n"
    "v_mfma_f32_16x16x32_bf16 a[12:15], v[8:11], v[68:71], a[12:15]\n"
    "v_fma_f32 v233, v233, s62, v28\n"
    "v_cvt_pk_bf16_f32 v226, v228, v229\n"
    "v_mfma_f32_16x16x32_bf16 a[12:15], v[12:15], v[92:95], a[12:15]\n"
    "v_exp_f32 v230, v230\n"
    "v_mfma_f32_16x16x32_bf16 a[20:23], v[8:11], v[72:75], a[20:23]\n"
    "v_exp_f32 v231, v231\n"
    "v_mfma_f32_16x16x32_bf16 a[20:23], v[12:15], v[96:99], a[20:23]\n"
    "v_fma_f32 v234, v234, s62, v28\n"
    "v_mfma_f32_16x16x32_bf16 a[28:31], v[8:11], v[76:79], a[28:31]\n"
    "v_fma_f32 v235, v235, s62, v28\n"
    "v_cvt_pk_bf16_f32 v227, v230, v231\n"
    "v_mfma_f32_16x16x32_bf16 a[28:31], v[12:15], v[100:103], a[28:31]\n"
    "v_exp_f32 v232, v232\n"
    "v_mfma_f32_16x16x32_bf16 a[36:39], v[8:11], v[80:83], a[36:39]\n"
    "v_exp_f32 v233, v233\n"
    "v_mfma_f32_16x16x32_bf16 a[36:39], v[12:15], v[104:107], a[36:39]\n"
    "v_fma_f32 v236, v236, s62, v28\n"
    "v_mfma_f32_16x16x32_bf16 a[44:47], v[8:11], v[84:87], a[44:47]\n"
    "v_fma_f32 v237, v237, s62, v28\n"
    "v_cvt_pk_bf16_f32 v232, v232, v233\n"
    "v_mfma_f32_16x16x32_bf16 a[44:47], v[12:15], v[108:111], a[44:47]\n"
    "ds_read_b64_tr_b16 v[0:1], v18 offset:32768\n"
    "ds_read_b64_tr_b16 v[2:3], v18 offset:34816\n"
    "v_mfma_f32_16x16x32_bf16 a[48:51], v[164:167], v[64:67], a[48:51]\n"
    "v_exp_f32 v234, v234\n"
    "v_mfma_f32_16x16x32_bf16 a[48:51], v[164:167], v[88:91], a[48:51]\n"
    "ds_read_b64_tr_b16 v[4:5], v18 offset:36864\n"
    "ds_read_b64_tr_b16 v[6:7], v18 offset:38912\n"
    "v_mfma_f32_16x16x32_bf16 a[56:59], v[164:167], v[68:71], a[56:59]\n"
    "v_fma_f32 v238, v238, s62, v28\n"
    "v_fma_f32 v239, v239, s62, v28\n"
    "v_mfma_f32_16x16x32_bf16 a[56:59], v[164:167], v[92:95], a[56:59]\n"
    "buffer_load_dwordx4 v31, s[48:51], s36 offen offset:2048 nt lds\n"
    "v_mfma_f32_16x16x32_bf16 a[64:67], v[164:167], v[72:75], a[64:67]\n"
    "v_exp_f32 v235, v235\n"
    "v_mfma_f32_16x16x32_bf16 a[64:67], v[164:167], v[96:99], a[64:67]\n"
    "v_exp_f32 v236, v236\n"
    "v_mfma_f32_16x16x32_bf16 a[72:75], v[164:167], v[76:79], a[72:75]\n"
    "v_cvt_pk_bf16_f32 v233, v234, v235\n"
    "v_fma_f32 v240, v240, s62, v29\n"
    "v_mfma_f32_16x16x32_bf16 a[72:75], v[164:167], v[100:103], a[72:75]\n"
    "s_memrealtime s[92:93]\n"
    "v_mfma_f32_16x16x32_bf16 a[80:83], v[164:167], v[80:83], a[80:83]\n"
    "v_exp_f32 v237, v237\n"
    "v_mfma_f32_16x16x32_bf16 a[80:83], v[164:167], v[104:107], a[80:83]\n"
    "v_exp_f32 v238, v238\n"
    "v_mfma_f32_16x16x32_bf16 a[88:91], v[164:167], v[84:87], a[88:91]\n"
    "v_fma_f32 v241, v241, s62, v29\n"
    "v_cvt_pk_bf16_f32 v234, v236, v237\n"
    "v_mfma_f32_16x16x32_bf16 a[88:91], v[164:167], v[108:111], a[88:91]\n"
    "s_waitcnt lgkmcnt(0)\n"
    "ds_read_b64_tr_b16 v[8:9], v18 offset:32776\n"
    "ds_read_b64_tr_b16 v[10:11], v18 offset:34824\n"
    "v_mfma_f32_16x16x32_bf16 a[0:3], v[0:3], v[112:115], a[0:3]\n"
    "v_exp_f32 v239, v239\n"
    "v_mfma_f32_16x16x32_bf16 a[0:3], v[4:7], v[136:139], a[0:3]\n"
    "s_add_u32 s94, s94, 220\n"
    "s_sub_u32 s95, s94, s92\n"
    "s_cmp_gt_i32 s95, 3\n"
    "s_cbranch_scc1 L_pc6_%=\n"
    "L_pr6_%=:\n"
    "ds_read_b64_tr_b16 v[12:13], v18 offset:36872\n"
    "ds_read_b64_tr_b16 v[14:15], v18 offset:38920\n"
    "v_mfma_f32_16x16x32_bf16 a[8:11], v[0:3], v[116:119], a[8:11]\n"
    "v_fma_f32 v242, v242, s62, v29\n"
    "v_fma_f32 v243, v243, s62, v29\n"
    "v_mfma_f32_16x16x32_bf16 a[8:11], v[4:7], v[140:143], a[8:11]\n"
    "s_add_u32 m0, s60, 73728\n"
    "v_mfma_f32_16x16x32_bf16 a[16:19], v[0:3], v[120:123], a[16:19]\n"
    "v_exp_f32 v240, v240\n"
    "v_mfma_f32_16x16x32_bf16 a[16:19], v[4:7], v[144:147], a[16:19]\n"
    "s_add_u32 s36, s71, 73984\n"
    "v_mfma_f32_16x16x32_bf16 a[24:27], v[0:3], v[124:127], a[24:27]\n"
    "v_cvt_pk_bf16_f32 v235, v238, v239\n"
    "v_fma_f32 v244, v244, s62, v29\n"
    "v_mfma_f32_16x16x32_bf16 a[24:27], v[4:7], v[148:151], a[24:27]\n"
    "buffer_load_dwordx4 v20, s[48:51], s36 offen nt lds\n"
    "v_mfma_f32_16x16x32_bf16 a[32:35], v[0:3], v[128:131], a[32:35]\n"
    "v_exp_f32 v241, v241\n"
    "v_mfma_f32_16x16x32_bf16 a[32:35], v[4:7], v[152:155], a[32:35]\n"
    "v_exp_f32 v242, v242\n"
    "v_mfma_f32_16x16x32_bf16 a[40:43], v[0:3], v[132:135], a[40:43]\n"
    "v_fma_f32 v245, v245, s62, v29\n"
    "v_cvt_pk_bf16_f32 v240, v240, v241\n"
    "v_mfma_f32_16x16x32_bf16 a[40:43], v[4:7], v[156:159], a[40:43]\n"
    "s_waitcnt lgkmcnt(0)\n"
    "v_exp_f32 v243, v243\n"
    "v_mfma_f32_16x16x32_bf16 a[4:7], v[8:11], v[112:115], a[4:7]\n"
    "v_exp_f32 v244, v244\n"
    "v_mfma_f32_16x16x32_bf16 a[4:7], v[12:15], v[136:139], a[4:7]\n"
    "v_fma_f32 v246, v246, s62, v29\n"
    "v_fma_f32 v247, v247, s62, v29\n"
    "v_mfma_f32_16x16x32_bf16 a[12:15], v[8:11], v[116:119], a[12:15]\n"
    "v_cvt_pk_bf16_f32 v241, v242, v243\n"
    "v_fma_f32 v248, v248, s62, v30\n"
    "v_mfma_f32_16x16x32_bf16 a[12:15], v[12:15], v[140:143], a[12:15]\n"
    "v_exp_f32 v245, v245\n"
    "v_mfma_f32_16x16x32_bf16 a[20:23], v[8:11], v[120:123], a[20:23]\n"
    "v_exp_f32 v246, v246\n"
    "v_mfma_f32_16x16x32_bf16 a[20:23], v[12:15], v[144:147], a[20:23]\n"
    "v_fma_f32 v249, v249, s62, v30\n"
    "v_exp_f32 v247, v247\n"
    "v_mfma_f32_16x16x32_bf16 a[28:31], v[8:11], v[124:127], a[28:31]\n"
    "v_cvt_pk_bf16_f32 v242, v244, v245\n"
    "v_fma_f32 v250, v250, s62, v30\n"
    "v_mfma_f32_16x16x32_bf16 a[28:31], v[12:15], v[148:151], a[28:31]\n"
    "v_exp_f32 v248, v248\n"
    "v_mfma_f32_16x16x32_bf16 a[36:39], v[8:11], v[128:131], a[36:39]\n"
    "v_exp_f32 v249, v249\n"
    "v_mfma_f32_16x16x32_bf16 a[36:39], v[12:15], v[152:155], a[36:39]\n"
    "v_fma_f32 v251, v251, s62, v30\n"
    "v_cvt_pk_bf16_f32 v243, v246, v247\n"
    "v_mfma_f32_16x16x32_bf16 a[44:47], v[8:11], v[132:135], a[44:47]\n"
    "v_fma_f32 v252, v252, s62, v30\n"
    "v_fma_f32 v253, v253, s62, v30\n"
    "v_mfma_f32_16x16x32_bf16 a[44:47], v[12:15], v[156:159], a[44:47]\n"
    "v_exp_f32 v250, v250\n"
    "v_mfma_f32_16x16x32_bf16 a[48:51], v[164:167], v[112:115], a[48:51]\n"
    "v_exp_f32 v251, v251\n"
    "v_mfma_f32_16x16x32_bf16 a[48:51], v[164:167], v[136:139], a[48:51]\n"
    "buffer_load_dwordx4 v21, s[48:51], s36 offen offset:1024 nt lds\n"
    "v_mfma_f32_16x16x32_bf16 a[56:59], v[164:167], v[116:119], a[56:59]\n"
    "v_cvt_pk_bf16_f32 v248, v248, v249\n"
    "v_fma_f32 v254, v254, s62, v30\n"
    "v_mfma_f32_16x16x32_bf16 a[56:59], v[164:167], v[140:143], a[56:59]\n"
    "v_exp_f32 v252, v252\n"
    "v_mfma_f32_16x16x32_bf16 a[64:67], v[164:167], v[120:123], a[64:67]\n"
    "v_exp_f32 v253, v253\n"
    "v_mfma_f32_16x16x32_bf16 a[64:67], v[164:167], v[144:147], a[64:67]\n"
    "v_fma_f32 v255, v255, s62, v30\n"
    "v_cvt_pk_bf16_f32 v249, v250, v251\n"
    "v_mfma_f32_16x16x32_bf16 a[72:75], v[164:167], v[124:127], a[72:75]\n"
    "v_exp_f32 v254, v254\n"
    "v_mfma_f32_16x16x32_bf16 a[72:75], v[164:167], v[148:151], a[72:75]\n"
    "v_exp_f32 v255, v255\n"
    "v_cvt_pk_bf16_f32 v250, v252, v253\n"
    "v_cvt_pk_bf16_f32 v251, v254, v255\n"
    "v_mfma_f32_16x16x32_bf16 a[80:83], v[164:167], v[128:131], a[80:83]\n"
    "v_mfma_f32_16x16x32_bf16 a[80:83], v[164:167], v[152:155], a[80:83]\n"
    "v_mfma_f32_16x16x32_bf16 a[88:91], v[164:167], v[132:135], a[88:91]\n"
    "v_mfma_f32_16x16x32_bf16 a[88:91], v[164:167], v[156:159], a[88:91]\n"
    "ds_read_b64_tr_b16 v[0:1], v18 offset:40960\n"
    "ds_read_b64_tr_b16 v[2:3], v18 offset:43008\n"
    "v_mfma_f32_16x16x32_bf16 a[48:51], v[164:167], v[160:163], a[48:51]\n"
    "v_mfma_f32_16x16x32_bf16 a[48:51], v[164:167], v[208:211], a[48:51]\n"
    "ds_read_b64_tr_b16 v[4:5], v18 offset:45056\n"
    "ds_read_b64_tr_b16 v[6:7], v18 offset:47104\n"
    "v_mfma_f32_16x16x32_bf16 a[56:59], v[164:167], v[168:171], a[56:59]\n"
    "v_mfma_f32_16x16x32_bf16 a[56:59], v[164:167], v[216:219], a[56:59]\n"
    "v_mfma_f32_16x16x32_bf16 a[64:67], v[164:167], v[176:179], a[64:67]\n"
    "v_mfma_f32_16x16x32_bf16 a[64:67], v[164:167], v[224:227], a[64:67]\n"
    "v_mfma_f32_16x16x32_bf16 a[72:75], v[164:167], v[184:187], a[72:75]\n"
    "v_mfma_f32_16x16x32_bf16 a[72:75], v[164:167], v[232:235], a[72:75]\n"
    "buffer_load_dwordx4 v31, s[48:51], s36 offen offset:2048 nt lds\n"
    "v_mfma_f32_16x16x32_bf16 a[80:83], v[164:167], v[192:195], a[80:83]\n"
    "v_mfma_f32_16x16x32_bf16 a[80:83], v[164:167], v[240:243], a[80:83]\n"
    "v_mfma_f32_16x16x32_bf16 a[88:91], v[164:167], v[200:203], a[88:91]\n"
    "v_mfma_f32_16x16x32_bf16 a[88:91], v[164:167], v[248:251], a[88:91]\n"
    "s_waitcnt lgkmcnt(0)\n"
    "ds_read_b64_tr_b16 v[8:9], v18 offset:40968\n"
    "ds_read_b64_tr_b16 v[10:11], v18 offset:43016\n"
    "v_mfma_f32_16x16x32_bf16 a[0:3], v[0:3], v[160:163], a[0:3]\n"
    "v_mfma_f32_16x16x32_bf16 a[0:3], v[4:7], v[208:211], a[0:3]\n"
    "ds_read_b64_tr_b16 v[12:13], v18 offset:45064\n"
    "ds_read_b64_tr_b16 v[14:15], v18 offset:47112\n"
    "v_mfma_f32_16x16x32_bf16 a[8:11], v[0:3], v[168:171], a[8:11]\n"
    "v_accvgpr_read_b32 v180, a48\n"
    "v_accvgpr_read_b32 v181, a56\n"
    "v_mfma_f32_16x16x32_bf16 a[8:11], v[4:7], v[216:219], a[8:11]\n"
    "v_mfma_f32_16x16x32_bf16 a[16:19], v[0:3], v[176:179], a[16:19]\n"
    "v_rcp_f32 v25, v180\n"
    "v_mfma_f32_16x16x32_bf16 a[16:19], v[4:7], v[224:227], a[16:19]\n"
    "v_mfma_f32_16x16x32_bf16 a[24:27], v[0:3], v[184:187], a[24:27]\n"
    "v_accvgpr_read_b32 v182, a64\n"
    "v_accvgpr_read_b32 v183, a72\n"
    "v_mfma_f32_16x16x32_bf16 a[24:27], v[4:7], v[232:235], a[24:27]\n"
    "v_mfma_f32_16x16x32_bf16 a[32:35], v[0:3], v[192:195], a[32:35]\n"
    "v_rcp_f32 v26, v181\n"
    "v_mfma_f32_16x16x32_bf16 a[32:35], v[4:7], v[240:243], a[32:35]\n"
    "v_mfma_f32_16x16x32_bf16 a[40:43], v[0:3], v[200:203], a[40:43]\n"
    "v_xor_b32 v18, 64, v18\n"
    "v_accvgpr_read_b32 v188, a80\n"
    "v_mfma_f32_16x16x32_bf16 a[40:43], v[4:7], v[248:251], a[40:43]\n"
    "s_waitcnt lgkmcnt(0)\n"
    "ds_read_b64_tr_b16 v[0:1], v18 offset:0\n"
    "ds_read_b64_tr_b16 v[2:3], v18 offset:2048\n"
    "s_add_u32 m0, s60, 86016\n"
    "v_mfma_f32_16x16x32_bf16 a[4:7], v[8:11], v[160:163], a[4:7]\n"
    "v_rcp_f32 v27, v182\n"
    "v_mfma_f32_16x16x32_bf16 a[4:7], v[12:15], v[208:211], a[4:7]\n"
    "ds_read_b64_tr_b16 v[4:5], v18 offset:4096\n"
    "ds_read_b64_tr_b16 v[6:7], v18 offset:6144\n"
    "s_add_u32 s36, s71, 110848\n"
    "v_mfma_f32_16x16x32_bf16 a[12:15], v[8:11], v[168:171], a[12:15]\n"
    "v_accvgpr_read_b32 v189, a88\n"
    "v_add_f32 v180, v180, v181\n"
    "v_mfma_f32_16x16x32_bf16 a[12:15], v[12:15], v[216:219], a[12:15]\n"
    "buffer_load_dwordx4 v20, s[48:51], s36 offen nt lds\n"
    "v_mfma_f32_16x16x32_bf16 a[20:23], v[8:11], v[176:179], a[20:23]\n"
    "v_rcp_f32 v28, v183\n"
    "v_mfma_f32_16x16x32_bf16 a[20:23], v[12:15], v[224:227], a[20:23]\n"
    "v_mfma_f32_16x16x32_bf16 a[28:31], v[8:11], v[184:187], a[28:31]\n"
    "v_add_f32 v180, v180, v182\n"
    "v_add_f32 v180, v180, v183\n"
    "v_mfma_f32_16x16x32_bf16 a[28:31], v[12:15], v[232:235], a[28:31]\n"
    "v_mfma_f32_16x16x32_bf16 a[36:39], v[8:11], v[192:195], a[36:39]\n"
    "v_rcp_f32 v29, v188\n"
    "v_mfma_f32_16x16x32_bf16 a[36:39], v[12:15], v[240:243], a[36:39]\n"
    "v_mfma_f32_16x16x32_bf16 a[44:47], v[8:11], v[200:203], a[44:47]\n"
    "v_add_f32 v180, v180, v188\n"
    "v_add_f32 v180, v180, v189\n"
    "v_mfma_f32_16x16x32_bf16 a[44:47], v[12:15], v[248:251], a[44:47]\n"
    "s_waitcnt lgkmcnt(0)\n"
    "ds_read_b64_tr_b16 v[8:9], v18 offset:8\n"
    "ds_read_b64_tr_b16 v[10:11], v18 offset:2056\n"
    "v_mfma_f32_16x16x32_bf16 a[48:51], v[0:3], a[144:147], 0\n"
    "v_rcp_f32 v30, v189\n"
    "v_mfma_f32_16x16x32_bf16 a[48:51], v[4:7], a[168:171], a[48:51]\n"
    "ds_read_b64_tr_b16 v[12:13], v18 offset:4104\n"
    "ds_read_b64_tr_b16 v[14:15], v18 offset:6152\n"
    "v_mfma_f32_16x16x32_bf16 a[56:59], v[0:3], a[148:151], 0\n"
    "v_cmp_nle_f32 vcc, v180, s63\n"
    "v_mfma_f32_16x16x32_bf16 a[56:59], v[4:7], a[172:175], a[56:59]\n"
    "s_nop 3\n"
    "s_cmp_lg_u64 vcc, 0\n"
    "s_cselect_b32 s66, s67, 0\n"
    "s_or_b32 s65, s65, s66\n"
    "v_mfma_f32_16x16x32_bf16 a[64:67], v[0:3], a[152:155], 0\n"
    "v_accvgpr_read_b32 v164, a0\n"
    "v_accvgpr_read_b32 v165, a1\n"
    "v_mfma_f32_16x16x32_bf16 a[64:67], v[4:7], a[176:179], a[64:67]\n"
    "v_accvgpr_read_b32 v166, a2\n"
    "v_mfma_f32_16x16x32_bf16 a[72:75], v[0:3], a[156:159], 0\n"
    "v_accvgpr_read_b32 v167, a3\n"
    "v_mul_f32 v164, v164, v25\n"
    "v_mfma_f32_16x16x32_bf16 a[72:75], v[4:7], a[180:183], a[72:75]\n"
    "buffer_load_dwordx4 v21, s[48:51], s36 offen offset:1024 nt lds\n"
    "v_mfma_f32_16x16x32_bf16 a[80:83], v[0:3], a[160:163], 0\n"
    "v_mul_f32 v165, v165, v25\n"
    "v_mul_f32 v166, v166, v25\n"
    "v_mfma_f32_16x16x32_bf16 a[80:83], v[4:7], a[184:187], a[80:83]\n"
    "v_mul_f32 v167, v167, v25\n"
    "v_mfma_f32_16x16x32_bf16 a[88:91], v[0:3], a[164:167], 0\n"
    "v_cvt_pk_bf16_f32 v212, v164, v165\n"
    "v_cvt_pk_bf16_f32 v213, v166, v167\n"
    "v_mfma_f32_16x16x32_bf16 a[88:91], v[4:7], a[188:191], a[88:91]\n"
    "s_waitcnt lgkmcnt(0)\n"
    "ds_read_b64_tr_b16 v[0:1], v18 offset:8192\n"
    "ds_read_b64_tr_b16 v[2:3], v18 offset:10240\n"
    "v_mfma_f32_16x16x32_bf16 a[52:55], v[8:11], a[144:147], 0\n"
    "v_accvgpr_read_b32 v172, a4\n"
    "v_accvgpr_read_b32 v173, a5\n"
    "v_mfma_f32_16x16x32_bf16 a[52:55], v[12:15], a[168:171], a[52:55]\n"
    "ds_read_b64_tr_b16 v[4:5], v18 offset:12288\n"
    "ds_read_b64_tr_b16 v[6:7], v18 offset:14336\n"
    "v_mfma_f32_16x16x32_bf16 a[60:63], v[8:11], a[148:151], 0\n"
    "v_accvgpr_read_b32 v174, a6\n"
    "v_accvgpr_read_b32 v175, a7\n"
    "v_mfma_f32_16x16x32_bf16 a[60:63], v[12:15], a[172:175], a[60:63]\n"
    "v_mul_f32 v172, v172, v25\n"
    "v_mfma_f32_16x16x32_bf16 a[68:71], v[8:11], a[152:155], 0\n"
    "v_mul_f32 v173, v173, v25\n"
    "v_mul_f32 v174, v174, v25\n"
    "v_mfma_f32_16x16x32_bf16 a[68:71], v[12:15], a[176:179], a[68:71]\n"
    "v_mul_f32 v175, v175, v25\n"
    "v_mfma_f32_16x16x32_bf16 a[76:79], v[8:11], a[156:159], 0\n"
    "v_cvt_pk_bf16_f32 v214, v172, v173\n"
    "v_cvt_pk_bf16_f32 v215, v174, v175\n"
    "v_mfma_f32_16x16x32_bf16 a[76:79], v[12:15], a[180:183], a[76:79]\n"
    "v_accvgpr_read_b32 v164, a8\n"
    "v_mfma_f32_16x16x32_bf16 a[84:87], v[8:11], a[160:163], 0\n"
    "v_accvgpr_read_b32 v165, a9\n"
    "v_accvgpr_read_b32 v166, a10\n"
    "v_mfma_f32_16x16x32_bf16 a[84:87], v[12:15], a[184:187], a[84:87]\n"
    "v_accvgpr_read_b32 v167, a11\n"
    "v_mfma_f32_16x16x32_bf16 a[92:95], v[8:11], a[164:167], 0\n"
    "v_mul_f32 v164, v164, v26\n"
    "v_mul_f32 v165, v165, v26\n"
    "v_mfma_f32_16x16x32_bf16 a[92:95], v[12:15], a[188:191], a[92:95]\n"
    "s_waitcnt lgkmcnt(0)\n"
    "ds_read_b64_tr_b16 v[8:9], v18 offset:8200\n"
    "ds_read_b64_tr_b16 v[10:11], v18 offset:10248\n"
    "v_mfma_f32_16x16x32_bf16 a[48:51], v[0:3], a[192:195], a[48:51]\n"
    "v_mul_f32 v166, v166, v26\n"
    "v_mul_f32 v167, v167, v26\n"
    "v_mfma_f32_16x16x32_bf16 a[48:51], v[4:7], a[216:219], a[48:51]\n"
    "ds_read_b64_tr_b16 v[12:13], v18 offset:12296\n"
    "ds_read_b64_tr_b16 v[14:15], v18 offset:14344\n"
    "v_mfma_f32_16x16x32_bf16 a[56:59], v[0:3], a[196:199], a[56:59]\n"
    "v_cvt_pk_bf16_f32 v220, v164, v165\n"
    "v_cvt_pk_bf16_f32 v221, v166, v167\n"
    "v_mfma_f32_16x16x32_bf16 a[56:59], v[4:7], a[220:223], a[56:59]\n"
    "buffer_load_dwordx4 v31, s[48:51], s36 offen offset:2048 nt lds\n"
    "v_mfma_f32_16x16x32_bf16 a[64:67], v[0:3], a[200:203], a[64:67]\n"
    "v_accvgpr_read_b32 v172, a12\n"
    "v_accvgpr_read_b32 v173, a13\n"
    "v_mfma_f32_16x16x32_bf16 a[64:67], v[4:7], a[224:227], a[64:67]\n"
    "v_accvgpr_read_b32 v174, a14\n"
    "v_mfma_f32_16x16x32_bf16 a[72:75], v[0:3], a[204:207], a[72:75]\n"
    "v_accvgpr_read_b32 v175, a15\n"
    "v_mul_f32 v172, v172, v26\n"
    "v_mfma_f32_16x16x32_bf16 a[72:75], v[4:7], a[228:231], a[72:75]\n"
    "v_mul_f32 v173, v173, v26\n"
    "v_mfma_f32_16x16x32_bf16 a[80:83], v[0:3], a[208:211], a[80:83]\n"
    "v_mul_f32 v174, v174, v26\n"
    "v_mul_f32 v175, v175, v26\n"
    "v_mfma_f32_16x16x32_bf16 a[80:83], v[4:7], a[232:235], a[80:83]\n"
    "v_cvt_pk_bf16_f32 v222, v172, v173\n"
    "v_mfma_f32_16x16x32_bf16 a[88:91], v[0:3], a[212:215], a[88:91]\n"
    "v_cvt_pk_bf16_f32 v223, v174, v175\n"
    "v_accvgpr_read_b32 v164, a16\n"
    "v_mfma_f32_16x16x32_bf16 a[88:91], v[4:7], a[236:239], a[88:91]\n"
    "s_waitcnt lgkmcnt(0)\n"
    "ds_read_b64_tr_b16 v[0:1], v18 offset:16384\n"
    "ds_read_b64_tr_b16 v[2:3], v18 offset:18432\n"
    "v_mfma_f32_16x16x32_bf16 a[52:55], v[8:11], a[192:195], a[52:55]\n"
    "v_accvgpr_read_b32 v165, a17\n"
    "v_accvgpr_read_b32 v166, a18\n"
    "v_mfma_f32_16x16x32_bf16 a[52:55], v[12:15], a[216:219], a[52:55]\n"
    "ds_read_b64_tr_b16 v[4:5], v18 offset:20480\n"
    "ds_read_b64_tr_b16 v[6:7], v18 offset:22528\n"
    "v_mfma_f32_16x16x32_bf16 a[60:63], v[8:11], a[196:199], a[60:63]\n"
    "v_accvgpr_read_b32 v167, a19\n"
    "v_mul_f32 v164, v164, v27\n"
    "v_mfma_f32_16x16x32_bf16 a[60:63], v[12:15], a[220:223], a[60:63]\n"
    "v_mfma_f32_16x16x32_bf16 a[68:71], v[8:11], a[200:203], a[68:71]\n"
    "v_mul_f32 v165, v165, v27\n"
    "v_mul_f32 v166, v166, v27\n"
    "v_mfma_f32_16x16x32_bf16 a[68:71], v[12:15], a[224:227], a[68:71]\n"
    "v_mul_f32 v167, v167, v27\n"
    "v_mfma_f32_16x16x32_bf16 a[76:79], v[8:11], a[204:207], a[76:79]\n"
    "v_cvt_pk_bf16_f32 v228, v164, v165\n"
    "v_cvt_pk_bf16_f32 v229, v166, v167\n"
    "v_mfma_f32_16x16x32_bf16 a[76:79], v[12:15], a[228:231], a[76:79]\n"
    "v_mfma_f32_16x16x32_bf16 a[84:87], v[8:11], a[208:211], a[84:87]\n"
    "v_accvgpr_read_b32 v172, a20\n"
    "v_accvgpr_read_b32 v173, a21\n"
    "v_mfma_f32_16x16x32_bf16 a[84:87], v[12:15], a[232:235], a[84:87]\n"
    "v_accvgpr_read_b32 v174, a22\n"
    "v_mfma_f32_16x16x32_bf16 a[92:95], v[8:11], a[212:215], a[92:95]\n"
    "v_accvgpr_read_b32 v175, a23\n"
    "v_mul_f32 v172, v172, v27\n"
    "v_mfma_f32_16x16x32_bf16 a[92:95], v[12:15], a[236:239], a[92:95]\n"
    "s_waitcnt lgkmcnt(0)\n"
    "ds_read_b64_tr_b16 v[8:9], v18 offset:16392\n"
    "ds_read_b64_tr_b16 v[10:11], v18 offset:18440\n"
    "v_mfma_f32_16x16x32_bf16 a[48:51], v[0:3], a[240:243], a[48:51]\n"
    "v_mul_f32 v173, v173, v27\n"
    "v_mul_f32 v174, v174, v27\n"
    "v_mfma_f32_16x16x32_bf16 a[48:51], v[4:7], v[40:43], a[48:51]\n"
    "ds_read_b64_tr_b16 v[12:13], v18 offset:20488\n"
    "ds_read_b64_tr_b16 v[14:15], v18 offset:22536\n"
    "v_mfma_f32_16x16x32_bf16 a[56:59], v[0:3], a[244:247], a[56:59]\n"
    "v_mul_f32 v175, v175, v27\n"
    "v_cvt_pk_bf16_f32 v230, v172, v173\n"
    "v_mfma_f32_16x16x32_bf16 a[56:59], v[4:7], v[44:47], a[56:59]\n"
    "v_mfma_f32_16x16x32_bf16 a[64:67], v[0:3], a[248:251], a[64:67]\n"
    "v_cvt_pk_bf16_f32 v231, v174, v175\n"
    "v_accvgpr_read_b32 v164, a24\n"
    "v_mfma_f32_16x16x32_bf16 a[64:67], v[4:7], v[48:51], a[64:67]\n"
    "v_mfma_f32_16x16x32_bf16 a[72:75], v[0:3], a[252:255], a[72:75]\n"
    "v_accvgpr_read_b32 v165, a25\n"
    "v_accvgpr_read_b32 v166, a26\n"
    "v_mfma_f32_16x16x32_bf16 a[72:75], v[4:7], v[52:55], a[72:75]\n"
    "v_mfma_f32_16x16x32_bf16 a[80:83], v[0:3], v[32:35], a[80:83]\n"
    "v_accvgpr_read_b32 v167, a27\n"
    "v_mul_f32 v164, v164, v28\n"
    "v_mfma_f32_16x16x32_bf16 a[80:83], v[4:7], v[56:59], a[80:83]\n"
    "s_waitcnt lgkmcnt(0)\n"
    "s_barrier\n"
    "v_mfma_f32_16x16x32_bf16 a[88:91], v[0:3], v[36:39], a[88:91]\n"
    "v_mul_f32 v165, v165, v28\n"
    "v_mul_f32 v166, v166, v28\n"
    "v_mfma_f32_16x16x32_bf16 a[88:91], v[4:7], v[60:63], a[88:91]\n"
    "ds_read_b64_tr_b16 v[0:1], v18 offset:24576\n"
    "ds_read_b64_tr_b16 v[2:3], v18 offset:26624\n"
    "s_add_u32 m0, s60, 24576\n"
    "v_mfma_f32_16x16x32_bf16 a[52:55], v[8:11], a[240:243], a[52:55]\n"
    "v_mul_f32 v167, v167, v28\n"
    "v_cvt_pk_bf16_f32 v236, v164, v165\n"
    "v_mfma_f32_16x16x32_bf16 a[52:55], v[12:15], v[40:43], a[52:55]\n"
    "ds_read_b64_tr_b16 v[4:5], v18 offset:28672\n"
    "ds_read_b64_tr_b16 v[6:7], v18 offset:30720\n"
    "s_add_u32 s36, s61, 12288\n"
    "v_mfma_f32_16x16x32_bf16 a[60:63], v[8:11], a[244:247], a[60:63]\n"
    "v_cvt_pk_bf16_f32 v237, v166, v167\n"
    "v_accvgpr_read_b32 v172, a28\n"
    "v_mfma_f32_16x16x32_bf16 a[60:63], v[12:15], v[44:47], a[60:63]\n"
    "buffer_load_dwordx4 v20, s[40:43], s36 offen nt lds\n"
    "v_mfma_f32_16x16x32_bf16 a[68:71], v[8:11], a[248:251], a[68:71]\n"
    "v_accvgpr_read_b32 v173, a29\n"
    "v_accvgpr_read_b32 v174, a30\n"
    "v_mfma_f32_16x16x32_bf16 a[68:71], v[12:15], v[48:51], a[68:71]\n"
    "v_mfma_f32_16x16x32_bf16 a[76:79], v[8:11], a[252:255], a[76:79]\n"
    "v_accvgpr_read_b32 v175, a31\n"
    "v_mul_f32 v172, v172, v28\n"
    "v_mfma_f32_16x16x32_bf16 a[76:79], v[12:15], v[52:55], a[76:79]\n"
    "v_mfma_f32_16x16x32_bf16 a[84:87], v[8:11], v[32:35], a[84:87]\n"
    "v_mul_f32 v173, v173, v28\n"
    "v_mul_f32 v174, v174, v28\n"
    "v_mfma_f32_16x16x32_bf16 a[84:87], v[12:15], v[56:59], a[84:87]\n"
    "v_mfma_f32_16x16x32_bf16 a[92:95], v[8:11], v[36:39], a[92:95]\n"
    "v_mul_f32 v175, v175, v28\n"
    "v_cvt_pk_bf16_f32 v238, v172, v173\n"
    "v_mfma_f32_16x16x32_bf16 a[92:95], v[12:15], v[60:63], a[92:95]\n"
    "s_waitcnt lgkmcnt(0)\n"
    "ds_read_b64_tr_b16 v[8:9], v18 offset:24584\n"
    "ds_read_b64_tr_b16 v[10:11], v18 offset:26632\n"
    "v_mfma_f32_16x16x32_bf16 a[48:51], v[0:3], v[64:67], a[48:51]\n"
    "v_cvt_pk_bf16_f32 v239, v174, v175\n"
    "v_accvgpr_read_b32 v164, a32\n"
    "v_mfma_f32_16x16x32_bf16 a[48:51], v[4:7], v[88:91], a[48:51]\n"
    "ds_read_b64_tr_b16 v[12:13], v18 offset:28680\n"
    "ds_read_b64_tr_b16 v[14:15], v18 offset:30728\n"
    "v_mfma_f32_16x16x32_bf16 a[56:59], v[0:3], v[68:71], a[56:59]\n"
    "v_accvgpr_read_b32 v165, a33\n"
    "v_accvgpr_read_b32 v166, a34\n"
    "v_mfma_f32_16x16x32_bf16 a[56:59], v[4:7], v[92:95], a[56:59]\n"
    "buffer_load_dwordx4 v22, s[40:43], s36 offen offset:1024 nt lds\n"
    "v_mfma_f32_16x16x32_bf16 a[64:67], v[0:3], v[72:75], a[64:67]\n"
    "v_accvgpr_read_b32 v167, a35\n"
    "v_mul_f32 v164, v164, v29\n"
    "v_mfma_f32_16x16x32_bf16 a[64:67], v[4:7], v[96:99], a[64:67]\n"
    "v_mfma_f32_16x16x32_bf16 a[72:75], v[0:3], v[76:79], a[72:75]\n"
    "v_mul_f32 v165, v165, v29\n"
    "v_mul_f32 v166, v166, v29\n"
    "v_mfma_f32_16x16x32_bf16 a[72:75], v[4:7], v[100:103], a[72:75]\n"
    "v_mfma_f32_16x16x32_bf16 a[80:83], v[0:3], v[80:83], a[80:83]\n"
    "v_mul_f32 v167, v167, v29\n"
    "v_cvt_pk_bf16_f32 v244, v164, v165\n"
    "v_mfma_f32_16x16x32_bf16 a[80:83], v[4:7], v[104:107], a[80:83]\n"
    "v_mfma_f32_16x16x32_bf16 a[88:91], v[0:3], v[84:87], a[88:91]\n"
    "v_cvt_pk_bf16_f32 v245, v166, v167\n"
    "v_accvgpr_read_b32 v172, a36\n"
    "v_mfma_f32_16x16x32_bf16 a[88:91], v[4:7], v[108:111], a[88:91]\n"
    "s_waitcnt lgkmcnt(0)\n"
    "ds_read_b64_tr_b16 v[0:1], v18 offset:32768\n"
    "ds_read_b64_tr_b16 v[2:3], v18 offset:34816\n"
    "v_mfma_f32_16x16x32_bf16 a[52:55], v[8:11], v[64:67], a[52:55]\n"
    "v_accvgpr_read_b32 v173, a37\n"
    "v_accvgpr_read_b32 v174, a38\n"
    "v_mfma_f32_16x16x32_bf16 a[52:55], v[12:15], v[88:91], a[52:55]\n"
    "ds_read_b64_tr_b16 v[4:5], v18 offset:36864\n"
    "ds_read_b64_tr_b16 v[6:7], v18 offset:38912\n"
    "v_mfma_f32_16x16x32_bf16 a[60:63], v[8:11], v[68:71], a[60:63]\n"
    "v_accvgpr_read_b32 v175, a39\n"
    "v_mul_f32 v172, v172, v29\n"
    "v_mfma_f32_16x16x32_bf16 a[60:63], v[12:15], v[92:95], a[60:63]\n"
    "buffer_load_dwordx4 v23, s[40:43], s36 offen offset:2048 nt lds\n"
    "v_mfma_f32_16x16x32_bf16 a[68:71], v[8:11], v[72:75], a[68:71]\n"
    "v_mul_f32 v173, v173, v29\n"
    "v_mul_f32 v174, v174, v29\n"
    "v_mfma_f32_16x16x32_bf16 a[68:71], v[12:15], v[96:99], a[68:71]\n"
    "v_mfma_f32_16x16x32_bf16 a[76:79], v[8:11], v[76:79], a[76:79]\n"
    "v_mul_f32 v175, v175, v29\n"
    "v_cvt_pk_bf16_f32 v246, v172, v173\n"
    "v_mfma_f32_16x16x32_bf16 a[76:79], v[12:15], v[100:103], a[76:79]\n"
    "s_memrealtime s[92:93]\n"
    "v_mfma_f32_16x16x32_bf16 a[84:87], v[8:11], v[80:83], a[84:87]\n"
    "v_cvt_pk_bf16_f32 v247, v174, v175\n"
    "v_accvgpr_read_b32 v164, a40\n"
    "v_mfma_f32_16x16x32_bf16 a[84:87], v[12:15], v[104:107], a[84:87]\n"
    "v_mfma_f32_16x16x32_bf16 a[92:95], v[8:11], v[84:87], a[92:95]\n"
    "v_accvgpr_read_b32 v165, a41\n"
    "v_accvgpr_read_b32 v166, a42\n"
    "v_mfma_f32_16x16x32_bf16 a[92:95], v[12:15], v[108:111], a[92:95]\n"
    "s_waitcnt lgkmcnt(0)\n"
    "ds_read_b64_tr_b16 v[8:9], v18 offset:32776\n"
    "ds_read_b64_tr_b16 v[10:11], v18 offset:34824\n"
    "s_add_u32 m0, s60, 36864\n"
    "v_mfma_f32_16x16x32_bf16 a[48:51], v[0:3], v[112:115], a[48:51]\n"
    "v_accvgpr_read_b32 v167, a43\n"
    "v_mul_f32 v164, v164, v30\n"
    "v_mfma_f32_16x16x32_bf16 a[48:51], v[4:7], v[136:139], a[48:51]\n"
    "s_add_u32 s94, s94, 201\n"
    "s_sub_u32 s95, s94, s92\n"
    "s_cmp_gt_i32 s95, 3\n"
    "s_cbranch_scc1 L_pc7_%=\n"
    "L_pr7_%=:\n"
    "ds_read_b64_tr_b16 v[12:13], v18 offset:36872\n"
    "ds_read_b64_tr_b16 v[14:15], v18 offset:38920\n"
    "s_add_u32 s36, s61, 49152\n"
    "v_mfma_f32_16x16x32_bf16 a[56:59], v[0:3], v[116:119], a[56:59]\n"
    "v_mul_f32 v165, v165, v30\n"
    "v_mul_f32 v166, v166, v30\n"
    "v_mfma_f32_16x16x32_bf16 a[56:59], v[4:7], v[140:143], a[56:59]\n"
    "buffer_load_dwordx4 v20, s[40:43], s36 offen nt lds\n"
    "v_mfma_f32_16x16x32_bf16 a[64:67], v[0:3], v[120:123], a[64:67]\n"
    "v_mul_f32 v167, v167, v30\n"
    "v_cvt_pk_bf16_f32 v252, v164, v165\n"
    "v_mfma_f32_16x16x32_bf16 a[64:67], v[4:7], v[144:147], a[64:67]\n"
    "v_mfma_f32_16x16x32_bf16 a[72:75], v[0:3], v[124:127], a[72:75]\n"
    "v_cvt_pk_bf16_f32 v253, v166, v167\n"
    "v_accvgpr_read_b32 v172, a44\n"
    "v_mfma_f32_16x16x32_bf16 a[72:75], v[4:7], v[148:151], a[72:75]\n"
    "v_mfma_f32_16x16x32_bf16 a[80:83], v[0:3], v[128:131], a[80:83]\n"
    "v_accvgpr_read_b32 v173, a45\n"
    "v_accvgpr_read_b32 v174, a46\n"
    "v_mfma_f32_16x16x32_bf16 a[80:83], v[4:7], v[152:155], a[80:83]\n"
    "v_mfma_f32_16x16x32_bf16 a[88:91], v[0:3], v[132:135], a[88:91]\n"
    "v_accvgpr_read_b32 v175, a47\n"
    "v_mul_f32 v172, v172, v30\n"
    "v_mfma_f32_16x16x32_bf16 a[88:91], v[4:7], v[156:159], a[88:91]\n"
    "s_waitcnt lgkmcnt(0)\n"
    "ds_read_b64_tr_b16 v[0:1], v18 offset:40960\n"
    "ds_read_b64_tr_b16 v[2:3], v18 offset:43008\n"
    "v_mfma_f32_16x16x32_bf16 a[52:55], v[8:11], v[112:115], a[52:55]\n"
    "v_mul_f32 v173, v173, v30\n"
    "v_mul_f32 v174, v174, v30\n"
    "v_mfma_f32_16x16x32_bf16 a[52:55], v[12:15], v[136:139], a[52:55]\n"
    "ds_read_b64_tr_b16 v[4:5], v18 offset:45056\n"
    "ds_read_b64_tr_b16 v[6:7], v18 offset:47104\n"
    "v_mfma_f32_16x16x32_bf16 a[60:63], v[8:11], v[116:119], a[60:63]\n"
    "v_mul_f32 v175, v175, v30\n"
    "v_cvt_pk_bf16_f32 v254, v172, v173\n"
    "v_mfma_f32_16x16x32_bf16 a[60:63], v[12:15], v[140:143], a[60:63]\n"
    "buffer_load_dwordx4 v22, s[40:43], s36 offen offset:1024 nt lds\n"
    "v_mfma_f32_16x16x32_bf16 a[68:71], v[8:11], v[120:123], a[68:71]\n"
    "v_cvt_pk_bf16_f32 v255, v174, v175\n"
    "v_mfma_f32_16x16x32_bf16 a[68:71], v[12:15], v[144:147], a[68:71]\n"
    "v_mfma_f32_16x16x32_bf16 a[76:79], v[8:11], v[124:127], a[76:79]\n"
    "v_mfma_f32_16x16x32_bf16 a[76:79], v[12:15], v[148:151], a[76:79]\n"
    "v_mfma_f32_16x16x32_bf16 a[84:87], v[8:11], v[128:131], a[84:87]\n"
    "v_mfma_f32_16x16x32_bf16 a[84:87], v[12:15], v[152:155], a[84:87]\n"
    "v_mfma_f32_16x16x32_bf16 a[92:95], v[8:11], v[132:135], a[92:95]\n"
    "v_mfma_f32_16x16x32_bf16 a[92:95], v[12:15], v[156:159], a[92:95]\n"
    "s_waitcnt lgkmcnt(0)\n"
    "ds_read_b64_tr_b16 v[8:9], v18 offset:40968\n"
    "ds_read_b64_tr_b16 v[10:11], v18 offset:43016\n"
    "v_mfma_f32_16x16x32_bf16 a[48:51], v[0:3], v[160:163], a[48:51]\n"
    "v_mfma_f32_16x16x32_bf16 a[48:51], v[4:7], v[208:211], a[48:51]\n"
    "s_add_u32 s48, s48, 4718592\n"
    "s_addc_u32 s49, s49, 0\n"
    "ds_read_b64_tr_b16 v[12:13], v18 offset:45064\n"
    "ds_read_b64_tr_b16 v[14:15], v18 offset:47112\n"
    "v_mfma_f32_16x16x32_bf16 a[56:59], v[0:3], v[168:171], a[56:59]\n"
    "v_mfma_f32_16x16x32_bf16 a[56:59], v[4:7], v[216:219], a[56:59]\n"
    "buffer_load_dwordx4 v23, s[40:43], s36 offen offset:2048 nt lds\n"
    "v_mfma_f32_16x16x32_bf16 a[64:67], v[0:3], v[176:179], a[64:67]\n"
    "v_mfma_f32_16x16x32_bf16 a[64:67], v[4:7], v[224:227], a[64:67]\n"
    "s_lshl_b32 s67, s67, 1\n"
    "v_mfma_f32_16x16x32_bf16 a[72:75], v[0:3], v[184:187], a[72:75]\n"
    "v_mfma_f32_16x16x32_bf16 a[72:75], v[4:7], v[232:235], a[72:75]\n"
    "v_mfma_f32_16x16x32_bf16 a[80:83], v[0:3], v[192:195], a[80:83]\n"
    "v_mfma_f32_16x16x32_bf16 a[80:83], v[4:7], v[240:243], a[80:83]\n"
    "s_waitcnt vmcnt(18) lgkmcnt(0)\n"
    "s_barrier\n"
    "v_mfma_f32_16x16x32_bf16 a[88:91], v[0:3], v[200:203], a[88:91]\n"
    "v_xor_b32 v18, 64, v18\n"
    "v_add_u32 v18, 73728, v18\n"
    "v_mfma_f32_16x16x32_bf16 a[88:91], v[4:7], v[248:251], a[88:91]\n"
    "ds_read_b64_tr_b16 v[0:1], v18 offset:36864\n"
    "ds_read_b64_tr_b16 v[2:3], v18 offset:38912\n"
    "s_add_u32 m0, s60, 49152\n"
    "v_mfma_f32_16x16x32_bf16 a[52:55], v[8:11], v[160:163], a[52:55]\n"
    "v_mfma_f32_16x16x32_bf16 a[52:55], v[12:15], v[208:211], a[52:55]\n"
    "ds_read_b64_tr_b16 v[4:5], v18 offset:40960\n"
    "ds_read_b64_tr_b16 v[6:7], v18 offset:43008\n"
    "s_add_u32 s36, s61, 86016\n"
    "v_mfma_f32_16x16x32_bf16 a[60:63], v[8:11], v[168:171], a[60:63]\n"
    "v_mfma_f32_16x16x32_bf16 a[60:63], v[12:15], v[216:219], a[60:63]\n"
    "buffer_load_dwordx4 v20, s[40:43], s36 offen nt lds\n"
    "v_mfma_f32_16x16x32_bf16 a[68:71], v[8:11], v[176:179], a[68:71]\n"
    "v_mfma_f32_16x16x32_bf16 a[68:71], v[12:15], v[224:227], a[68:71]\n"
    "v_mfma_f32_16x16x32_bf16 a[76:79], v[8:11], v[184:187], a[76:79]\n"
    "v_mfma_f32_16x16x32_bf16 a[76:79], v[12:15], v[232:235], a[76:79]\n"
    "v_mfma_f32_16x16x32_bf16 a[84:87], v[8:11], v[192:195], a[84:87]\n"
    "v_mfma_f32_16x16x32_bf16 a[84:87], v[12:15], v[240:243], a[84:87]\n"
    "v_mfma_f32_16x16x32_bf16 a[92:95], v[8:11], v[200:203], a[92:95]\n"
    "v_mfma_f32_16x16x32_bf16 a[92:95], v[12:15], v[248:251], a[92:95]\n"
    "s_waitcnt lgkmcnt(0)\n"
    "ds_read_b64_tr_b16 v[8:9], v18 offset:36872\n"
    "ds_read_b64_tr_b16 v[10:11], v18 offset:38920\n"
    "v_mfma_f32_16x16x32_bf16 a[0:3], v[0:3], a[144:147], 0\n"
    "v_mfma_f32_16x16x32_bf16 a[0:3], v[4:7], a[168:171], a[0:3]\n"
    "ds_read_b64_tr_b16 v[12:13], v18 offset:40968\n"
    "ds_read_b64_tr_b16 v[14:15], v18 offset:43016\n"
    "v_mfma_f32_16x16x32_bf16 a[8:11], v[0:3], a[148:151], 0\n"
    "v_accvgpr_read_b32 v164, a48\n"
    "v_accvgpr_read_b32 v165, a49\n"
    "v_mfma_f32_16x16x32_bf16 a[8:11], v[4:7], a[172:175], a[8:11]\n"
    "v_accvgpr_read_b32 v166, a50\n"
    "v_accvgpr_read_b32 v167, a51\n"
    "v_mfma_f32_16x16x32_bf16 a[16:19], v[0:3], a[152:155], 0\n"
    "v_mul_f32 v164, v164, v25\n"
    "v_mul_f32 v165, v165, v25\n"
    "v_mfma_f32_16x16x32_bf16 a[16:19], v[4:7], a[176:179], a[16:19]\n"
    "v_mul_f32 v166, v166, v25\n"
    "v_mul_f32 v167, v167, v25\n"
    "v_mfma_f32_16x16x32_bf16 a[24:27], v[0:3], a[156:159], 0\n"
    "v_cvt_pk_bf16_f32 v180, v164, v165\n"
    "v_cvt_pk_bf16_f32 v181, v166, v167\n"
    "v_mfma_f32_16x16x32_bf16 a[24:27], v[4:7], a[180:183], a[24:27]\n"
    "v_accvgpr_read_b32 v172, a52\n"
    "v_accvgpr_read_b32 v173, a53\n"
    "v_mfma_f32_16x16x32_bf16 a[32:35], v[0:3], a[160:163], 0\n"
    "v_accvgpr_read_b32 v174, a54\n"
    "v_accvgpr_read_b32 v175, a55\n"
    "v_mfma_f32_16x16x32_bf16 a[32:35], v[4:7], a[184:187], a[32:35]\n"
    "v_mul_f32 v172, v172, v25\n"
    "v_mul_f32 v173, v173, v25\n"
    "v_mfma_f32_16x16x32_bf16 a[40:43], v[0:3], a[164:167], 0\n"
    "v_mul_f32 v174, v174, v25\n"
    "v_mul_f32 v175, v175, v25\n"
    "v_mfma_f32_16x16x32_bf16 a[40:43], v[4:7], a[188:191], a[40:43]\n"
    "s_waitcnt lgkmcnt(0)\n"
    "ds_read_b64_tr_b16 v[0:1], v18 offset:45056\n"
    "ds_read_b64_tr_b16 v[2:3], v18 offset:47104\n"
    "v_mfma_f32_16x16x32_bf16 a[4:7], v[8:11], a[144:147], 0\n"
    "v_cvt_pk_bf16_f32 v182, v172, v173\n"
    "v_cvt_pk_bf16_f32 v183, v174, v175\n"
    "v_mfma_f32_16x16x32_bf16 a[4:7], v[12:15], a[168:171], a[4:7]\n"
    "ds_read_b64_tr_b16 v[4:5], v18 offset:0\n"
    "ds_read_b64_tr_b16 v[6:7], v18 offset:2048\n"
    "v_mfma_f32_16x16x32_bf16 a[12:15], v[8:11], a[148:151], 0\n"
    "v_mfma_f32_16x16x32_bf16 a[12:15], v[12:15], a[172:175], a[12:15]\n"
    "buffer_load_dwordx4 v22, s[40:43], s36 offen offset:1024 nt lds\n"
    "v_mfma_f32_16x16x32_bf16 a[20:23], v[8:11], a[152:155], 0\n"
    "v_mfma_f32_16x16x32_bf16 a[20:23], v[12:15], a[176:179], a[20:23]\n"
    "s_mov_b32 s56, 0\n"
    "s_mov_b64 vcc, s[98:99]\n"
    "v_mfma_f32_16x16x32_bf16 a[28:31], v[8:11], a[156:159], 0\n"
    "v_cndmask_b32_dpp v188, v212, v180, vcc row_ror:8 row_mask:0xf bank_mask:0xf\n"
    "v_cndmask_b32_dpp v189, v213, v181, vcc row_ror:8 row_mask:0xf bank_mask:0xf\n"
    "v_mfma_f32_16x16x32_bf16 a[28:31], v[12:15], a[180:183], a[28:31]\n"
    "v_cndmask_b32_dpp v190, v214, v182, vcc row_ror:8 row_mask:0xf bank_mask:0xf\n"
    "v_cndmask_b32_dpp v191, v215, v183, vcc row_ror:8 row_mask:0xf bank_mask:0xf\n"
    "v_mfma_f32_16x16x32_bf16 a[36:39], v[8:11], a[160:163], 0\n"
    "v_accvgpr_read_b32 v164, a56\n"
    "v_accvgpr_read_b32 v165, a57\n"
    "v_mfma_f32_16x16x32_bf16 a[36:39], v[12:15], a[184:187], a[36:39]\n"
    "s_mov_b64 vcc, s[96:97]\n"
    "v_cndmask_b32_dpp v212, v180, v212, vcc row_ror:8 row_mask:0xf bank_mask:0xf\n"
    "v_mfma_f32_16x16x32_bf16 a[44:47], v[8:11], a[164:167], 0\n"
    "v_cndmask_b32_dpp v213, v181, v213, vcc row_ror:8 row_mask:0xf bank_mask:0xf\n"
    "v_cndmask_b32_dpp v214, v182, v214, vcc row_ror:8 row_mask:0xf bank_mask:0xf\n"
    "v_mfma_f32_16x16x32_bf16 a[44:47], v[12:15], a[188:191], a[44:47]\n"
    "s_waitcnt lgkmcnt(0)\n"
    "ds_read_b64_tr_b16 v[8:9], v18 offset:45064\n"
    "ds_read_b64_tr_b16 v[10:11], v18 offset:47112\n"
    "v_mfma_f32_16x16x32_bf16 a[0:3], v[0:3], a[192:195], a[0:3]\n"
    "v_cndmask_b32_dpp v215, v183, v215, vcc row_ror:8 row_mask:0xf bank_mask:0xf\n"
    "v_accvgpr_read_b32 v166, a58\n"
    "v_mfma_f32_16x16x32_bf16 a[0:3], v[4:7], a[216:219], a[0:3]\n"
    "ds_read_b64_tr_b16 v[12:13], v18 offset:8\n"
    "ds_read_b64_tr_b16 v[14:15], v18 offset:2056\n"
    "v_mfma_f32_16x16x32_bf16 a[8:11], v[0:3], a[196:199], a[8:11]\n"
    "v_accvgpr_read_b32 v167, a59\n"
    "v_mul_f32 v164, v164, v26\n"
    "v_mfma_f32_16x16x32_bf16 a[8:11], v[4:7], a[220:223], a[8:11]\n"
    "buffer_store_dwordx4 v[188:191], v24, s[52:55], s56 offen offset:3072 sc0 sc1\n"
    "v_mfma_f32_16x16x32_bf16 a[16:19], v[0:3], a[200:203], a[16:19]\n"
    "v_mul_f32 v165, v165, v26\n"
    "v_mul_f32 v166, v166, v26\n"
    "v_mfma_f32_16x16x32_bf16 a[16:19], v[4:7], a[224:227], a[16:19]\n"
    "v_mul_f32 v167, v167, v26\n"
    "v_cvt_pk_bf16_f32 v196, v164, v165\n"
    "v_mfma_f32_16x16x32_bf16 a[24:27], v[0:3], a[204:207], a[24:27]\n"
    "v_cvt_pk_bf16_f32 v197, v166, v167\n"
    "v_accvgpr_read_b32 v172, a60\n"
    "v_mfma_f32_16x16x32_bf16 a[24:27], v[4:7], a[228:231], a[24:27]\n"
    "v_accvgpr_read_b32 v173, a61\n"
    "v_accvgpr_read_b32 v174, a62\n"
    "v_mfma_f32_16x16x32_bf16 a[32:35], v[0:3], a[208:211], a[32:35]\n"
    "v_accvgpr_read_b32 v175, a63\n"
    "v_mul_f32 v172, v172, v26\n"
    "v_mfma_f32_16x16x32_bf16 a[32:35], v[4:7], a[232:235], a[32:35]\n"
    "v_mul_f32 v173, v173, v26\n"
    "s_mov_b32 s57, 6144\n"
    "v_mfma_f32_16x16x32_bf16 a[40:43], v[0:3], a[212:215], a[40:43]\n"
    "v_mul_f32 v174, v174, v26\n"
    "v_mul_f32 v175, v175, v26\n"
    "v_mfma_f32_16x16x32_bf16 a[40:43], v[4:7], a[236:239], a[40:43]\n"
    "s_waitcnt lgkmcnt(0)\n"
    "ds_read_b64_tr_b16 v[0:1], v18 offset:4096\n"
    "ds_read_b64_tr_b16 v[2:3], v18 offset:6144\n"
    "v_mfma_f32_16x16x32_bf16 a[4:7], v[8:11], a[192:195], a[4:7]\n"
    "v_cvt_pk_bf16_f32 v198, v172, v173\n"
    "v_cvt_pk_bf16_f32 v199, v174, v175\n"
    "v_mfma_f32_16x16x32_bf16 a[4:7], v[12:15], a[216:219], a[4:7]\n"
    "ds_read_b64_tr_b16 v[4:5], v18 offset:8192\n"
    "ds_read_b64_tr_b16 v[6:7], v18 offset:10240\n"
    "v_mfma_f32_16x16x32_bf16 a[12:15], v[8:11], a[196:199], a[12:15]\n"
    "v_mfma_f32_16x16x32_bf16 a[12:15], v[12:15], a[220:223], a[12:15]\n"
    "buffer_load_dwordx4 v23, s[40:43], s36 offen offset:2048 nt lds\n"
    "v_mfma_f32_16x16x32_bf16 a[20:23], v[8:11], a[200:203], a[20:23]\n"
    "v_mfma_f32_16x16x32_bf16 a[20:23], v[12:15], a[224:227], a[20:23]\n"
    "s_mov_b64 vcc, s[98:99]\n"
    "v_cndmask_b32_dpp v204, v220, v196, vcc row_ror:8 row_mask:0xf bank_mask:0xf\n"
    "v_mfma_f32_16x16x32_bf16 a[28:31], v[8:11], a[204:207], a[28:31]\n"
    "v_cndmask_b32_dpp v205, v221, v197, vcc row_ror:8 row_mask:0xf bank_mask:0xf\n"
    "v_cndmask_b32_dpp v206, v222, v198, vcc row_ror:8 row_mask:0xf bank_mask:0xf\n"
    "v_mfma_f32_16x16x32_bf16 a[28:31], v[12:15], a[228:231], a[28:31]\n"
    "v_cndmask_b32_dpp v207, v223, v199, vcc row_ror:8 row_mask:0xf bank_mask:0xf\n"
    "s_mov_b64 vcc, s[96:97]\n"
    "v_mfma_f32_16x16x32_bf16 a[36:39], v[8:11], a[208:211], a[36:39]\n"
    "v_cndmask_b32_dpp v220, v196, v220, vcc row_ror:8 row_mask:0xf bank_mask:0xf\n"
    "v_cndmask_b32_dpp v221, v197, v221, vcc row_ror:8 row_mask:0xf bank_mask:0xf\n"
    "v_mfma_f32_16x16x32_bf16 a[36:39], v[12:15], a[232:235], a[36:39]\n"
    "v_cndmask_b32_dpp v222, v198, v222, vcc row_ror:8 row_mask:0xf bank_mask:0xf\n"
    "v_cndmask_b32_dpp v223, v199, v223, vcc row_ror:8 row_mask:0xf bank_mask:0xf\n"
    "v_mfma_f32_16x16x32_bf16 a[44:47], v[8:11], a[212:215], a[44:47]\n"
    "v_accvgpr_read_b32 v164, a64\n"
    "v_accvgpr_read_b32 v165, a65\n"
    "v_mfma_f32_16x16x32_bf16 a[44:47], v[12:15], a[236:239], a[44:47]\n"
    "s_waitcnt lgkmcnt(0)\n"
    "ds_read_b64_tr_b16 v[8:9], v18 offset:4104\n"
    "ds_read_b64_tr_b16 v[10:11], v18 offset:6152\n"
    "s_add_u32 m0, s60, 61440\n"
    "v_mfma_f32_16x16x32_bf16 a[0:3], v[0:3], a[240:243], a[0:3]\n"
    "v_accvgpr_read_b32 v166, a66\n"
    "v_accvgpr_read_b32 v167, a67\n"
    "v_mfma_f32_16x16x32_bf16 a[0:3], v[4:7], v[40:43], a[0:3]\n"
    "ds_read_b64_tr_b16 v[12:13], v18 offset:8200\n"
    "ds_read_b64_tr_b16 v[14:15], v18 offset:10248\n"
    "s_add_u32 s36, s61, 122880\n"
    "v_mfma_f32_16x16x32_bf16 a[8:11], v[0:3], a[244:247], a[8:11]\n"
    "v_mul_f32 v164, v164, v27\n"
    "v_mul_f32 v165, v165, v27\n"
    "v_mfma_f32_16x16x32_bf16 a[8:11], v[4:7], v[44:47], a[8:11]\n"
    "buffer_load_dwordx4 v20, s[40:43], s36 offen nt lds\n"
    "v_mfma_f32_16x16x32_bf16 a[16:19], v[0:3], a[248:251], a[16:19]\n"
    "v_mul_f32 v166, v166, v27\n"
    "v_mul_f32 v167, v167, v27\n"
    "v_mfma_f32_16x16x32_bf16 a[16:19], v[4:7], v[48:51], a[16:19]\n"
    "buffer_store_dwordx4 v[204:207], v24, s[52:55], s57 offen offset:3072 sc0 sc1\n"
    "v_mfma_f32_16x16x32_bf16 a[24:27], v[0:3], a[252:255], a[24:27]\n"
    "v_cvt_pk_bf16_f32 v180, v164, v165\n"
    "v_cvt_pk_bf16_f32 v181, v166, v167\n"
    "v_mfma_f32_16x16x32_bf16 a[24:27], v[4:7], v[52:55], a[24:27]\n"
    "v_accvgpr_read_b32 v172, a68\n"
    "v_accvgpr_read_b32 v173, a69\n"
    "v_mfma_f32_16x16x32_bf16 a[32:35], v[0:3], v[32:35], a[32:35]\n"
    "v_accvgpr_read_b32 v174, a70\n"
    "v_accvgpr_read_b32 v175, a71\n"
    "v_mfma_f32_16x16x32_bf16 a[32:35], v[4:7], v[56:59], a[32:35]\n"
    "v_mul_f32 v172, v172, v27\n"
    "v_mul_f32 v173, v173, v27\n"
    "v_mfma_f32_16x16x32_bf16 a[40:43], v[0:3], v[36:39], a[40:43]\n"
    "v_mul_f32 v174, v174, v27\n"
    "v_mul_f32 v175, v175, v27\n"
    "v_mfma_f32_16x16x32_bf16 a[40:43], v[4:7], v[60:63], a[40:43]\n"
    "s_waitcnt lgkmcnt(0)\n"
    "ds_read_b64_tr_b16 v[0:1], v18 offset:12288\n"
    "ds_read_b64_tr_b16 v[2:3], v18 offset:14336\n"
    "v_mfma_f32_16x16x32_bf16 a[4:7], v[8:11], a[240:243], a[4:7]\n"
    "v_cvt_pk_bf16_f32 v182, v172, v173\n"
    "v_cvt_pk_bf16_f32 v183, v174, v175\n"
    "v_mfma_f32_16x16x32_bf16 a[4:7], v[12:15], v[40:43], a[4:7]\n"
    "ds_read_b64_tr_b16 v[4:5], v18 offset:16384\n"
    "ds_read_b64_tr_b16 v[6:7], v18 offset:18432\n"
    "v_mfma_f32_16x16x32_bf16 a[12:15], v[8:11], a[244:247], a[12:15]\n"
    "v_mfma_f32_16x16x32_bf16 a[12:15], v[12:15], v[44:47], a[12:15]\n"
    "s_mov_b32 s56, 12288\n"
    "s_mov_b64 vcc, s[98:99]\n"
    "v_mfma_f32_16x16x32_bf16 a[20:23], v[8:11], a[248:251], a[20:23]\n"
    "v_cndmask_b32_dpp v188, v228, v180, vcc row_ror:8 row_mask:0xf bank_mask:0xf\n"
    "v_cndmask_b32_dpp v189, v229, v181, vcc row_ror:8 row_mask:0xf bank_mask:0xf\n"
    "v_mfma_f32_16x16x32_bf16 a[20:23], v[12:15], v[48:51], a[20:23]\n"
    "v_cndmask_b32_dpp v190, v230, v182, vcc row_ror:8 row_mask:0xf bank_mask:0xf\n"
    "v_cndmask_b32_dpp v191, v231, v183, vcc row_ror:8 row_mask:0xf bank_mask:0xf\n"
    "v_mfma_f32_16x16x32_bf16 a[28:31], v[8:11], a[252:255], a[28:31]\n"
    "v_accvgpr_read_b32 v164, a72\n"
    "v_accvgpr_read_b32 v165, a73\n"
    "v_mfma_f32_16x16x32_bf16 a[28:31], v[12:15], v[52:55], a[28:31]\n"
    "s_mov_b64 vcc, s[96:97]\n"
    "v_cndmask_b32_dpp v228, v180, v228, vcc row_ror:8 row_mask:0xf bank_mask:0xf\n"
    "v_mfma_f32_16x16x32_bf16 a[36:39], v[8:11], v[32:35], a[36:39]\n"
    "v_cndmask_b32_dpp v229, v181, v229, vcc row_ror:8 row_mask:0xf bank_mask:0xf\n"
    "v_cndmask_b32_dpp v230, v182, v230, vcc row_ror:8 row_mask:0xf bank_mask:0xf\n"
    "v_mfma_f32_16x16x32_bf16 a[36:39], v[12:15], v[56:59], a[36:39]\n"
    "v_cndmask_b32_dpp v231, v183, v231, vcc row_ror:8 row_mask:0xf bank_mask:0xf\n"
    "v_mfma_f32_16x16x32_bf16 a[44:47], v[8:11], v[36:39], a[44:47]\n"
    "v_accvgpr_read_b32 v166, a74\n"
    "v_accvgpr_read_b32 v167, a75\n"
    "v_mfma_f32_16x16x32_bf16 a[44:47], v[12:15], v[60:63], a[44:47]\n"
    "s_waitcnt lgkmcnt(0)\n"
    "ds_read_b64_tr_b16 v[8:9], v18 offset:12296\n"
    "ds_read_b64_tr_b16 v[10:11], v18 offset:14344\n"
    "v_mfma_f32_16x16x32_bf16 a[0:3], v[0:3], v[64:67], a[0:3]\n"
    "v_mul_f32 v164, v164, v28\n"
    "v_mul_f32 v165, v165, v28\n"
    "v_mfma_f32_16x16x32_bf16 a[0:3], v[4:7], v[88:91], a[0:3]\n"
    "ds_read_b64_tr_b16 v[12:13], v18 offset:16392\n"
    "ds_read_b64_tr_b16 v[14:15], v18 offset:18440\n"
    "v_mfma_f32_16x16x32_bf16 a[8:11], v[0:3], v[68:71], a[8:11]\n"
    "v_mul_f32 v166, v166, v28\n"
    "v_mul_f32 v167, v167, v28\n"
    "v_mfma_f32_16x16x32_bf16 a[8:11], v[4:7], v[92:95], a[8:11]\n"
    "buffer_store_dwordx4 v[188:191], v24, s[52:55], s56 offen offset:3072 sc0 sc1\n"
    "v_mfma_f32_16x16x32_bf16 a[16:19], v[0:3], v[72:75], a[16:19]\n"
    "v_cvt_pk_bf16_f32 v196, v164, v165\n"
    "v_cvt_pk_bf16_f32 v197, v166, v167\n"
    "v_mfma_f32_16x16x32_bf16 a[16:19], v[4:7], v[96:99], a[16:19]\n"
    "v_accvgpr_read_b32 v172, a76\n"
    "v_accvgpr_read_b32 v173, a77\n"
    "v_mfma_f32_16x16x32_bf16 a[24:27], v[0:3], v[76:79], a[24:27]\n"
    "v_accvgpr_read_b32 v174, a78\n"
    "v_accvgpr_read_b32 v175, a79\n"
    "v_mfma_f32_16x16x32_bf16 a[24:27], v[4:7], v[100:103], a[24:27]\n"
    "v_mul_f32 v172, v172, v28\n"
    "v_mul_f32 v173, v173, v28\n"
    "v_mfma_f32_16x16x32_bf16 a[32:35], v[0:3], v[80:83], a[32:35]\n"
    "v_mul_f32 v174, v174, v28\n"
    "v_mul_f32 v175, v175, v28\n"
    "v_mfma_f32_16x16x32_bf16 a[32:35], v[4:7], v[104:107], a[32:35]\n"
    "buffer_load_dwordx4 v22, s[40:43], s36 offen offset:1024 nt lds\n"
    "v_mfma_f32_16x16x32_bf16 a[40:43], v[0:3], v[84:87], a[40:43]\n"
    "v_cvt_pk_bf16_f32 v198, v172, v173\n"
    "v_cvt_pk_bf16_f32 v199, v174, v175\n"
    "v_mfma_f32_16x16x32_bf16 a[40:43], v[4:7], v[108:111], a[40:43]\n"
    "s_waitcnt lgkmcnt(0)\n"
    "ds_read_b64_tr_b16 v[0:1], v18 offset:20480\n"
    "ds_read_b64_tr_b16 v[2:3], v18 offset:22528\n"
    "v_mfma_f32_16x16x32_bf16 a[4:7], v[8:11], v[64:67], a[4:7]\n"
    "v_mfma_f32_16x16x32_bf16 a[4:7], v[12:15], v[88:91], a[4:7]\n"
    "ds_read_b64_tr_b16 v[4:5], v18 offset:24576\n"
    "ds_read_b64_tr_b16 v[6:7], v18 offset:26624\n"
    "v_mfma_f32_16x16x32_bf16 a[12:15], v[8:11], v[68:71], a[12:15]\n"
    "v_mfma_f32_16x16x32_bf16 a[12:15], v[12:15], v[92:95], a[12:15]\n"
    "s_mov_b32 s57, 18432\n"
    "s_mov_b64 vcc, s[98:99]\n"
    "v_mfma_f32_16x16x32_bf16 a[20:23], v[8:11], v[72:75], a[20:23]\n"
    "v_cndmask_b32_dpp v204, v236, v196, vcc row_ror:8 row_mask:0xf bank_mask:0xf\n"
    "v_cndmask_b32_dpp v205, v237, v197, vcc row_ror:8 row_mask:0xf bank_mask:0xf\n"
    "v_mfma_f32_16x16x32_bf16 a[20:23], v[12:15], v[96:99], a[20:23]\n"
    "v_cndmask_b32_dpp v206, v238, v198, vcc row_ror:8 row_mask:0xf bank_mask:0xf\n"
    "v_cndmask_b32_dpp v207, v239, v199, vcc row_ror:8 row_mask:0xf bank_mask:0xf\n"
    "v_mfma_f32_16x16x32_bf16 a[28:31], v[8:11], v[76:79], a[28:31]\n"
    "v_accvgpr_read_b32 v164, a80\n"
    "v_accvgpr_read_b32 v165, a81\n"
    "v_mfma_f32_16x16x32_bf16 a[28:31], v[12:15], v[100:103], a[28:31]\n"
    "s_memrealtime s[92:93]\n"
    "s_mov_b64 vcc, s[96:97]\n"
    "v_mfma_f32_16x16x32_bf16 a[36:39], v[8:11], v[80:83], a[36:39]\n"
    "v_cndmask_b32_dpp v236, v196, v236, vcc row_ror:8 row_mask:0xf bank_mask:0xf\n"
    "v_cndmask_b32_dpp v237, v197, v237, vcc row_ror:8 row_mask:0xf bank_mask:0xf\n"
    "v_mfma_f32_16x16x32_bf16 a[36:39], v[12:15], v[104:107], a[36:39]\n"
    "v_cndmask_b32_dpp v238, v198, v238, vcc row_ror:8 row_mask:0xf bank_mask:0xf\n"
    "v_cndmask_b32_dpp v239, v199, v239, vcc row_ror:8 row_mask:0xf bank_mask:0xf\n"
    "v_mfma_f32_16x16x32_bf16 a[44:47], v[8:11], v[84:87], a[44:47]\n"
    "v_accvgpr_read_b32 v166, a82\n"
    "v_accvgpr_read_b32 v167, a83\n"
    "v_mfma_f32_16x16x32_bf16 a[44:47], v[12:15], v[108:111], a[44:47]\n"
    "s_waitcnt lgkmcnt(0)\n"
    "ds_read_b64_tr_b16 v[8:9], v18 offset:20488\n"
    "ds_read_b64_tr_b16 v[10:11], v18 offset:22536\n"
    "v_mfma_f32_16x16x32_bf16 a[0:3], v[0:3], v[112:115], a[0:3]\n"
    "v_mul_f32 v164, v164, v29\n"
    "v_mul_f32 v165, v165, v29\n"
    "v_mfma_f32_16x16x32_bf16 a[0:3], v[4:7], v[136:139], a[0:3]\n"
    "s_add_u32 s94, s94, 172\n"
    "s_sub_u32 s95, s94, s92\n"
    "s_cmp_gt_i32 s95, 3\n"
    "s_cbranch_scc1 L_pc8_%=\n"
    "L_pr8_%=:\n"
    "ds_read_b64_tr_b16 v[12:13], v18 offset:24584\n"
    "ds_read_b64_tr_b16 v[14:15], v18 offset:26632\n"
    "v_mfma_f32_16x16x32_bf16 a[8:11], v[0:3], v[116:119], a[8:11]\n"
    "v_mul_f32 v166, v166, v29\n"
    "v_mul_f32 v167, v167, v29\n"
    "v_mfma_f32_16x16x32_bf16 a[8:11], v[4:7], v[140:143], a[8:11]\n"
    "buffer_store_dwordx4 v[204:207], v24, s[52:55], s57 offen offset:3072 sc0 sc1\n"
    "v_mfma_f32_16x16x32_bf16 a[16:19], v[0:3], v[120:123], a[16:19]\n"
    "v_cvt_pk_bf16_f32 v180, v164, v165\n"
    "v_cvt_pk_bf16_f32 v181, v166, v167\n"
    "v_mfma_f32_16x16x32_bf16 a[16:19], v[4:7], v[144:147], a[16:19]\n"
    "v_accvgpr_read_b32 v172, a84\n"
    "v_accvgpr_read_b32 v173, a85\n"
    "v_mfma_f32_16x16x32_bf16 a[24:27], v[0:3], v[124:127], a[24:27]\n"
    "v_accvgpr_read_b32 v174, a86\n"
    "v_accvgpr_read_b32 v175, a87\n"
    "v_mfma_f32_16x16x32_bf16 a[24:27], v[4:7], v[148:151], a[24:27]\n"
    "v_mul_f32 v172, v172, v29\n"
    "v_mul_f32 v173, v173, v29\n"
    "v_mfma_f32_16x16x32_bf16 a[32:35], v[0:3], v[128:131], a[32:35]\n"
    "v_mul_f32 v174, v174, v29\n"
    "v_mul_f32 v175, v175, v29\n"
    "v_mfma_f32_16x16x32_bf16 a[32:35], v[4:7], v[152:155], a[32:35]\n"
    "s_mov_b32 s56, 24576\n"
    "s_mov_b64 vcc, s[98:99]\n"
    "v_mfma_f32_16x16x32_bf16 a[40:43], v[0:3], v[132:135], a[40:43]\n"
    "v_cvt_pk_bf16_f32 v182, v172, v173\n"
    "v_cvt_pk_bf16_f32 v183, v174, v175\n"
    "v_mfma_f32_16x16x32_bf16 a[40:43], v[4:7], v[156:159], a[40:43]\n"
    "s_waitcnt lgkmcnt(0)\n"
    "ds_read_b64_tr_b16 v[0:1], v18 offset:28672\n"
    "ds_read_b64_tr_b16 v[2:3], v18 offset:30720\n"
    "v_mfma_f32_16x16x32_bf16 a[4:7], v[8:11], v[112:115], a[4:7]\n"
    "v_cndmask_b32_dpp v188, v244, v180, vcc row_ror:8 row_mask:0xf bank_mask:0xf\n"
    "v_cndmask_b32_dpp v189, v245, v181, vcc row_ror:8 row_mask:0xf bank_mask:0xf\n"
    "v_mfma_f32_16x16x32_bf16 a[4:7], v[12:15], v[136:139], a[4:7]\n"
    "ds_read_b64_tr_b16 v[4:5], v18 offset:32768\n"
    "ds_read_b64_tr_b16 v[6:7], v18 offset:34816\n"
    "v_mfma_f32_16x16x32_bf16 a[12:15], v[8:11], v[116:119], a[12:15]\n"
    "v_cndmask_b32_dpp v190, v246, v182, vcc row_ror:8 row_mask:0xf bank_mask:0xf\n"
    "v_cndmask_b32_dpp v191, v247, v183, vcc row_ror:8 row_mask:0xf bank_mask:0xf\n"
    "v_mfma_f32_16x16x32_bf16 a[12:15], v[12:15], v[140:143], a[12:15]\n"
    "buffer_load_dwordx4 v23, s[40:43], s36 offen offset:2048 nt lds\n"
    "v_mfma_f32_16x16x32_bf16 a[20:23], v[8:11], v[120:123], a[20:23]\n"
    "v_accvgpr_read_b32 v164, a88\n"
    "v_accvgpr_read_b32 v165, a89\n"
    "v_mfma_f32_16x16x32_bf16 a[20:23], v[12:15], v[144:147], a[20:23]\n"
    "s_mov_b64 vcc, s[96:97]\n"
    "v_cndmask_b32_dpp v244, v180, v244, vcc row_ror:8 row_mask:0xf bank_mask:0xf\n"
    "v_mfma_f32_16x16x32_bf16 a[28:31], v[8:11], v[124:127], a[28:31]\n"
    "v_cndmask_b32_dpp v245, v181, v245, vcc row_ror:8 row_mask:0xf bank_mask:0xf\n"
    "v_cndmask_b32_dpp v246, v182, v246, vcc row_ror:8 row_mask:0xf bank_mask:0xf\n"
    "v_mfma_f32_16x16x32_bf16 a[28:31], v[12:15], v[148:151], a[28:31]\n"
    "v_cndmask_b32_dpp v247, v183, v247, vcc row_ror:8 row_mask:0xf bank_mask:0xf\n"
    "v_mfma_f32_16x16x32_bf16 a[36:39], v[8:11], v[128:131], a[36:39]\n"
    "v_accvgpr_read_b32 v166, a90\n"
    "v_accvgpr_read_b32 v167, a91\n"
    "v_mfma_f32_16x16x32_bf16 a[36:39], v[12:15], v[152:155], a[36:39]\n"
    "buffer_store_dwordx4 v[188:191], v24, s[52:55], s56 offen offset:3072 sc0 sc1\n"
    "v_mfma_f32_16x16x32_bf16 a[44:47], v[8:11], v[132:135], a[44:47]\n"
    "v_mul_f32 v164, v164, v30\n"
    "v_mul_f32 v165, v165, v30\n"
    "v_mfma_f32_16x16x32_bf16 a[44:47], v[12:15], v[156:159], a[44:47]\n"
    "s_waitcnt lgkmcnt(0)\n"
    "ds_read_b64_tr_b16 v[8:9], v18 offset:28680\n"
    "ds_read_b64_tr_b16 v[10:11], v18 offset:30728\n"
    "v_mfma_f32_16x16x32_bf16 a[0:3], v[0:3], v[160:163], a[0:3]\n"
    "v_mul_f32 v166, v166, v30\n"
    "v_mul_f32 v167, v167, v30\n"
    "v_mfma_f32_16x16x32_bf16 a[0:3], v[4:7], v[208:211], a[0:3]\n"
    "ds_read_b64_tr_b16 v[12:13], v18 offset:32776\n"
    "ds_read_b64_tr_b16 v[14:15], v18 offset:34824\n"
    "v_mfma_f32_16x16x32_bf16 a[8:11], v[0:3], v[168:171], a[8:11]\n"
    "v_cvt_pk_bf16_f32 v196, v164, v165\n"
    "v_cvt_pk_bf16_f32 v197, v166, v167\n"
    "v_mfma_f32_16x16x32_bf16 a[8:11], v[4:7], v[216:219], a[8:11]\n"
    "v_accvgpr_read_b32 v172, a92\n"
    "v_mfma_f32_16x16x32_bf16 a[16:19], v[0:3], v[176:179], a[16:19]\n"
    "v_accvgpr_read_b32 v173, a93\n"
    "v_accvgpr_read_b32 v174, a94\n"
    "v_mfma_f32_16x16x32_bf16 a[16:19], v[4:7], v[224:227], a[16:19]\n"
    "v_accvgpr_read_b32 v175, a95\n"
    "v_mul_f32 v172, v172, v30\n"
    "v_mfma_f32_16x16x32_bf16 a[24:27], v[0:3], v[184:187], a[24:27]\n"
    "v_mul_f32 v173, v173, v30\n"
    "v_mul_f32 v174, v174, v30\n"
    "v_mfma_f32_16x16x32_bf16 a[24:27], v[4:7], v[232:235], a[24:27]\n"
    "s_mov_b32 s57, 30720\n"
    "s_mov_b64 vcc, s[98:99]\n"
    "v_mfma_f32_16x16x32_bf16 a[32:35], v[0:3], v[192:195], a[32:35]\n"
    "v_mul_f32 v175, v175, v30\n"
    "v_cvt_pk_bf16_f32 v198, v172, v173\n"
    "v_mfma_f32_16x16x32_bf16 a[32:35], v[4:7], v[240:243], a[32:35]\n"
    "v_cvt_pk_bf16_f32 v199, v174, v175\n"
    "v_mfma_f32_16x16x32_bf16 a[40:43], v[0:3], v[200:203], a[40:43]\n"
    "v_xor_b32 v18, 64, v18\n"
    "v_cndmask_b32_dpp v204, v252, v196, vcc row_ror:8 row_mask:0xf bank_mask:0xf\n"
    "v_mfma_f32_16x16x32_bf16 a[40:43], v[4:7], v[248:251], a[40:43]\n"
    "s_waitcnt lgkmcnt(0)\n"
    "ds_read_b64_tr_b16 v[0:1], v18 offset:36864\n"
    "ds_read_b64_tr_b16 v[2:3], v18 offset:38912\n"
    "v_mfma_f32_16x16x32_bf16 a[4:7], v[8:11], v[160:163], a[4:7]\n"
    "v_cndmask_b32_dpp v205, v253, v197, vcc row_ror:8 row_mask:0xf bank_mask:0xf\n"
    "v_cndmask_b32_dpp v206, v254, v198, vcc row_ror:8 row_mask:0xf bank_mask:0xf\n"
    "v_mfma_f32_16x16x32_bf16 a[4:7], v[12:15], v[208:211], a[4:7]\n"
    "ds_read_b64_tr_b16 v[4:5], v18 offset:40960\n"
    "ds_read_b64_tr_b16 v[6:7], v18 offset:43008\n"
    "v_mfma_f32_16x16x32_bf16 a[12:15], v[8:11], v[168:171], a[12:15]\n"
    "v_cndmask_b32_dpp v207, v255, v199, vcc row_ror:8 row_mask:0xf bank_mask:0xf\n"
    "v_mfma_f32_16x16x32_bf16 a[12:15], v[12:15], v[216:219], a[12:15]\n"
    "s_mov_b64 vcc, s[96:97]\n"
    "v_cndmask_b32_dpp v252, v196, v252, vcc row_ror:8 row_mask:0xf bank_mask:0xf\n"
    "v_mfma_f32_16x16x32_bf16 a[20:23], v[8:11], v[176:179], a[20:23]\n"
    "v_cndmask_b32_dpp v253, v197, v253, vcc row_ror:8 row_mask:0xf bank_mask:0xf\n"
    "v_cndmask_b32_dpp v254, v198, v254, vcc row_ror:8 row_mask:0xf bank_mask:0xf\n"
    "v_mfma_f32_16x16x32_bf16 a[20:23], v[12:15], v[224:227], a[20:23]\n"
    "v_cndmask_b32_dpp v255, v199, v255, vcc row_ror:8 row_mask:0xf bank_mask:0xf\n"
    "v_mfma_f32_16x16x32_bf16 a[28:31], v[8:11], v[184:187], a[28:31]\n"
    "v_mfma_f32_16x16x32_bf16 a[28:31], v[12:15], v[232:235], a[28:31]\n"
    "buffer_store_dwordx4 v[204:207], v24, s[52:55], s57 offen offset:3072 sc0 sc1\n"
    "v_mfma_f32_16x16x32_bf16 a[36:39], v[8:11], v[192:195], a[36:39]\n"
    "v_mfma_f32_16x16x32_bf16 a[36:39], v[12:15], v[240:243], a[36:39]\n"
    "v_mfma_f32_16x16x32_bf16 a[44:47], v[8:11], v[200:203], a[44:47]\n"
    "v_mfma_f32_16x16x32_bf16 a[44:47], v[12:15], v[248:251], a[44:47]\n"
    "s_waitcnt lgkmcnt(0)\n"
    "ds_read_b64_tr_b16 v[8:9], v18 offset:36872\n"
    "ds_read_b64_tr_b16 v[10:11], v18 offset:38920\n"
    "v_mfma_f32_16x16x32_bf16 a[48:51], v[0:3], a[144:147], 0\n"
    "v_mfma_f32_16x16x32_bf16 a[48:51], v[4:7], a[168:171], a[48:51]\n"
    "ds_read_b64_tr_b16 v[12:13], v18 offset:40968\n"
    "ds_read_b64_tr_b16 v[14:15], v18 offset:43016\n"
    "v_mfma_f32_16x16x32_bf16 a[56:59], v[0:3], a[148:151], 0\n"
    "v_accvgpr_read_b32 v164, a0\n"
    "v_accvgpr_read_b32 v165, a1\n"
    "v_mfma_f32_16x16x32_bf16 a[56:59], v[4:7], a[172:175], a[56:59]\n"
    "s_mov_b32 s56, 0\n"
    "v_mfma_f32_16x16x32_bf16 a[64:67], v[0:3], a[152:155], 0\n"
    "v_accvgpr_read_b32 v166, a2\n"
    "v_accvgpr_read_b32 v167, a3\n"
    "v_mfma_f32_16x16x32_bf16 a[64:67], v[4:7], a[176:179], a[64:67]\n"
    "v_mul_f32 v164, v164, v25\n"
    "v_mfma_f32_16x16x32_bf16 a[72:75], v[0:3], a[156:159], 0\n"
    "v_mul_f32 v165, v165, v25\n"
    "v_mul_f32 v166, v166, v25\n"
    "v_mfma_f32_16x16x32_bf16 a[72:75], v[4:7], a[180:183], a[72:75]\n"
    "buffer_store_dwordx4 v[212:215], v24, s[52:55], s56 offen sc0 sc1\n"
    "v_mfma_f32_16x16x32_bf16 a[80:83], v[0:3], a[160:163], 0\n"
    "v_mul_f32 v167, v167, v25\n"
    "v_cvt_pk_bf16_f32 v212, v164, v165\n"
    "v_mfma_f32_16x16x32_bf16 a[80:83], v[4:7], a[184:187], a[80:83]\n"
    "v_cvt_pk_bf16_f32 v213, v166, v167\n"
    "v_mfma_f32_16x16x32_bf16 a[88:91], v[0:3], a[164:167], 0\n"
    "v_accvgpr_read_b32 v172, a4\n"
    "v_accvgpr_read_b32 v173, a5\n"
    "v_mfma_f32_16x16x32_bf16 a[88:91], v[4:7], a[188:191], a[88:91]\n"
    "s_waitcnt lgkmcnt(0)\n"
    "ds_read_b64_tr_b16 v[0:1], v18 offset:45056\n"
    "ds_read_b64_tr_b16 v[2:3], v18 offset:47104\n"
    "v_mfma_f32_16x16x32_bf16 a[52:55], v[8:11], a[144:147], 0\n"
    "v_accvgpr_read_b32 v174, a6\n"
    "v_accvgpr_read_b32 v175, a7\n"
    "v_mfma_f32_16x16x32_bf16 a[52:55], v[12:15], a[168:171], a[52:55]\n"
    "ds_read_b64_tr_b16 v[4:5], v18 offset:0\n"
    "ds_read_b64_tr_b16 v[6:7], v18 offset:2048\n"
    "v_mfma_f32_16x16x32_bf16 a[60:63], v[8:11], a[148:151], 0\n"
    "v_mul_f32 v172, v172, v25\n"
    "v_mul_f32 v173, v173, v25\n"
    "v_mfma_f32_16x16x32_bf16 a[60:63], v[12:15], a[172:175], a[60:63]\n"
    "v_mul_f32 v174, v174, v25\n"
    "v_mfma_f32_16x16x32_bf16 a[68:71], v[8:11], a[152:155], 0\n"
    "v_mul_f32 v175, v175, v25\n"
    "v_cvt_pk_bf16_f32 v214, v172, v173\n"
    "v_mfma_f32_16x16x32_bf16 a[68:71], v[12:15], a[176:179], a[68:71]\n"
    "v_cvt_pk_bf16_f32 v215, v174, v175\n"
    "s_mov_b32 s57, 6144\n"
    "v_mfma_f32_16x16x32_bf16 a[76:79], v[8:11], a[156:159], 0\n"
    "v_accvgpr_read_b32 v164, a8\n"
    "v_accvgpr_read_b32 v165, a9\n"
    "v_mfma_f32_16x16x32_bf16 a[76:79], v[12:15], a[180:183], a[76:79]\n"
    "v_accvgpr_read_b32 v166, a10\n"
    "v_mfma_f32_16x16x32_bf16 a[84:87], v[8:11], a[160:163], 0\n"
    "v_accvgpr_read_b32 v167, a11\n"
    "v_mul_f32 v164, v164, v26\n"
    "v_mfma_f32_16x16x32_bf16 a[84:87], v[12:15], a[184:187], a[84:87]\n"
    "v_mul_f32 v165, v165, v26\n"
    "v_mfma_f32_16x16x32_bf16 a[92:95], v[8:11], a[164:167], 0\n"
    "v_mul_f32 v166, v166, v26\n"
    "v_mul_f32 v167, v167, v26\n"
    "v_mfma_f32_16x16x32_bf16 a[92:95], v[12:15], a[188:191], a[92:95]\n"
    "s_waitcnt lgkmcnt(0)\n"
    "ds_read_b64_tr_b16 v[8:9], v18 offset:45064\n"
    "ds_read_b64_tr_b16 v[10:11], v18 offset:47112\n"
    "v_mfma_f32_16x16x32_bf16 a[48:51], v[0:3], a[192:195], a[48:51]\n"
    "v_accvgpr_read_b32 v172, a12\n"
    "v_accvgpr_read_b32 v173, a13\n"
    "v_mfma_f32_16x16x32_bf16 a[48:51], v[4:7], a[216:219], a[48:51]\n"
    "ds_read_b64_tr_b16 v[12:13], v18 offset:8\n"
    "ds_read_b64_tr_b16 v[14:15], v18 offset:2056\n"
    "v_mfma_f32_16x16x32_bf16 a[56:59], v[0:3], a[196:199], a[56:59]\n"
    "v_accvgpr_read_b32 v174, a14\n"
    "v_accvgpr_read_b32 v175, a15\n"
    "v_mfma_f32_16x16x32_bf16 a[56:59], v[4:7], a[220:223], a[56:59]\n"
    "buffer_store_dwordx4 v[220:223], v24, s[52:55], s57 offen sc0 sc1\n"
    "v_mfma_f32_16x16x32_bf16 a[64:67], v[0:3], a[200:203], a[64:67]\n"
    "s_nop 0\n"
    "v_cvt_pk_bf16_f32 v220, v164, v165\n"
    "v_cvt_pk_bf16_f32 v221, v166, v167\n"
    "v_mfma_f32_16x16x32_bf16 a[64:67], v[4:7], a[224:227], a[64:67]\n"
    "v_mul_f32 v172, v172, v26\n"
    "v_mfma_f32_16x16x32_bf16 a[72:75], v[0:3], a[204:207], a[72:75]\n"
    "v_mul_f32 v173, v173, v26\n"
    "v_mul_f32 v174, v174, v26\n"
    "v_mfma_f32_16x16x32_bf16 a[72:75], v[4:7], a[228:231], a[72:75]\n"
    "v_mul_f32 v175, v175, v26\n"
    "v_mfma_f32_16x16x32_bf16 a[80:83], v[0:3], a[208:211], a[80:83]\n"
    "v_cvt_pk_bf16_f32 v222, v172, v173\n"
    "v_cvt_pk_bf16_f32 v223, v174, v175\n"
    "v_mfma_f32_16x16x32_bf16 a[80:83], v[4:7], a[232:235], a[80:83]\n"
    "s_mov_b32 s56, 12288\n"
    "v_mfma_f32_16x16x32_bf16 a[88:91], v[0:3], a[212:215], a[88:91]\n"
    "v_accvgpr_read_b32 v164, a16\n"
    "v_accvgpr_read_b32 v165, a17\n"
    "v_mfma_f32_16x16x32_bf16 a[88:91], v[4:7], a[236:239], a[88:91]\n"
    "s_waitcnt lgkmcnt(0)\n"
    "ds_read_b64_tr_b16 v[0:1], v18 offset:4096\n"
    "ds_read_b64_tr_b16 v[2:3], v18 offset:6144\n"
    "v_mfma_f32_16x16x32_bf16 a[52:55], v[8:11], a[192:195], a[52:55]\n"
    "v_accvgpr_read_b32 v166, a18\n"
    "v_accvgpr_read_b32 v167, a19\n"
    "v_mfma_f32_16x16x32_bf16 a[52:55], v[12:15], a[216:219], a[52:55]\n"
    "ds_read_b64_tr_b16 v[4:5], v18 offset:8192\n"
    "ds_read_b64_tr_b16 v[6:7], v18 offset:10240\n"
    "v_mfma_f32_16x16x32_bf16 a[60:63], v[8:11], a[196:199], a[60:63]\n"
    "v_mul_f32 v164, v164, v27\n"
    "v_mul_f32 v165, v165, v27\n"
    "v_mfma_f32_16x16x32_bf16 a[60:63], v[12:15], a[220:223], a[60:63]\n"
    "buffer_store_dwordx4 v[228:231], v24, s[52:55], s56 offen sc0 sc1\n"
    "v_mfma_f32_16x16x32_bf16 a[68:71], v[8:11], a[200:203], a[68:71]\n"
    "v_mul_f32 v166, v166, v27\n"
    "v_mul_f32 v167, v167, v27\n"
    "v_mfma_f32_16x16x32_bf16 a[68:71], v[12:15], a[224:227], a[68:71]\n"
    "v_cvt_pk_bf16_f32 v228, v164, v165\n"
    "v_mfma_f32_16x16x32_bf16 a[76:79], v[8:11], a[204:207], a[76:79]\n"
    "v_cvt_pk_bf16_f32 v229, v166, v167\n"
    "v_accvgpr_read_b32 v172, a20\n"
    "v_mfma_f32_16x16x32_bf16 a[76:79], v[12:15], a[228:231], a[76:79]\n"
    "v_accvgpr_read_b32 v173, a21\n"
    "v_mfma_f32_16x16x32_bf16 a[84:87], v[8:11], a[208:211], a[84:87]\n"
    "v_accvgpr_read_b32 v174, a22\n"
    "v_accvgpr_read_b32 v175, a23\n"
    "v_mfma_f32_16x16x32_bf16 a[84:87], v[12:15], a[232:235], a[84:87]\n"
    "v_mul_f32 v172, v172, v27\n"
    "v_mfma_f32_16x16x32_bf16 a[92:95], v[8:11], a[212:215], a[92:95]\n"
    "v_mul_f32 v173, v173, v27\n"
    "v_mul_f32 v174, v174, v27\n"
    "v_mfma_f32_16x16x32_bf16 a[92:95], v[12:15], a[236:239], a[92:95]\n"
    "s_waitcnt lgkmcnt(0)\n"
    "ds_read_b64_tr_b16 v[8:9], v18 offset:4104\n"
    "ds_read_b64_tr_b16 v[10:11], v18 offset:6152\n"
    "v_mfma_f32_16x16x32_bf16 a[48:51], v[0:3], a[240:243], a[48:51]\n"
    "v_mul_f32 v175, v175, v27\n"
    "v_cvt_pk_bf16_f32 v230, v172, v173\n"
    "v_mfma_f32_16x16x32_bf16 a[48:51], v[4:7], v[40:43], a[48:51]\n"
    "ds_read_b64_tr_b16 v[12:13], v18 offset:8200\n"
    "ds_read_b64_tr_b16 v[14:15], v18 offset:10248\n"
    "v_mfma_f32_16x16x32_bf16 a[56:59], v[0:3], a[244:247], a[56:59]\n"
    "v_cvt_pk_bf16_f32 v231, v174, v175\n"
    "v_accvgpr_read_b32 v164, a24\n"
    "v_mfma_f32_16x16x32_bf16 a[56:59], v[4:7], v[44:47], a[56:59]\n"
    "s_mov_b32 s57, 18432\n"
    "v_mfma_f32_16x16x32_bf16 a[64:67], v[0:3], a[248:251], a[64:67]\n"
    "v_accvgpr_read_b32 v165, a25\n"
    "v_accvgpr_read_b32 v166, a26\n"
    "v_mfma_f32_16x16x32_bf16 a[64:67], v[4:7], v[48:51], a[64:67]\n"
    "v_accvgpr_read_b32 v167, a27\n"
    "v_mfma_f32_16x16x32_bf16 a[72:75], v[0:3], a[252:255], a[72:75]\n"
    "v_mul_f32 v164, v164, v28\n"
    "v_mul_f32 v165, v165, v28\n"
    "v_mfma_f32_16x16x32_bf16 a[72:75], v[4:7], v[52:55], a[72:75]\n"
    "buffer_store_dwordx4 v[236:239], v24, s[52:55], s57 offen sc0 sc1\n"
    "v_mfma_f32_16x16x32_bf16 a[80:83], v[0:3], v[32:35], a[80:83]\n"
    "v_mul_f32 v166, v166, v28\n"
    "v_mul_f32 v167, v167, v28\n"
    "v_mfma_f32_16x16x32_bf16 a[80:83], v[4:7], v[56:59], a[80:83]\n"
    "s_waitcnt lgkmcnt(0)\n"
    "s_barrier\n"
    "v_mfma_f32_16x16x32_bf16 a[88:91], v[0:3], v[36:39], a[88:91]\n"
    "v_cvt_pk_bf16_f32 v236, v164, v165\n"
    "v_cvt_pk_bf16_f32 v237, v166, v167\n"
    "v_mfma_f32_16x16x32_bf16 a[88:91], v[4:7], v[60:63], a[88:91]\n"
    "ds_read_b64_tr_b16 v[0:1], v18 offset:12288\n"
    "ds_read_b64_tr_b16 v[2:3], v18 offset:14336\n"
    "s_add_u32 m0, s60, 135168\n"
    "v_mfma_f32_16x16x32_bf16 a[52:55], v[8:11], a[240:243], a[52:55]\n"
    "v_accvgpr_read_b32 v172, a28\n"
    "v_accvgpr_read_b32 v173, a29\n"
    "v_mfma_f32_16x16x32_bf16 a[52:55], v[12:15], v[40:43], a[52:55]\n"
    "ds_read_b64_tr_b16 v[4:5], v18 offset:16384\n"
    "ds_read_b64_tr_b16 v[6:7], v18 offset:18432\n"
    "s_add_u32 s36, s61, 24576\n"
    "v_mfma_f32_16x16x32_bf16 a[60:63], v[8:11], a[244:247], a[60:63]\n"
    "v_accvgpr_read_b32 v174, a30\n"
    "v_accvgpr_read_b32 v175, a31\n"
    "v_mfma_f32_16x16x32_bf16 a[60:63], v[12:15], v[44:47], a[60:63]\n"
    "buffer_load_dwordx4 v20, s[40:43], s36 offen nt lds\n"
    "v_mfma_f32_16x16x32_bf16 a[68:71], v[8:11], a[248:251], a[68:71]\n"
    "v_mul_f32 v172, v172, v28\n"
    "v_mul_f32 v173, v173, v28\n"
    "v_mfma_f32_16x16x32_bf16 a[68:71], v[12:15], v[48:51], a[68:71]\n"
    "v_mfma_f32_16x16x32_bf16 a[76:79], v[8:11], a[252:255], a[76:79]\n"
    "v_mul_f32 v174, v174, v28\n"
    "v_mul_f32 v175, v175, v28\n"
    "v_mfma_f32_16x16x32_bf16 a[76:79], v[12:15], v[52:55], a[76:79]\n"
    "v_mfma_f32_16x16x32_bf16 a[84:87], v[8:11], v[32:35], a[84:87]\n"
    "v_cvt_pk_bf16_f32 v238, v172, v173\n"
    "v_cvt_pk_bf16_f32 v239, v174, v175\n"
    "v_mfma_f32_16x16x32_bf16 a[84:87], v[12:15], v[56:59], a[84:87]\n"
    "s_mov_b32 s56, 24576\n"
    "v_mfma_f32_16x16x32_bf16 a[92:95], v[8:11], v[36:39], a[92:95]\n"
    "v_accvgpr_read_b32 v164, a32\n"
    "v_accvgpr_read_b32 v165, a33\n"
    "v_mfma_f32_16x16x32_bf16 a[92:95], v[12:15], v[60:63], a[92:95]\n"
    "s_waitcnt lgkmcnt(0)\n"
    "ds_read_b64_tr_b16 v[8:9], v18 offset:12296\n"
    "ds_read_b64_tr_b16 v[10:11], v18 offset:14344\n"
    "v_mfma_f32_16x16x32_bf16 a[48:51], v[0:3], v[64:67], a[48:51]\n"
    "v_accvgpr_read_b32 v166, a34\n"
    "v_accvgpr_read_b32 v167, a35\n"
    "v_mfma_f32_16x16x32_bf16 a[48:51], v[4:7], v[88:91], a[48:51]\n"
    "ds_read_b64_tr_b16 v[12:13], v18 offset:16392\n"
    "ds_read_b64_tr_b16 v[14:15], v18 offset:18440\n"
    "v_mfma_f32_16x16x32_bf16 a[56:59], v[0:3], v[68:71], a[56:59]\n"
    "v_mul_f32 v164, v164, v29\n"
    "v_mul_f32 v165, v165, v29\n"
    "v_mfma_f32_16x16x32_bf16 a[56:59], v[4:7], v[92:95], a[56:59]\n"
    "buffer_store_dwordx4 v[244:247], v24, s[52:55], s56 offen sc0 sc1\n"
    "v_mfma_f32_16x16x32_bf16 a[64:67], v[0:3], v[72:75], a[64:67]\n"
    "v_mul_f32 v166, v166, v29\n"
    "v_mul_f32 v167, v167, v29\n"
    "v_mfma_f32_16x16x32_bf16 a[64:67], v[4:7], v[96:99], a[64:67]\n"
    "v_mfma_f32_16x16x32_bf16 a[72:75], v[0:3], v[76:79], a[72:75]\n"
    "v_cvt_pk_bf16_f32 v244, v164, v165\n"
    "v_cvt_pk_bf16_f32 v245, v166, v167\n"
    "v_mfma_f32_16x16x32_bf16 a[72:75], v[4:7], v[100:103], a[72:75]\n"
    "buffer_load_dwordx4 v22, s[40:43], s36 offen offset:1024 nt lds\n"
    "v_mfma_f32_16x16x32_bf16 a[80:83], v[0:3], v[80:83], a[80:83]\n"
    "v_accvgpr_read_b32 v172, a36\n"
    "v_accvgpr_read_b32 v173, a37\n"
    "v_mfma_f32_16x16x32_bf16 a[80:83], v[4:7], v[104:107], a[80:83]\n"
    "v_mfma_f32_16x16x32_bf16 a[88:91], v[0:3], v[84:87], a[88:91]\n"
    "v_accvgpr_read_b32 v174, a38\n"
    "v_accvgpr_read_b32 v175, a39\n"
    "v_mfma_f32_16x16x32_bf16 a[88:91], v[4:7], v[108:111], a[88:91]\n"
    "s_waitcnt lgkmcnt(0)\n"
    "ds_read_b64_tr_b16 v[0:1], v18 offset:20480\n"
    "ds_read_b64_tr_b16 v[2:3], v18 offset:22528\n"
    "v_mfma_f32_16x16x32_bf16 a[52:55], v[8:11], v[64:67], a[52:55]\n"
    "v_mul_f32 v172, v172, v29\n"
    "v_mul_f32 v173, v173, v29\n"
    "v_mfma_f32_16x16x32_bf16 a[52:55], v[12:15], v[88:91], a[52:55]\n"
    "ds_read_b64_tr_b16 v[4:5], v18 offset:24576\n"
    "ds_read_b64_tr_b16 v[6:7], v18 offset:26624\n"
    "v_mfma_f32_16x16x32_bf16 a[60:63], v[8:11], v[68:71], a[60:63]\n"
    "v_mul_f32 v174, v174, v29\n"
    "v_mul_f32 v175, v175, v29\n"
    "v_mfma_f32_16x16x32_bf16 a[60:63], v[12:15], v[92:95], a[60:63]\n"
    "v_mfma_f32_16x16x32_bf16 a[68:71], v[8:11], v[72:75], a[68:71]\n"
    "v_cvt_pk_bf16_f32 v246, v172, v173\n"
    "v_cvt_pk_bf16_f32 v247, v174, v175\n"
    "v_mfma_f32_16x16x32_bf16 a[68:71], v[12:15], v[96:99], a[68:71]\n"
    "s_mov_b32 s57, 30720\n"
    "v_mfma_f32_16x16x32_bf16 a[76:79], v[8:11], v[76:79], a[76:79]\n"
    "v_accvgpr_read_b32 v164, a40\n"
    "v_accvgpr_read_b32 v165, a41\n"
    "v_mfma_f32_16x16x32_bf16 a[76:79], v[12:15], v[100:103], a[76:79]\n"
    "s_memrealtime s[92:93]\n"
    "v_mfma_f32_16x16x32_bf16 a[84:87], v[8:11], v[80:83], a[84:87]\n"
    "v_accvgpr_read_b32 v166, a42\n"
    "v_accvgpr_read_b32 v167, a43\n"
    "v_mfma_f32_16x16x32_bf16 a[84:87], v[12:15], v[104:107], a[84:87]\n"
    "v_mfma_f32_16x16x32_bf16 a[92:95], v[8:11], v[84:87], a[92:95]\n"
    "v_mul_f32 v164, v164, v30\n"
    "v_mul_f32 v165, v165, v30\n"
    "v_mfma_f32_16x16x32_bf16 a[92:95], v[12:15], v[108:111], a[92:95]\n"
    "s_waitcnt lgkmcnt(0)\n"
    "ds_read_b64_tr_b16 v[8:9], v18 offset:20488\n"
    "ds_read_b64_tr_b16 v[10:11], v18 offset:22536\n"
    "v_mfma_f32_16x16x32_bf16 a[48:51], v[0:3], v[112:115], a[48:51]\n"
    "v_mul_f32 v166, v166, v30\n"
    "v_mul_f32 v167, v167, v30\n"
    "v_mfma_f32_16x16x32_bf16 a[48:51], v[4:7], v[136:139], a[48:51]\n"
    "s_add_u32 s94, s94, 173\n"
    "s_sub_u32 s95, s94, s92\n"
    "s_cmp_gt_i32 s95, 3\n"
    "s_cbranch_scc1 L_pc9_%=\n"
    "L_pr9_%=:\n"
    "ds_read_b64_tr_b16 v[12:13], v18 offset:24584\n"
    "ds_read_b64_tr_b16 v[14:15], v18 offset:26632\n"
    "v_mfma_f32_16x16x32_bf16 a[56:59], v[0:3], v[116:119], a[56:59]\n"
    "v_accvgpr_read_b32 v172, a44\n"
    "v_accvgpr_read_b32 v173, a45\n"
    "v_mfma_f32_16x16x32_bf16 a[56:59], v[4:7], v[140:143], a[56:59]\n"
    "buffer_load_dwordx4 v23, s[40:43], s36 offen offset:2048 nt lds\n"
    "v_mfma_f32_16x16x32_bf16 a[64:67], v[0:3], v[120:123], a[64:67]\n"
    "v_accvgpr_read_b32 v174, a46\n"
    "v_accvgpr_read_b32 v175, a47\n"
    "v_mfma_f32_16x16x32_bf16 a[64:67], v[4:7], v[144:147], a[64:67]\n"
    "buffer_store_dwordx4 v[252:255], v24, s[52:55], s57 offen sc0 sc1\n"
    "v_mfma_f32_16x16x32_bf16 a[72:75], v[0:3], v[124:127], a[72:75]\n"
    "s_nop 0\n"
    "v_cvt_pk_bf16_f32 v252, v164, v165\n"
    "v_cvt_pk_bf16_f32 v253, v166, v167\n"
    "v_mfma_f32_16x16x32_bf16 a[72:75], v[4:7], v[148:151], a[72:75]\n"
    "v_mfma_f32_16x16x32_bf16 a[80:83], v[0:3], v[128:131], a[80:83]\n"
    "v_mul_f32 v172, v172, v30\n"
    "v_mul_f32 v173, v173, v30\n"
    "v_mfma_f32_16x16x32_bf16 a[80:83], v[4:7], v[152:155], a[80:83]\n"
    "v_mfma_f32_16x16x32_bf16 a[88:91], v[0:3], v[132:135], a[88:91]\n"
    "v_mul_f32 v174, v174, v30\n"
    "v_mfma_f32_16x16x32_bf16 a[88:91], v[4:7], v[156:159], a[88:91]\n"
    "s_waitcnt lgkmcnt(0)\n"
    "ds_read_b64_tr_b16 v[0:1], v18 offset:28672\n"
    "ds_read_b64_tr_b16 v[2:3], v18 offset:30720\n"
    "v_mfma_f32_16x16x32_bf16 a[52:55], v[8:11], v[112:115], a[52:55]\n"
    "v_mul_f32 v175, v175, v30\n"
    "v_mfma_f32_16x16x32_bf16 a[52:55], v[12:15], v[136:139], a[52:55]\n"
    "ds_read_b64_tr_b16 v[4:5], v18 offset:32768\n"
    "ds_read_b64_tr_b16 v[6:7], v18 offset:34816\n"
    "v_mfma_f32_16x16x32_bf16 a[60:63], v[8:11], v[116:119], a[60:63]\n"
    "v_cvt_pk_bf16_f32 v254, v172, v173\n"
    "v_mfma_f32_16x16x32_bf16 a[60:63], v[12:15], v[140:143], a[60:63]\n"
    "v_mfma_f32_16x16x32_bf16 a[68:71], v[8:11], v[120:123], a[68:71]\n"
    "v_cvt_pk_bf16_f32 v255, v174, v175\n"
    "v_mfma_f32_16x16x32_bf16 a[68:71], v[12:15], v[144:147], a[68:71]\n"
    "v_mfma_f32_16x16x32_bf16 a[76:79], v[8:11], v[124:127], a[76:79]\n"
    "v_mfma_f32_16x16x32_bf16 a[76:79], v[12:15], v[148:151], a[76:79]\n"
    "v_mfma_f32_16x16x32_bf16 a[84:87], v[8:11], v[128:131], a[84:87]\n"
    "v_mfma_f32_16x16x32_bf16 a[84:87], v[12:15], v[152:155], a[84:87]\n"
    "v_mfma_f32_16x16x32_bf16 a[92:95], v[8:11], v[132:135], a[92:95]\n"
    "v_mfma_f32_16x16x32_bf16 a[92:95], v[12:15], v[156:159], a[92:95]\n"
    "s_waitcnt lgkmcnt(0)\n"
    "ds_read_b64_tr_b16 v[8:9], v18 offset:28680\n"
    "ds_read_b64_tr_b16 v[10:11], v18 offset:30728\n"
    "s_add_u32 m0, s60, 98304\n"
    "v_mfma_f32_16x16x32_bf16 a[48:51], v[0:3], v[160:163], a[48:51]\n"
    "v_mfma_f32_16x16x32_bf16 a[48:51], v[4:7], v[208:211], a[48:51]\n"
    "ds_read_b64_tr_b16 v[12:13], v18 offset:32776\n"
    "ds_read_b64_tr_b16 v[14:15], v18 offset:34824\n"
    "s_add_u32 s36, s61, 61440\n"
    "v_mfma_f32_16x16x32_bf16 a[56:59], v[0:3], v[168:171], a[56:59]\n"
    "v_mfma_f32_16x16x32_bf16 a[56:59], v[4:7], v[216:219], a[56:59]\n"
    "buffer_load_dwordx4 v20, s[40:43], s36 offen nt lds\n"
    "v_mfma_f32_16x16x32_bf16 a[64:67], v[0:3], v[176:179], a[64:67]\n"
    "v_mfma_f32_16x16x32_bf16 a[64:67], v[4:7], v[224:227], a[64:67]\n"
    "v_mfma_f32_16x16x32_bf16 a[72:75], v[0:3], v[184:187], a[72:75]\n"
    "v_mfma_f32_16x16x32_bf16 a[72:75], v[4:7], v[232:235], a[72:75]\n"
    "v_mfma_f32_16x16x32_bf16 a[80:83], v[0:3], v[192:195], a[80:83]\n"
    "v_mfma_f32_16x16x32_bf16 a[80:83], v[4:7], v[240:243], a[80:83]\n"
    "s_waitcnt vmcnt(28) lgkmcnt(0)\n"
    "s_barrier\n"
    "v_mfma_f32_16x16x32_bf16 a[88:91], v[0:3], v[200:203], a[88:91]\n"
    "v_xor_b32 v18, 64, v18\n"
    "v_subrev_u32 v18, 98304, v18\n"
    "v_mfma_f32_16x16x32_bf16 a[88:91], v[4:7], v[248:251], a[88:91]\n"
    "ds_read_b64_tr_b16 v[0:1], v18 offset:0\n"
    "ds_read_b64_tr_b16 v[2:3], v18 offset:2048\n"
    "v_mfma_f32_16x16x32_bf16 a[52:55], v[8:11], v[160:163], a[52:55]\n"
    "v_mfma_f32_16x16x32_bf16 a[52:55], v[12:15], v[208:211], a[52:55]\n"
    "ds_read_b64_tr_b16 v[4:5], v18 offset:4096\n"
    "ds_read_b64_tr_b16 v[6:7], v18 offset:6144\n"
    "v_mfma_f32_16x16x32_bf16 a[60:63], v[8:11], v[168:171], a[60:63]\n"
    "v_mfma_f32_16x16x32_bf16 a[60:63], v[12:15], v[216:219], a[60:63]\n"
    "v_mfma_f32_16x16x32_bf16 a[68:71], v[8:11], v[176:179], a[68:71]\n"
    "v_mfma_f32_16x16x32_bf16 a[68:71], v[12:15], v[224:227], a[68:71]\n"
    "v_mfma_f32_16x16x32_bf16 a[76:79], v[8:11], v[184:187], a[76:79]\n"
    "v_mfma_f32_16x16x32_bf16 a[76:79], v[12:15], v[232:235], a[76:79]\n"
    "v_mfma_f32_16x16x32_bf16 a[84:87], v[8:11], v[192:195], a[84:87]\n"
    "v_mfma_f32_16x16x32_bf16 a[84:87], v[12:15], v[240:243], a[84:87]\n"
    "buffer_load_dwordx4 v22, s[40:43], s36 offen offset:1024 nt lds\n"
    "v_mfma_f32_16x16x32_bf16 a[92:95], v[8:11], v[200:203], a[92:95]\n"
    "v_mfma_f32_16x16x32_bf16 a[92:95], v[12:15], v[248:251], a[92:95]\n"
    "s_waitcnt lgkmcnt(0)\n"
    "ds_read_b64_tr_b16 v[8:9], v18 offset:8\n"
    "ds_read_b64_tr_b16 v[10:11], v18 offset:2056\n"
    "v_mfma_f32_16x16x32_bf16 a[0:3], v[0:3], a[144:147], 0\n"
    "v_mfma_f32_16x16x32_bf16 a[0:3], v[4:7], a[168:171], a[0:3]\n"
    "ds_read_b64_tr_b16 v[12:13], v18 offset:4104\n"
    "ds_read_b64_tr_b16 v[14:15], v18 offset:6152\n"
    "v_mfma_f32_16x16x32_bf16 a[8:11], v[0:3], a[148:151], 0\n"
    "v_accvgpr_read_b32 v164, a48\n"
    "v_accvgpr_read_b32 v165, a49\n"
    "v_mfma_f32_16x16x32_bf16 a[8:11], v[4:7], a[172:175], a[8:11]\n"
    "v_accvgpr_read_b32 v166, a50\n"
    "v_accvgpr_read_b32 v167, a51\n"
    "v_mfma_f32_16x16x32_bf16 a[16:19], v[0:3], a[152:155], 0\n"
    "v_mul_f32 v164, v164, v25\n"
    "v_mul_f32 v165, v165, v25\n"
    "v_mfma_f32_16x16x32_bf16 a[16:19], v[4:7], a[176:179], a[16:19]\n"
    "v_mul_f32 v166, v166, v25\n"
    "v_mul_f32 v167, v167, v25\n"
    "v_mfma_f32_16x16x32_bf16 a[24:27], v[0:3], a[156:159], 0\n"
    "v_cvt_pk_bf16_f32 v180, v164, v165\n"
    "v_cvt_pk_bf16_f32 v181, v166, v167\n"
    "v_mfma_f32_16x16x32_bf16 a[24:27], v[4:7], a[180:183], a[24:27]\n"
    "v_accvgpr_read_b32 v172, a52\n"
    "v_accvgpr_read_b32 v173, a53\n"
    "v_mfma_f32_16x16x32_bf16 a[32:35], v[0:3], a[160:163], 0\n"
    "v_accvgpr_read_b32 v174, a54\n"
    "v_accvgpr_read_b32 v175, a55\n"
    "v_mfma_f32_16x16x32_bf16 a[32:35], v[4:7], a[184:187], a[32:35]\n"
    "v_mul_f32 v172, v172, v25\n"
    "v_mul_f32 v173, v173, v25\n"
    "v_mfma_f32_16x16x32_bf16 a[40:43], v[0:3], a[164:167], 0\n"
    "v_mul_f32 v174, v174, v25\n"
    "v_mul_f32 v175, v175, v25\n"
    "v_mfma_f32_16x16x32_bf16 a[40:43], v[4:7], a[188:191], a[40:43]\n"
    "s_waitcnt lgkmcnt(0)\n"
    "ds_read_b64_tr_b16 v[0:1], v18 offset:8192\n"
    "ds_read_b64_tr_b16 v[2:3], v18 offset:10240\n"
    "v_mfma_f32_16x16x32_bf16 a[4:7], v[8:11], a[144:147], 0\n"
    "v_cvt_pk_bf16_f32 v182, v172, v173\n"
    "v_cvt_pk_bf16_f32 v183, v174, v175\n"
    "v_mfma_f32_16x16x32_bf16 a[4:7], v[12:15], a[168:171], a[4:7]\n"
    "ds_read_b64_tr_b16 v[4:5], v18 offset:12288\n"
    "ds_read_b64_tr_b16 v[6:7], v18 offset:14336\n"
    "v_mfma_f32_16x16x32_bf16 a[12:15], v[8:11], a[148:151], 0\n"
    "v_mfma_f32_16x16x32_bf16 a[12:15], v[12:15], a[172:175], a[12:15]\n"
    "s_mov_b32 s56, 128\n"
    "s_mov_b64 vcc, s[98:99]\n"
    "v_mfma_f32_16x16x32_bf16 a[20:23], v[8:11], a[152:155], 0\n"
    "v_cndmask_b32_dpp v188, v212, v180, vcc row_ror:8 row_mask:0xf bank_mask:0xf\n"
    "v_cndmask_b32_dpp v189, v213, v181, vcc row_ror:8 row_mask:0xf bank_mask:0xf\n"
    "v_mfma_f32_16x16x32_bf16 a[20:23], v[12:15], a[176:179], a[20:23]\n"
    "v_cndmask_b32_dpp v190, v214, v182, vcc row_ror:8 row_mask:0xf bank_mask:0xf\n"
    "v_cndmask_b32_dpp v191, v215, v183, vcc row_ror:8 row_mask:0xf bank_mask:0xf\n"
    "v_mfma_f32_16x16x32_bf16 a[28:31], v[8:11], a[156:159], 0\n"
    "v_accvgpr_read_b32 v164, a56\n"
    "v_accvgpr_read_b32 v165, a57\n"
    "v_mfma_f32_16x16x32_bf16 a[28:31], v[12:15], a[180:183], a[28:31]\n"
    "buffer_load_dwordx4 v23, s[40:43], s36 offen offset:2048 nt lds\n"
    "v_mfma_f32_16x16x32_bf16 a[36:39], v[8:11], a[160:163], 0\n"
    "v_accvgpr_read_b32 v166, a58\n"
    "v_accvgpr_read_b32 v167, a59\n"
    "v_mfma_f32_16x16x32_bf16 a[36:39], v[12:15], a[184:187], a[36:39]\n"
    "s_mov_b64 vcc, s[96:97]\n"
    "v_cndmask_b32_dpp v212, v180, v212, vcc row_ror:8 row_mask:0xf bank_mask:0xf\n"
    "v_mfma_f32_16x16x32_bf16 a[44:47], v[8:11], a[164:167], 0\n"
    "v_cndmask_b32_dpp v213, v181, v213, vcc row_ror:8 row_mask:0xf bank_mask:0xf\n"
    "v_cndmask_b32_dpp v214, v182, v214, vcc row_ror:8 row_mask:0xf bank_mask:0xf\n"
    "v_mfma_f32_16x16x32_bf16 a[44:47], v[12:15], a[188:191], a[44:47]\n"
    "s_waitcnt lgkmcnt(0)\n"
    "ds_read_b64_tr_b16 v[8:9], v18 offset:8200\n"
    "ds_read_b64_tr_b16 v[10:11], v18 offset:10248\n"
    "s_add_u32 m0, s60, 110592\n"
    "v_mfma_f32_16x16x32_bf16 a[0:3], v[0:3], a[192:195], a[0:3]\n"
    "v_cndmask_b32_dpp v215, v183, v215, vcc row_ror:8 row_mask:0xf bank_mask:0xf\n"
    "v_mul_f32 v164, v164, v26\n"
    "v_mfma_f32_16x16x32_bf16 a[0:3], v[4:7], a[216:219], a[0:3]\n"
    "ds_read_b64_tr_b16 v[12:13], v18 offset:12296\n"
    "ds_read_b64_tr_b16 v[14:15], v18 offset:14344\n"
    "s_add_u32 s36, s61, 98304\n"
    "v_mfma_f32_16x16x32_bf16 a[8:11], v[0:3], a[196:199], a[8:11]\n"
    "v_mul_f32 v165, v165, v26\n"
    "v_mul_f32 v166, v166, v26\n"
    "v_mfma_f32_16x16x32_bf16 a[8:11], v[4:7], a[220:223], a[8:11]\n"
    "buffer_load_dwordx4 v20, s[40:43], s36 offen nt lds\n"
    "v_mfma_f32_16x16x32_bf16 a[16:19], v[0:3], a[200:203], a[16:19]\n"
    "v_mul_f32 v167, v167, v26\n"
    "v_cvt_pk_bf16_f32 v196, v164, v165\n"
    "v_mfma_f32_16x16x32_bf16 a[16:19], v[4:7], a[224:227], a[16:19]\n"
    "buffer_store_dwordx4 v[188:191], v24, s[52:55], s56 offen offset:3072 sc0 sc1\n"
    "v_mfma_f32_16x16x32_bf16 a[24:27], v[0:3], a[204:207], a[24:27]\n"
    "v_cvt_pk_bf16_f32 v197, v166, v167\n"
    "v_accvgpr_read_b32 v172, a60\n"
    "v_mfma_f32_16x16x32_bf16 a[24:27], v[4:7], a[228:231], a[24:27]\n"
    "v_accvgpr_read_b32 v173, a61\n"
    "v_accvgpr_read_b32 v174, a62\n"
    "v_mfma_f32_16x16x32_bf16 a[32:35], v[0:3], a[208:211], a[32:35]\n"
    "v_accvgpr_read_b32 v175, a63\n"
    "v_mul_f32 v172, v172, v26\n"
    "v_mfma_f32_16x16x32_bf16 a[32:35], v[4:7], a[232:235], a[32:35]\n"
    "v_mul_f32 v173, v173, v26\n"
    "s_mov_b32 s57, 6272\n"
    "v_mfma_f32_16x16x32_bf16 a[40:43], v[0:3], a[212:215], a[40:43]\n"
    "v_mul_f32 v174, v174, v26\n"
    "v_mul_f32 v175, v175, v26\n"
    "v_mfma_f32_16x16x32_bf16 a[40:43], v[4:7], a[236:239], a[40:43]\n"
    "s_waitcnt lgkmcnt(0)\n"
    "ds_read_b64_tr_b16 v[0:1], v18 offset:16384\n"
    "ds_read_b64_tr_b16 v[2:3], v18 offset:18432\n"
    "v_mfma_f32_16x16x32_bf16 a[4:7], v[8:11], a[192:195], a[4:7]\n"
    "v_cvt_pk_bf16_f32 v198, v172, v173\n"
    "v_cvt_pk_bf16_f32 v199, v174, v175\n"
    "v_mfma_f32_16x16x32_bf16 a[4:7], v[12:15], a[216:219], a[4:7]\n"
    "ds_read_b64_tr_b16 v[4:5], v18 offset:20480\n"
    "ds_read_b64_tr_b16 v[6:7], v18 offset:22528\n"
    "v_mfma_f32_16x16x32_bf16 a[12:15], v[8:11], a[196:199], a[12:15]\n"
    "v_mfma_f32_16x16x32_bf16 a[12:15], v[12:15], a[220:223], a[12:15]\n"
    "buffer_load_dwordx4 v22, s[40:43], s36 offen offset:1024 nt lds\n"
    "v_mfma_f32_16x16x32_bf16 a[20:23], v[8:11], a[200:203], a[20:23]\n"
    "v_mfma_f32_16x16x32_bf16 a[20:23], v[12:15], a[224:227], a[20:23]\n"
    "s_mov_b64 vcc, s[98:99]\n"
    "v_cndmask_b32_dpp v204, v220, v196, vcc row_ror:8 row_mask:0xf bank_mask:0xf\n"
    "v_cndmask_b32_dpp v205, v221, v197, vcc row_ror:8 row_mask:0xf bank_mask:0xf\n"
    "v_cndmask_b32_dpp v206, v222, v198, vcc row_ror:8 row_mask:0xf bank_mask:0xf\n"
    "v_mfma_f32_16x16x32_bf16 a[28:31], v[8:11], a[204:207], a[28:31]\n"
    "v_cndmask_b32_dpp v207, v223, v199, vcc row_ror:8 row_mask:0xf bank_mask:0xf\n"
    "v_accvgpr_read_b32 v164, a64\n"
    "v_mfma_f32_16x16x32_bf16 a[28:31], v[12:15], a[228:231], a[28:31]\n"
    "s_mov_b64 vcc, s[96:97]\n"
    "v_cndmask_b32_dpp v220, v196, v220, vcc row_ror:8 row_mask:0xf bank_mask:0xf\n"
    "v_mfma_f32_16x16x32_bf16 a[36:39], v[8:11], a[208:211], a[36:39]\n"
    "v_cndmask_b32_dpp v221, v197, v221, vcc row_ror:8 row_mask:0xf bank_mask:0xf\n"
    "v_cndmask_b32_dpp v222, v198, v222, vcc row_ror:8 row_mask:0xf bank_mask:0xf\n"
    "v_mfma_f32_16x16x32_bf16 a[36:39], v[12:15], a[232:235], a[36:39]\n"
    "v_cndmask_b32_dpp v223, v199, v223, vcc row_ror:8 row_mask:0xf bank_mask:0xf\n"
    "v_mfma_f32_16x16x32_bf16 a[44:47], v[8:11], a[212:215], a[44:47]\n"
    "v_accvgpr_read_b32 v165, a65\n"
    "v_accvgpr_read_b32 v166, a66\n"
    "v_mfma_f32_16x16x32_bf16 a[44:47], v[12:15], a[236:239], a[44:47]\n"
    "s_waitcnt lgkmcnt(0)\n"
    "ds_read_b64_tr_b16 v[8:9], v18 offset:16392\n"
    "ds_read_b64_tr_b16 v[10:11], v18 offset:18440\n"
    "v_mfma_f32_16x16x32_bf16 a[0:3], v[0:3], a[240:243], a[0:3]\n"
    "v_accvgpr_read_b32 v167, a67\n"
    "v_mul_f32 v164, v164, v27\n"
    "v_mfma_f32_16x16x32_bf16 a[0:3], v[4:7], v[40:43], a[0:3]\n"
    "ds_read_b64_tr_b16 v[12:13], v18 offset:20488\n"
    "ds_read_b64_tr_b16 v[14:15], v18 offset:22536\n"
    "v_mfma_f32_16x16x32_bf16 a[8:11], v[0:3], a[244:247], a[8:11]\n"
    "v_mul_f32 v165, v165, v27\n"
    "v_mul_f32 v166, v166, v27\n"
    "v_mfma_f32_16x16x32_bf16 a[8:11], v[4:7], v[44:47], a[8:11]\n"
    "buffer_store_dwordx4 v[204:207], v24, s[52:55], s57 offen offset:3072 sc0 sc1\n"
    "v_mfma_f32_16x16x32_bf16 a[16:19], v[0:3], a[248:251], a[16:19]\n"
    "v_mul_f32 v167, v167, v27\n"
    "v_cvt_pk_bf16_f32 v180, v164, v165\n"
    "v_mfma_f32_16x16x32_bf16 a[16:19], v[4:7], v[48:51], a[16:19]\n"
    "v_cvt_pk_bf16_f32 v181, v166, v167\n"
    "v_accvgpr_read_b32 v172, a68\n"
    "v_mfma_f32_16x16x32_bf16 a[24:27], v[0:3], a[252:255], a[24:27]\n"
    "v_accvgpr_read_b32 v173, a69\n"
    "v_accvgpr_read_b32 v174, a70\n"
    "v_mfma_f32_16x16x32_bf16 a[24:27], v[4:7], v[52:55], a[24:27]\n"
    "v_accvgpr_read_b32 v175, a71\n"
    "v_mul_f32 v172, v172, v27\n"
    "v_mfma_f32_16x16x32_bf16 a[32:35], v[0:3], v[32:35], a[32:35]\n"
    "v_mul_f32 v173, v173, v27\n"
    "v_mul_f32 v174, v174, v27\n"
    "v_mfma_f32_16x16x32_bf16 a[32:35], v[4:7], v[56:59], a[32:35]\n"
    "s_mov_b32 s56, 12416\n"
    "s_mov_b64 vcc, s[98:99]\n"
    "v_mfma_f32_16x16x32_bf16 a[40:43], v[0:3], v[36:39], a[40:43]\n"
    "v_add_u32 v18, 73728, v18\n"
    "v_mul_f32 v175, v175, v27\n"
    "v_mfma_f32_16x16x32_bf16 a[40:43], v[4:7], v[60:63], a[40:43]\n"
    "s_waitcnt lgkmcnt(0)\n"
    "ds_read_b64_tr_b16 v[0:1], v18 offset:0\n"
    "ds_read_b64_tr_b16 v[2:3], v18 offset:2048\n"
    "v_mfma_f32_16x16x32_bf16 a[4:7], v[8:11], a[240:243], a[4:7]\n"
    "v_cvt_pk_bf16_f32 v182, v172, v173\n"
    "v_cvt_pk_bf16_f32 v183, v174, v175\n"
    "v_mfma_f32_16x16x32_bf16 a[4:7], v[12:15], v[40:43], a[4:7]\n"
    "ds_read_b64_tr_b16 v[4:5], v18 offset:4096\n"
    "ds_read_b64_tr_b16 v[6:7], v18 offset:6144\n"
    "v_mfma_f32_16x16x32_bf16 a[12:15], v[8:11], a[244:247], a[12:15]\n"
    "v_cndmask_b32_dpp v188, v228, v180, vcc row_ror:8 row_mask:0xf bank_mask:0xf\n"
    "v_cndmask_b32_dpp v189, v229, v181, vcc row_ror:8 row_mask:0xf bank_mask:0xf\n"
    "v_mfma_f32_16x16x32_bf16 a[12:15], v[12:15], v[44:47], a[12:15]\n"
    "buffer_load_dwordx4 v23, s[40:43], s36 offen offset:2048 nt lds\n"
    "v_mfma_f32_16x16x32_bf16 a[20:23], v[8:11], a[248:251], a[20:23]\n"
    "v_cndmask_b32_dpp v190, v230, v182, vcc row_ror:8 row_mask:0xf bank_mask:0xf\n"
    "v_cndmask_b32_dpp v191, v231, v183, vcc row_ror:8 row_mask:0xf bank_mask:0xf\n"
    "v_mfma_f32_16x16x32_bf16 a[20:23], v[12:15], v[48:51], a[20:23]\n"
    "s_mov_b64 vcc, s[96:97]\n"
    "v_cndmask_b32_dpp v228, v180, v228, vcc row_ror:8 row_mask:0xf bank_mask:0xf\n"
    "v_mfma_f32_16x16x32_bf16 a[28:31], v[8:11], a[252:255], a[28:31]\n"
    "v_cndmask_b32_dpp v229, v181, v229, vcc row_ror:8 row_mask:0xf bank_mask:0xf\n"
    "v_cndmask_b32_dpp v230, v182, v230, vcc row_ror:8 row_mask:0xf bank_mask:0xf\n"
    "v_mfma_f32_16x16x32_bf16 a[28:31], v[12:15], v[52:55], a[28:31]\n"
    "v_cndmask_b32_dpp v231, v183, v231, vcc row_ror:8 row_mask:0xf bank_mask:0xf\n"
    "v_mfma_f32_16x16x32_bf16 a[36:39], v[8:11], v[32:35], a[36:39]\n"
    "v_accvgpr_read_b32 v164, a72\n"
    "v_accvgpr_read_b32 v165, a73\n"
    "v_mfma_f32_16x16x32_bf16 a[36:39], v[12:15], v[56:59], a[36:39]\n"
    "buffer_store_dwordx4 v[188:191], v24, s[52:55], s56 offen offset:3072 sc0 sc1\n"
    "v_mfma_f32_16x16x32_bf16 a[44:47], v[8:11], v[36:39], a[44:47]\n"
    "v_accvgpr_read_b32 v166, a74\n"
    "v_accvgpr_read_b32 v167, a75\n"
    "v_mfma_f32_16x16x32_bf16 a[44:47], v[12:15], v[60:63], a[44:47]\n"
    "s_waitcnt lgkmcnt(0)\n"
    "ds_read_b64_tr_b16 v[8:9], v18 offset:8\n"
    "ds_read_b64_tr_b16 v[10:11], v18 offset:2056\n"
    "v_mfma_f32_16x16x32_bf16 a[0:3], v[0:3], v[64:67], a[0:3]\n"
    "v_mul_f32 v164, v164, v28\n"
    "v_mul_f32 v165, v165, v28\n"
    "v_mfma_f32_16x16x32_bf16 a[0:3], v[4:7], v[88:91], a[0:3]\n"
    "ds_read_b64_tr_b16 v[12:13], v18 offset:4104\n"
    "ds_read_b64_tr_b16 v[14:15], v18 offset:6152\n"
    "v_mfma_f32_16x16x32_bf16 a[8:11], v[0:3], v[68:71], a[8:11]\n"
    "v_mul_f32 v166, v166, v28\n"
    "v_mul_f32 v167, v167, v28\n"
    "v_mfma_f32_16x16x32_bf16 a[8:11], v[4:7], v[92:95], a[8:11]\n"
    "v_cvt_pk_bf16_f32 v196, v164, v165\n"
    "v_cvt_pk_bf16_f32 v197, v166, v167\n"
    "v_mfma_f32_16x16x32_bf16 a[16:19], v[0:3], v[72:75], a[16:19]\n"
    "v_accvgpr_read_b32 v172, a76\n"
    "v_accvgpr_read_b32 v173, a77\n"
    "v_mfma_f32_16x16x32_bf16 a[16:19], v[4:7], v[96:99], a[16:19]\n"
    "v_accvgpr_read_b32 v174, a78\n"
    "v_accvgpr_read_b32 v175, a79\n"
    "v_mfma_f32_16x16x32_bf16 a[24:27], v[0:3], v[76:79], a[24:27]\n"
    "v_mul_f32 v172, v172, v28\n"
    "v_mul_f32 v173, v173, v28\n"
    "v_mfma_f32_16x16x32_bf16 a[24:27], v[4:7], v[100:103], a[24:27]\n"
    "s_mov_b32 s57, 18560\n"
    "s_mov_b64 vcc, s[98:99]\n"
    "v_mfma_f32_16x16x32_bf16 a[32:35], v[0:3], v[80:83], a[32:35]\n"
    "v_mul_f32 v174, v174, v28\n"
    "v_mul_f32 v175, v175, v28\n"
    "v_mfma_f32_16x16x32_bf16 a[32:35], v[4:7], v[104:107], a[32:35]\n"
    "v_cvt_pk_bf16_f32 v198, v172, v173\n"
    "v_cvt_pk_bf16_f32 v199, v174, v175\n"
    "v_mfma_f32_16x16x32_bf16 a[40:43], v[0:3], v[84:87], a[40:43]\n"
    "v_cndmask_b32_dpp v204, v236, v196, vcc row_ror:8 row_mask:0xf bank_mask:0xf\n"
    "v_cndmask_b32_dpp v205, v237, v197, vcc row_ror:8 row_mask:0xf bank_mask:0xf\n"
    "v_mfma_f32_16x16x32_bf16 a[40:43], v[4:7], v[108:111], a[40:43]\n"
    "s_waitcnt lgkmcnt(0)\n"
    "ds_read_b64_tr_b16 v[0:1], v18 offset:8192\n"
    "ds_read_b64_tr_b16 v[2:3], v18 offset:10240\n"
    "s_add_u32 m0, s60, 122880\n"
    "v_mfma_f32_16x16x32_bf16 a[4:7], v[8:11], v[64:67], a[4:7]\n"
    "v_cndmask_b32_dpp v206, v238, v198, vcc row_ror:8 row_mask:0xf bank_mask:0xf\n"
    "v_cndmask_b32_dpp v207, v239, v199, vcc row_ror:8 row_mask:0xf bank_mask:0xf\n"
    "v_mfma_f32_16x16x32_bf16 a[4:7], v[12:15], v[88:91], a[4:7]\n"
    "ds_read_b64_tr_b16 v[4:5], v18 offset:12288\n"
    "ds_read_b64_tr_b16 v[6:7], v18 offset:14336\n"
    "s_add_u32 s36, s61, 135168\n"
    "v_mfma_f32_16x16x32_bf16 a[12:15], v[8:11], v[68:71], a[12:15]\n"
    "v_accvgpr_read_b32 v164, a80\n"
    "v_accvgpr_read_b32 v165, a81\n"
    "v_mfma_f32_16x16x32_bf16 a[12:15], v[12:15], v[92:95], a[12:15]\n"
    "buffer_load_dwordx4 v20, s[40:43], s36 offen nt lds\n"
    "v_mfma_f32_16x16x32_bf16 a[20:23], v[8:11], v[72:75], a[20:23]\n"
    "v_accvgpr_read_b32 v166, a82\n"
    "v_accvgpr_read_b32 v167, a83\n"
    "v_mfma_f32_16x16x32_bf16 a[20:23], v[12:15], v[96:99], a[20:23]\n"
    "s_mov_b64 vcc, s[96:97]\n"
    "v_cndmask_b32_dpp v236, v196, v236, vcc row_ror:8 row_mask:0xf bank_mask:0xf\n"
    "v_mfma_f32_16x16x32_bf16 a[28:31], v[8:11], v[76:79], a[28:31]\n"
    "v_cndmask_b32_dpp v237, v197, v237, vcc row_ror:8 row_mask:0xf bank_mask:0xf\n"
    "v_cndmask_b32_dpp v238, v198, v238, vcc row_ror:8 row_mask:0xf bank_mask:0xf\n"
    "v_mfma_f32_16x16x32_bf16 a[28:31], v[12:15], v[100:103], a[28:31]\n"
    "s_memrealtime s[92:93]\n"
    "v_cndmask_b32_dpp v239, v199, v239, vcc row_ror:8 row_mask:0xf bank_mask:0xf\n"
    "v_mfma_f32_16x16x32_bf16 a[36:39], v[8:11], v[80:83], a[36:39]\n"
    "v_mul_f32 v164, v164, v29\n"
    "v_mul_f32 v165, v165, v29\n"
    "v_mfma_f32_16x16x32_bf16 a[36:39], v[12:15], v[104:107], a[36:39]\n"
    "buffer_store_dwordx4 v[204:207], v24, s[52:55], s57 offen offset:3072 sc0 sc1\n"
    "v_mfma_f32_16x16x32_bf16 a[44:47], v[8:11], v[84:87], a[44:47]\n"
    "v_mul_f32 v166, v166, v29\n"
    "v_mul_f32 v167, v167, v29\n"
    "v_mfma_f32_16x16x32_bf16 a[44:47], v[12:15], v[108:111], a[44:47]\n"
    "s_waitcnt lgkmcnt(0)\n"
    "ds_read_b64_tr_b16 v[8:9], v18 offset:8200\n"
    "ds_read_b64_tr_b16 v[10:11], v18 offset:10248\n"
    "v_mfma_f32_16x16x32_bf16 a[0:3], v[0:3], v[112:115], a[0:3]\n"
    "v_cvt_pk_bf16_f32 v180, v164, v165\n"
    "v_cvt_pk_bf16_f32 v181, v166, v167\n"
    "v_mfma_f32_16x16x32_bf16 a[0:3], v[4:7], v[136:139], a[0:3]\n"
    "s_add_u32 s94, s94, 172\n"
    "s_sub_u32 s95, s94, s92\n"
    "s_cmp_gt_i32 s95, 3\n"
    "s_cbranch_scc1 L_pc10_%=\n"
    "L_pr10_%=:\n"
    "ds_read_b64_tr_b16 v[12:13], v18 offset:12296\n"
    "ds_read_b64_tr_b16 v[14:15], v18 offset:14344\n"
    "v_mfma_f32_16x16x32_bf16 a[8:11], v[0:3], v[116:119], a[8:11]\n"
    "v_accvgpr_read_b32 v172, a84\n"
    "v_accvgpr_read_b32 v173, a85\n"
    "v_mfma_f32_16x16x32_bf16 a[8:11], v[4:7], v[140:143], a[8:11]\n"
    "v_accvgpr_read_b32 v174, a86\n"
    "v_accvgpr_read_b32 v175, a87\n"
    "v_mfma_f32_16x16x32_bf16 a[16:19], v[0:3], v[120:123], a[16:19]\n"
    "v_mul_f32 v172, v172, v29\n"
    "v_mul_f32 v173, v173, v29\n"
    "v_mfma_f32_16x16x32_bf16 a[16:19], v[4:7], v[144:147], a[16:19]\n"
    "s_mov_b32 s56, 24704\n"
    "s_mov_b64 vcc, s[98:99]\n"
    "v_mfma_f32_16x16x32_bf16 a[24:27], v[0:3], v[124:127], a[24:27]\n"
    "v_mul_f32 v174, v174, v29\n"
    "v_mul_f32 v175, v175, v29\n"
    "v_mfma_f32_16x16x32_bf16 a[24:27], v[4:7], v[148:151], a[24:27]\n"
    "v_cvt_pk_bf16_f32 v182, v172, v173\n"
    "v_cvt_pk_bf16_f32 v183, v174, v175\n"
    "v_mfma_f32_16x16x32_bf16 a[32:35], v[0:3], v[128:131], a[32:35]\n"
    "v_cndmask_b32_dpp v188, v244, v180, vcc row_ror:8 row_mask:0xf bank_mask:0xf\n"
    "v_cndmask_b32_dpp v189, v245, v181, vcc row_ror:8 row_mask:0xf bank_mask:0xf\n"
    "v_mfma_f32_16x16x32_bf16 a[32:35], v[4:7], v[152:155], a[32:35]\n"
    "v_cndmask_b32_dpp v190, v246, v182, vcc row_ror:8 row_mask:0xf bank_mask:0xf\n"
    "v_cndmask_b32_dpp v191, v247, v183, vcc row_ror:8 row_mask:0xf bank_mask:0xf\n"
    "v_mfma_f32_16x16x32_bf16 a[40:43], v[0:3], v[132:135], a[40:43]\n"
    "v_accvgpr_read_b32 v164, a88\n"
    "v_accvgpr_read_b32 v165, a89\n"
    "v_mfma_f32_16x16x32_bf16 a[40:43], v[4:7], v[156:159], a[40:43]\n"
    "s_waitcnt lgkmcnt(0)\n"
    "ds_read_b64_tr_b16 v[0:1], v18 offset:16384\n"
    "ds_read_b64_tr_b16 v[2:3], v18 offset:18432\n"
    "v_mfma_f32_16x16x32_bf16 a[4:7], v[8:11], v[112:115], a[4:7]\n"
    "v_accvgpr_read_b32 v166, a90\n"
    "v_accvgpr_read_b32 v167, a91\n"
    "v_mfma_f32_16x16x32_bf16 a[4:7], v[12:15], v[136:139], a[4:7]\n"
    "ds_read_b64_tr_b16 v[4:5], v18 offset:20480\n"
    "ds_read_b64_tr_b16 v[6:7], v18 offset:22528\n"
    "v_mfma_f32_16x16x32_bf16 a[12:15], v[8:11], v[116:119], a[12:15]\n"
    "v_mul_f32 v164, v164, v30\n"
    "v_mul_f32 v165, v165, v30\n"
    "v_mfma_f32_16x16x32_bf16 a[12:15], v[12:15], v[140:143], a[12:15]\n"
    "buffer_load_dwordx4 v22, s[40:43], s36 offen offset:1024 nt lds\n"
    "v_mfma_f32_16x16x32_bf16 a[20:23], v[8:11], v[120:123], a[20:23]\n"
    "v_mul_f32 v166, v166, v30\n"
    "v_mul_f32 v167, v167, v30\n"
    "v_mfma_f32_16x16x32_bf16 a[20:23], v[12:15], v[144:147], a[20:23]\n"
    "s_mov_b64 vcc, s[96:97]\n"
    "v_cndmask_b32_dpp v244, v180, v244, vcc row_ror:8 row_mask:0xf bank_mask:0xf\n"
    "v_mfma_f32_16x16x32_bf16 a[28:31], v[8:11], v[124:127], a[28:31]\n"
    "v_cndmask_b32_dpp v245, v181, v245, vcc row_ror:8 row_mask:0xf bank_mask:0xf\n"
    "v_cndmask_b32_dpp v246, v182, v246, vcc row_ror:8 row_mask:0xf bank_mask:0xf\n"
    "v_mfma_f32_16x16x32_bf16 a[28:31], v[12:15], v[148:151], a[28:31]\n"
    "v_cndmask_b32_dpp v247, v183, v247, vcc row_ror:8 row_mask:0xf bank_mask:0xf\n"
    "v_mfma_f32_16x16x32_bf16 a[36:39], v[8:11], v[128:131], a[36:39]\n"
    "v_cvt_pk_bf16_f32 v196, v164, v165\n"
    "v_cvt_pk_bf16_f32 v197, v166, v167\n"
    "v_mfma_f32_16x16x32_bf16 a[36:39], v[12:15], v[152:155], a[36:39]\n"
    "buffer_store_dwordx4 v[188:191], v24, s[52:55], s56 offen offset:3072 sc0 sc1\n"
    "v_mfma_f32_16x16x32_bf16 a[44:47], v[8:11], v[132:135], a[44:47]\n"
    "v_accvgpr_read_b32 v172, a92\n"
    "v_accvgpr_read_b32 v173, a93\n"
    "v_mfma_f32_16x16x32_bf16 a[44:47], v[12:15], v[156:159], a[44:47]\n"
    "s_waitcnt lgkmcnt(0)\n"
    "ds_read_b64_tr_b16 v[8:9], v18 offset:16392\n"
    "ds_read_b64_tr_b16 v[10:11], v18 offset:18440\n"
    "v_mfma_f32_16x16x32_bf16 a[0:3], v[0:3], v[160:163], a[0:3]\n"
    "v_accvgpr_read_b32 v174, a94\n"
    "v_accvgpr_read_b32 v175, a95\n"
    "v_mfma_f32_16x16x32_bf16 a[0:3], v[4:7], v[208:211], a[0:3]\n"
    "ds_read_b64_tr_b16 v[12:13], v18 offset:20488\n"
    "ds_read_b64_tr_b16 v[14:15], v18 offset:22536\n"
    "v_mfma_f32_16x16x32_bf16 a[8:11], v[0:3], v[168:171], a[8:11]\n"
    "v_mul_f32 v172, v172, v30\n"
    "v_mul_f32 v173, v173, v30\n"
    "v_mfma_f32_16x16x32_bf16 a[8:11], v[4:7], v[216:219], a[8:11]\n"
    "s_mov_b32 s57, 30848\n"
    "s_mov_b64 vcc, s[98:99]\n"
    "v_mfma_f32_16x16x32_bf16 a[16:19], v[0:3], v[176:179], a[16:19]\n"
    "v_mul_f32 v174, v174, v30\n"
    "v_mul_f32 v175, v175, v30\n"
    "v_mfma_f32_16x16x32_bf16 a[16:19], v[4:7], v[224:227], a[16:19]\n"
    "v_cvt_pk_bf16_f32 v198, v172, v173\n"
    "v_mfma_f32_16x16x32_bf16 a[24:27], v[0:3], v[184:187], a[24:27]\n"
    "v_cvt_pk_bf16_f32 v199, v174, v175\n"
    "v_cndmask_b32_dpp v204, v252, v196, vcc row_ror:8 row_mask:0xf bank_mask:0xf\n"
    "v_mfma_f32_16x16x32_bf16 a[24:27], v[4:7], v[232:235], a[24:27]\n"
    "v_cndmask_b32_dpp v205, v253, v197, vcc row_ror:8 row_mask:0xf bank_mask:0xf\n"
    "v_mfma_f32_16x16x32_bf16 a[32:35], v[0:3], v[192:195], a[32:35]\n"
    "v_cndmask_b32_dpp v206, v254, v198, vcc row_ror:8 row_mask:0xf bank_mask:0xf\n"
    "v_cndmask_b32_dpp v207, v255, v199, vcc row_ror:8 row_mask:0xf bank_mask:0xf\n"
    "v_mfma_f32_16x16x32_bf16 a[32:35], v[4:7], v[240:243], a[32:35]\n"
    "s_mov_b64 vcc, s[96:97]\n"
    "v_mfma_f32_16x16x32_bf16 a[40:43], v[0:3], v[200:203], a[40:43]\n"
    "v_xor_b32 v18, 64, v18\n"
    "v_subrev_u32 v18, 73728, v18\n"
    "v_mfma_f32_16x16x32_bf16 a[40:43], v[4:7], v[248:251], a[40:43]\n"
    "s_waitcnt lgkmcnt(0)\n"
    "ds_read_b64_tr_b16 v[0:1], v18 offset:0\n"
    "ds_read_b64_tr_b16 v[2:3], v18 offset:2048\n"
    "v_mfma_f32_16x16x32_bf16 a[4:7], v[8:11], v[160:163], a[4:7]\n"
    "v_cndmask_b32_dpp v252, v196, v252, vcc row_ror:8 row_mask:0xf bank_mask:0xf\n"
    "v_cndmask_b32_dpp v253, v197, v253, vcc row_ror:8 row_mask:0xf bank_mask:0xf\n"
    "v_mfma_f32_16x16x32_bf16 a[4:7], v[12:15], v[208:211], a[4:7]\n"
    "ds_read_b64_tr_b16 v[4:5], v18 offset:4096\n"
    "ds_read_b64_tr_b16 v[6:7], v18 offset:6144\n"
    "v_mfma_f32_16x16x32_bf16 a[12:15], v[8:11], v[168:171], a[12:15]\n"
    "v_cndmask_b32_dpp v254, v198, v254, vcc row_ror:8 row_mask:0xf bank_mask:0xf\n"
    "v_cndmask_b32_dpp v255, v199, v255, vcc row_ror:8 row_mask:0xf bank_mask:0xf\n"
    "v_mfma_f32_16x16x32_bf16 a[12:15], v[12:15], v[216:219], a[12:15]\n"
    "buffer_load_dwordx4 v23, s[40:43], s36 offen offset:2048 nt lds\n"
    "v_mfma_f32_16x16x32_bf16 a[20:23], v[8:11], v[176:179], a[20:23]\n"
    "v_mfma_f32_16x16x32_bf16 a[20:23], v[12:15], v[224:227], a[20:23]\n"
    "s_add_u32 s64, s64, 1\n"
    "v_mfma_f32_16x16x32_bf16 a[28:31], v[8:11], v[184:187], a[28:31]\n"
    "v_mfma_f32_16x16x32_bf16 a[28:31], v[12:15], v[232:235], a[28:31]\n"
    "buffer_store_dwordx4 v[204:207], v24, s[52:55], s57 offen offset:3072 sc0 sc1\n"
    "v_mfma_f32_16x16x32_bf16 a[36:39], v[8:11], v[192:195], a[36:39]\n"
    "v_mfma_f32_16x16x32_bf16 a[36:39], v[12:15], v[240:243], a[36:39]\n"
    "v_mfma_f32_16x16x32_bf16 a[44:47], v[8:11], v[200:203], a[44:47]\n"
    "v_mfma_f32_16x16x32_bf16 a[44:47], v[12:15], v[248:251], a[44:47]\n"
    "s_waitcnt lgkmcnt(0)\n"
    "ds_read_b64_tr_b16 v[8:9], v18 offset:8\n"
    "ds_read_b64_tr_b16 v[10:11], v18 offset:2056\n"
    "v_mfma_f32_16x16x32_bf16 a[48:51], v[0:3], a[144:147], 0\n"
    "v_mfma_f32_16x16x32_bf16 a[48:51], v[4:7], a[168:171], a[48:51]\n"
    "ds_read_b64_tr_b16 v[12:13], v18 offset:4104\n"
    "ds_read_b64_tr_b16 v[14:15], v18 offset:6152\n"
    "v_mfma_f32_16x16x32_bf16 a[56:59], v[0:3], a[148:151], 0\n"
    "v_accvgpr_read_b32 v164, a0\n"
    "v_accvgpr_read_b32 v165, a1\n"
    "v_mfma_f32_16x16x32_bf16 a[56:59], v[4:7], a[172:175], a[56:59]\n"
    "s_mov_b32 s56, 128\n"
    "v_accvgpr_read_b32 v166, a2\n"
    "v_accvgpr_read_b32 v167, a3\n"
    "v_mfma_f32_16x16x32_bf16 a[64:67], v[0:3], a[152:155], 0\n"
    "v_mul_f32 v164, v164, v25\n"
    "v_mul_f32 v165, v165, v25\n"
    "v_mfma_f32_16x16x32_bf16 a[64:67], v[4:7], a[176:179], a[64:67]\n"
    "buffer_store_dwordx4 v[212:215], v24, s[52:55], s56 offen sc0 sc1\n"
    "v_mfma_f32_16x16x32_bf16 a[72:75], v[0:3], a[156:159], 0\n"
    "v_mul_f32 v166, v166, v25\n"
    "v_mul_f32 v167, v167, v25\n"
    "v_mfma_f32_16x16x32_bf16 a[72:75], v[4:7], a[180:183], a[72:75]\n"
    "v_cvt_pk_bf16_f32 v212, v164, v165\n"
    "v_cvt_pk_bf16_f32 v213, v166, v167\n"
    "v_mfma_f32_16x16x32_bf16 a[80:83], v[0:3], a[160:163], 0\n"
    "v_accvgpr_read_b32 v172, a4\n"
    "v_accvgpr_read_b32 v173, a5\n"
    "v_mfma_f32_16x16x32_bf16 a[80:83], v[4:7], a[184:187], a[80:83]\n"
    "v_accvgpr_read_b32 v174, a6\n"
    "v_accvgpr_read_b32 v175, a7\n"
    "v_mul_f32 v172, v172, v25\n"
    "v_mfma_f32_16x16x32_bf16 a[88:91], v[0:3], a[164:167], 0\n"
    "v_mul_f32 v173, v173, v25\n"
    "v_mul_f32 v174, v174, v25\n"
    "v_mfma_f32_16x16x32_bf16 a[88:91], v[4:7], a[188:191], a[88:91]\n"
    "s_waitcnt lgkmcnt(0)\n"
    "ds_read_b64_tr_b16 v[0:1], v18 offset:8192\n"
    "ds_read_b64_tr_b16 v[2:3], v18 offset:10240\n"
    "v_mfma_f32_16x16x32_bf16 a[52:55], v[8:11], a[144:147], 0\n"
    "v_mul_f32 v175, v175, v25\n"
    "v_cvt_pk_bf16_f32 v214, v172, v173\n"
    "v_mfma_f32_16x16x32_bf16 a[52:55], v[12:15], a[168:171], a[52:55]\n"
    "ds_read_b64_tr_b16 v[4:5], v18 offset:12288\n"
    "ds_read_b64_tr_b16 v[6:7], v18 offset:14336\n"
    "v_cvt_pk_bf16_f32 v215, v174, v175\n"
    "v_mfma_f32_16x16x32_bf16 a[60:63], v[8:11], a[148:151], 0\n"
    "v_accvgpr_read_b32 v164, a8\n"
    "v_accvgpr_read_b32 v165, a9\n"
    "v_mfma_f32_16x16x32_bf16 a[60:63], v[12:15], a[172:175], a[60:63]\n"
    "s_mov_b32 s57, 6272\n"
    "v_accvgpr_read_b32 v166, a10\n"
    "v_accvgpr_read_b32 v167, a11\n"
    "v_mfma_f32_16x16x32_bf16 a[68:71], v[8:11], a[152:155], 0\n"
    "v_mul_f32 v164, v164, v26\n"
    "v_mul_f32 v165, v165, v26\n"
    "v_mfma_f32_16x16x32_bf16 a[68:71], v[12:15], a[176:179], a[68:71]\n"
    "buffer_store_dwordx4 v[220:223], v24, s[52:55], s57 offen sc0 sc1\n"
    "v_mfma_f32_16x16x32_bf16 a[76:79], v[8:11], a[156:159], 0\n"
    "v_mul_f32 v166, v166, v26\n"
    "v_mul_f32 v167, v167, v26\n"
    "v_mfma_f32_16x16x32_bf16 a[76:79], v[12:15], a[180:183], a[76:79]\n"
    "s_memrealtime s[92:93]\n"
    "v_cvt_pk_bf16_f32 v220, v164, v165\n"
    "v_cvt_pk_bf16_f32 v221, v166, v167\n"
    "v_mfma_f32_16x16x32_bf16 a[84:87], v[8:11], a[160:163], 0\n"
    "v_accvgpr_read_b32 v172, a12\n"
    "v_accvgpr_read_b32 v173, a13\n"
    "v_mfma_f32_16x16x32_bf16 a[84:87], v[12:15], a[184:187], a[84:87]\n"
    "v_accvgpr_read_b32 v174, a14\n"
    "v_accvgpr_read_b32 v175, a15\n"
    "v_mfma_f32_16x16x32_bf16 a[92:95], v[8:11], a[164:167], 0\n"
    "v_mul_f32 v172, v172, v26\n"
    "v_mul_f32 v173, v173, v26\n"
    "v_mfma_f32_16x16x32_bf16 a[92:95], v[12:15], a[188:191], a[92:95]\n"
    "s_waitcnt lgkmcnt(0)\n"
    "ds_read_b64_tr_b16 v[8:9], v18 offset:8200\n"
    "ds_read_b64_tr_b16 v[10:11], v18 offset:10248\n"
    "v_mfma_f32_16x16x32_bf16 a[48:51], v[0:3], a[192:195], a[48:51]\n"
    "v_mul_f32 v174, v174, v26\n"
    "v_mul_f32 v175, v175, v26\n"
    "v_mfma_f32_16x16x32_bf16 a[48:51], v[4:7], a[216:219], a[48:51]\n"
    "s_add_u32 s94, s94, 86\n"
    "s_sub_u32 s95, s94, s92\n"
    "s_cmp_gt_i32 s95, 3\n"
    "s_cbranch_scc1 L_pc11_%=\n"
    "L_pr11_%=:\n"
    "ds_read_b64_tr_b16 v[12:13], v18 offset:12296\n"
    "ds_read_b64_tr_b16 v[14:15], v18 offset:14344\n"
    "v_mfma_f32_16x16x32_bf16 a[56:59], v[0:3], a[196:199], a[56:59]\n"
    "v_cvt_pk_bf16_f32 v222, v172, v173\n"
    "v_cvt_pk_bf16_f32 v223, v174, v175\n"
    "v_mfma_f32_16x16x32_bf16 a[56:59], v[4:7], a[220:223], a[56:59]\n"
    "v_mfma_f32_16x16x32_bf16 a[64:67], v[0:3], a[200:203], a[64:67]\n"
    "v_accvgpr_read_b32 v164, a16\n"
    "v_accvgpr_read_b32 v165, a17\n"
    "v_mfma_f32_16x16x32_bf16 a[64:67], v[4:7], a[224:227], a[64:67]\n"
    "v_mfma_f32_16x16x32_bf16 a[72:75], v[0:3], a[204:207], a[72:75]\n"
    "v_accvgpr_read_b32 v166, a18\n"
    "v_accvgpr_read_b32 v167, a19\n"
    "v_mfma_f32_16x16x32_bf16 a[72:75], v[4:7], a[228:231], a[72:75]\n"
    "s_waitcnt vmcnt(28) lgkmcnt(2)\n"
    "s_barrier\n"
    "v_mfma_f32_16x16x32_bf16 a[80:83], v[0:3], a[208:211], a[80:83]\n"
    "v_mul_f32 v164, v164, v27\n"
    "v_mul_f32 v165, v165, v27\n"
    "v_mfma_f32_16x16x32_bf16 a[80:83], v[4:7], a[232:235], a[80:83]\n"
    "ds_read_b128 a[0:3], v16 offset:0\n"
    "ds_read_b128 a[4:7], v17 offset:0\n"
    "s_mov_b32 s56, 12416\n"
    "v_mfma_f32_16x16x32_bf16 a[88:91], v[0:3], a[212:215], a[88:91]\n"
    "v_mul_f32 v166, v166, v27\n"
    "v_mul_f32 v167, v167, v27\n"
    "v_mfma_f32_16x16x32_bf16 a[88:91], v[4:7], a[236:239], a[88:91]\n"
    "s_waitcnt lgkmcnt(2)\n"
    "ds_read_b64_tr_b16 v[0:1], v18 offset:16384\n"
    "ds_read_b64_tr_b16 v[2:3], v18 offset:18432\n"
    "s_add_u32 m0, s60, 0\n"
    "v_mfma_f32_16x16x32_bf16 a[52:55], v[8:11], a[192:195], a[52:55]\n"
    "v_accvgpr_read_b32 v172, a20\n"
    "v_accvgpr_read_b32 v173, a21\n"
    "v_mfma_f32_16x16x32_bf16 a[52:55], v[12:15], a[216:219], a[52:55]\n"
    "ds_read_b64_tr_b16 v[4:5], v18 offset:20480\n"
    "ds_read_b64_tr_b16 v[6:7], v18 offset:22528\n"
    "s_add_u32 s36, s61, 12288\n"
    "v_mfma_f32_16x16x32_bf16 a[60:63], v[8:11], a[196:199], a[60:63]\n"
    "v_accvgpr_read_b32 v174, a22\n"
    "v_accvgpr_read_b32 v175, a23\n"
    "v_mfma_f32_16x16x32_bf16 a[60:63], v[12:15], a[220:223], a[60:63]\n"
    "buffer_load_dwordx4 v20, s[44:47], s36 offen nt lds\n"
    "v_mfma_f32_16x16x32_bf16 a[68:71], v[8:11], a[200:203], a[68:71]\n"
    "v_mul_f32 v172, v172, v27\n"
    "v_mul_f32 v173, v173, v27\n"
    "v_mfma_f32_16x16x32_bf16 a[68:71], v[12:15], a[224:227], a[68:71]\n"
    "buffer_store_dwordx4 v[228:231], v24, s[52:55], s56 offen sc0 sc1\n"
    "v_mfma_f32_16x16x32_bf16 a[76:79], v[8:11], a[204:207], a[76:79]\n"
    "s_nop 0\n"
    "v_cvt_pk_bf16_f32 v228, v164, v165\n"
    "v_cvt_pk_bf16_f32 v229, v166, v167\n"
    "v_mfma_f32_16x16x32_bf16 a[76:79], v[12:15], a[228:231], a[76:79]\n"
    "v_mul_f32 v174, v174, v27\n"
    "v_mul_f32 v175, v175, v27\n"
    "v_cvt_pk_bf16_f32 v230, v172, v173\n"
    "v_mfma_f32_16x16x32_bf16 a[84:87], v[8:11], a[208:211], a[84:87]\n"
    "v_cvt_pk_bf16_f32 v231, v174, v175\n"
    "v_accvgpr_read_b32 v164, a24\n"
    "v_mfma_f32_16x16x32_bf16 a[84:87], v[12:15], a[232:235], a[84:87]\n"
    "ds_read_b128 a[8:11], v16 offset:1024\n"
    "ds_read_b128 a[12:15], v17 offset:1024\n"
    "s_mov_b32 s57, 18560\n"
    "v_mfma_f32_16x16x32_bf16 a[92:95], v[8:11], a[212:215], a[92:95]\n"
    "v_accvgpr_read_b32 v165, a25\n"
    "v_accvgpr_read_b32 v166, a26\n"
    "v_mfma_f32_16x16x32_bf16 a[92:95], v[12:15], a[236:239], a[92:95]\n"
    "s_waitcnt lgkmcnt(2)\n"
    "ds_read_b64_tr_b16 v[8:9], v18 offset:16392\n"
    "ds_read_b64_tr_b16 v[10:11], v18 offset:18440\n"
    "v_accvgpr_read_b32 v167, a27\n"
    "v_mfma_f32_16x16x32_bf16 a[48:51], v[0:3], a[240:243], a[48:51]\n"
    "v_mul_f32 v164, v164, v28\n"
    "v_mul_f32 v165, v165, v28\n"
    "v_mfma_f32_16x16x32_bf16 a[48:51], v[4:7], v[40:43], a[48:51]\n"
    "ds_read_b64_tr_b16 v[12:13], v18 offset:20488\n"
    "ds_read_b64_tr_b16 v[14:15], v18 offset:22536\n"
    "v_mfma_f32_16x16x32_bf16 a[56:59], v[0:3], a[244:247], a[56:59]\n"
    "v_mul_f32 v166, v166, v28\n"
    "v_mul_f32 v167, v167, v28\n"
    "v_mfma_f32_16x16x32_bf16 a[56:59], v[4:7], v[44:47], a[56:59]\n"
    "buffer_load_dwordx4 v22, s[44:47], s36 offen offset:1024 nt lds\n"
    "v_mfma_f32_16x16x32_bf16 a[64:67], v[0:3], a[248:251], a[64:67]\n"
    "v_accvgpr_read_b32 v172, a28\n"
    "v_accvgpr_read_b32 v173, a29\n"
    "v_mfma_f32_16x16x32_bf16 a[64:67], v[4:7], v[48:51], a[64:67]\n"
    "buffer_store_dwordx4 v[236:239], v24, s[52:55], s57 offen sc0 sc1\n"
    "v_mfma_f32_16x16x32_bf16 a[72:75], v[0:3], a[252:255], a[72:75]\n"
    "s_nop 0\n"
    "v_cvt_pk_bf16_f32 v236, v164, v165\n"
    "v_cvt_pk_bf16_f32 v237, v166, v167\n"
    "v_mfma_f32_16x16x32_bf16 a[72:75], v[4:7], v[52:55], a[72:75]\n"
    "v_accvgpr_read_b32 v174, a30\n"
    "v_accvgpr_read_b32 v175, a31\n"
    "v_mfma_f32_16x16x32_bf16 a[80:83], v[0:3], v[32:35], a[80:83]\n"
    "v_mul_f32 v172, v172, v28\n"
    "v_mul_f32 v173, v173, v28\n"
    "v_mfma_f32_16x16x32_bf16 a[80:83], v[4:7], v[56:59], a[80:83]\n"
    "s_waitcnt lgkmcnt(0)\n"
    "s_barrier\n"
    "v_mul_f32 v174, v174, v28\n"
    "v_mfma_f32_16x16x32_bf16 a[88:91], v[0:3], v[36:39], a[88:91]\n"
    "v_add_u32 v18, 73728, v18\n"
    "v_mul_f32 v175, v175, v28\n"
    "v_mfma_f32_16x16x32_bf16 a[88:91], v[4:7], v[60:63], a[88:91]\n"
    "ds_read_b64_tr_b16 v[0:1], v18 offset:0\n"
    "ds_read_b64_tr_b16 v[2:3], v18 offset:2048\n"
    "v_cvt_pk_bf16_f32 v238, v172, v173\n"
    "v_mfma_f32_16x16x32_bf16 a[52:55], v[8:11], a[240:243], a[52:55]\n"
    "v_cvt_pk_bf16_f32 v239, v174, v175\n"
    "v_accvgpr_read_b32 v164, a32\n"
    "v_mfma_f32_16x16x32_bf16 a[52:55], v[12:15], v[40:43], a[52:55]\n"
    "ds_read_b64_tr_b16 v[4:5], v18 offset:4096\n"
    "ds_read_b64_tr_b16 v[6:7], v18 offset:6144\n"
    "v_mfma_f32_16x16x32_bf16 a[60:63], v[8:11], a[244:247], a[60:63]\n"
    "v_accvgpr_read_b32 v165, a33\n"
    "v_accvgpr_read_b32 v166, a34\n"
    "v_mfma_f32_16x16x32_bf16 a[60:63], v[12:15], v[44:47], a[60:63]\n"
    "ds_read_b128 a[16:19], v16 offset:2048\n"
    "ds_read_b128 a[20:23], v17 offset:2048\n"
    "s_mov_b32 s56, 24704\n"
    "v_mfma_f32_16x16x32_bf16 a[68:71], v[8:11], a[248:251], a[68:71]\n"
    "v_accvgpr_read_b32 v167, a35\n"
    "v_mul_f32 v164, v164, v29\n"
    "v_mfma_f32_16x16x32_bf16 a[68:71], v[12:15], v[48:51], a[68:71]\n"
    "v_mul_f32 v165, v165, v29\n"
    "v_mfma_f32_16x16x32_bf16 a[76:79], v[8:11], a[252:255], a[76:79]\n"
    "v_mul_f32 v166, v166, v29\n"
    "v_mul_f32 v167, v167, v29\n"
    "v_mfma_f32_16x16x32_bf16 a[76:79], v[12:15], v[52:55], a[76:79]\n"
    "buffer_load_dwordx4 v23, s[44:47], s36 offen offset:2048 nt lds\n"
    "v_mfma_f32_16x16x32_bf16 a[84:87], v[8:11], v[32:35], a[84:87]\n"
    "v_accvgpr_read_b32 v172, a36\n"
    "v_accvgpr_read_b32 v173, a37\n"
    "v_mfma_f32_16x16x32_bf16 a[84:87], v[12:15], v[56:59], a[84:87]\n"
    "buffer_store_dwordx4 v[244:247], v24, s[52:55], s56 offen sc0 sc1\n"
    "v_mfma_f32_16x16x32_bf16 a[92:95], v[8:11], v[36:39], a[92:95]\n"
    "s_nop 0\n"
    "v_cvt_pk_bf16_f32 v244, v164, v165\n"
    "v_cvt_pk_bf16_f32 v245, v166, v167\n"
    "v_mfma_f32_16x16x32_bf16 a[92:95], v[12:15], v[60:63], a[92:95]\n"
    "s_waitcnt lgkmcnt(2)\n"
    "ds_read_b64_tr_b16 v[8:9], v18 offset:8\n"
    "ds_read_b64_tr_b16 v[10:11], v18 offset:2056\n"
    "v_mfma_f32_16x16x32_bf16 a[48:51], v[0:3], v[64:67], a[48:51]\n"
    "v_accvgpr_read_b32 v174, a38\n"
    "v_accvgpr_read_b32 v175, a39\n"
    "v_mfma_f32_16x16x32_bf16 a[48:51], v[4:7], v[88:91], a[48:51]\n"
    "ds_read_b64_tr_b16 v[12:13], v18 offset:4104\n"
    "ds_read_b64_tr_b16 v[14:15], v18 offset:6152\n"
    "v_mfma_f32_16x16x32_bf16 a[56:59], v[0:3], v[68:71], a[56:59]\n"
    "v_mul_f32 v172, v172, v29\n"
    "v_mul_f32 v173, v173, v29\n"
    "v_mfma_f32_16x16x32_bf16 a[56:59], v[4:7], v[92:95], a[56:59]\n"
    "ds_read_b64_tr_b16 v[32:33], v18 offset:8200\n"
    "ds_read_b64_tr_b16 v[34:35], v18 offset:10248\n"
    "v_mfma_f32_16x16x32_bf16 a[64:67], v[0:3], v[72:75], a[64:67]\n"
    "v_mul_f32 v174, v174, v29\n"
    "v_mul_f32 v175, v175, v29\n"
    "v_mfma_f32_16x16x32_bf16 a[64:67], v[4:7], v[96:99], a[64:67]\n"
    "ds_read_b64_tr_b16 v[36:37], v18 offset:12296\n"
    "ds_read_b64_tr_b16 v[38:39], v18 offset:14344\n"
    "v_cvt_pk_bf16_f32 v246, v172, v173\n"
    "v_mfma_f32_16x16x32_bf16 a[72:75], v[0:3], v[76:79], a[72:75]\n"
    "v_cvt_pk_bf16_f32 v247, v174, v175\n"
    "v_accvgpr_read_b32 v164, a40\n"
    "v_mfma_f32_16x16x32_bf16 a[72:75], v[4:7], v[100:103], a[72:75]\n"
    "ds_read_b64_tr_b16 v[40:41], v18 offset:16384\n"
    "ds_read_b64_tr_b16 v[42:43], v18 offset:18432\n"
    "v_mfma_f32_16x16x32_bf16 a[80:83], v[0:3], v[80:83], a[80:83]\n"
    "v_accvgpr_read_b32 v165, a41\n"
    "v_accvgpr_read_b32 v166, a42\n"
    "v_mfma_f32_16x16x32_bf16 a[80:83], v[4:7], v[104:107], a[80:83]\n"
    "ds_read_b64_tr_b16 v[44:45], v18 offset:20480\n"
    "ds_read_b64_tr_b16 v[46:47], v18 offset:22528\n"
    "ds_read_b128 a[24:27], v16 offset:6144\n"
    "v_mfma_f32_16x16x32_bf16 a[88:91], v[0:3], v[84:87], a[88:91]\n"
    "v_accvgpr_read_b32 v167, a43\n"
    "v_mul_f32 v164, v164, v30\n"
    "v_mfma_f32_16x16x32_bf16 a[88:91], v[4:7], v[108:111], a[88:91]\n"
    "s_waitcnt lgkmcnt(9)\n"
    "ds_read_b64_tr_b16 v[0:1], v18 offset:8192\n"
    "ds_read_b64_tr_b16 v[2:3], v18 offset:10240\n"
    "v_mfma_f32_16x16x32_bf16 a[52:55], v[8:11], v[64:67], a[52:55]\n"
    "v_mul_f32 v165, v165, v30\n"
    "v_mul_f32 v166, v166, v30\n"
    "v_mfma_f32_16x16x32_bf16 a[52:55], v[12:15], v[88:91], a[52:55]\n"
    "ds_read_b64_tr_b16 v[4:5], v18 offset:12288\n"
    "ds_read_b64_tr_b16 v[6:7], v18 offset:14336\n"
    "ds_read_b128 a[28:31], v17 offset:6144\n"
    "v_mfma_f32_16x16x32_bf16 a[60:63], v[8:11], v[68:71], a[60:63]\n"
    "v_mul_f32 v167, v167, v30\n"
    "v_accvgpr_read_b32 v172, a44\n"
    "v_mfma_f32_16x16x32_bf16 a[60:63], v[12:15], v[92:95], a[60:63]\n"
    "ds_read_b64_tr_b16 v[48:49], v18 offset:16392\n"
    "ds_read_b64_tr_b16 v[50:51], v18 offset:18440\n"
    "s_add_u32 m0, s60, 12288\n"
    "v_mfma_f32_16x16x32_bf16 a[68:71], v[8:11], v[72:75], a[68:71]\n"
    "v_accvgpr_read_b32 v173, a45\n"
    "v_accvgpr_read_b32 v174, a46\n"
    "v_mfma_f32_16x16x32_bf16 a[68:71], v[12:15], v[96:99], a[68:71]\n"
    "ds_read_b64_tr_b16 v[52:53], v18 offset:20488\n"
    "ds_read_b64_tr_b16 v[54:55], v18 offset:22536\n"
    "s_add_u32 s36, s61, 24576\n"
    "v_mfma_f32_16x16x32_bf16 a[76:79], v[8:11], v[76:79], a[76:79]\n"
    "v_accvgpr_read_b32 v175, a47\n"
    "v_mul_f32 v172, v172, v30\n"
    "v_mfma_f32_16x16x32_bf16 a[76:79], v[12:15], v[100:103], a[76:79]\n"
    "buffer_load_dwordx4 v20, s[44:47], s36 offen nt lds\n"
    "v_mfma_f32_16x16x32_bf16 a[84:87], v[8:11], v[80:83], a[84:87]\n"
    "v_mul_f32 v173, v173, v30\n"
    "v_mul_f32 v174, v174, v30\n"
    "v_mfma_f32_16x16x32_bf16 a[84:87], v[12:15], v[104:107], a[84:87]\n"
    "s_mov_b32 s57, 30848\n"
    "v_mfma_f32_16x16x32_bf16 a[92:95], v[8:11], v[84:87], a[92:95]\n"
    "v_mul_f32 v175, v175, v30\n"
    "v_mfma_f32_16x16x32_bf16 a[92:95], v[12:15], v[108:111], a[92:95]\n"
    "s_waitcnt lgkmcnt(5)\n"
    "buffer_load_dwordx4 v22, s[44:47], s36 offen offset:1024 nt lds\n"
    "v_mfma_f32_16x16x32_bf16 a[48:51], v[0:3], v[112:115], a[48:51]\n"
    "v_mfma_f32_16x16x32_bf16 a[48:51], v[4:7], v[136:139], a[48:51]\n"
    "buffer_store_dwordx4 v[252:255], v24, s[52:55], s57 offen sc0 sc1\n"
    "s_nop 1\n"
    "v_cvt_pk_bf16_f32 v252, v164, v165\n"
    "v_mfma_f32_16x16x32_bf16 a[48:51], v[40:43], v[160:163], a[48:51]\n"
    "v_cvt_pk_bf16_f32 v253, v166, v167\n"
    "v_cvt_pk_bf16_f32 v254, v172, v173\n"
    "v_mfma_f32_16x16x32_bf16 a[48:51], v[44:47], v[208:211], a[48:51]\n"
    "s_waitcnt lgkmcnt(0)\n"
    "v_cvt_pk_bf16_f32 v255, v174, v175\n"
    "ds_read_b128 a[32:35], v16 offset:7168\n"
    "ds_read_b128 a[36:39], v17 offset:7168\n"
    "ds_read_b128 a[40:43], v16 offset:8192\n"
    "ds_read_b128 a[44:47], v17 offset:8192\n"
    "v_mfma_f32_16x16x32_bf16 a[52:55], v[32:35], v[112:115], a[52:55]\n"
    "v_mfma_f32_16x16x32_bf16 a[52:55], v[36:39], v[136:139], a[52:55]\n"
    "v_mfma_f32_16x16x32_bf16 a[52:55], v[48:51], v[160:163], a[52:55]\n"
    "v_add_u32 v16, s37, v16\n"
    "v_add_u32 v17, s37, v17\n"
    "v_mfma_f32_16x16x32_bf16 a[52:55], v[52:55], v[208:211], a[52:55]\n"
    "s_cmp_eq_u32 s64, 4\n"
    "s_cbranch_scc1 L_tail_%=\n"
    "v_mfma_f32_16x16x32_bf16 a[56:59], v[0:3], v[116:119], a[56:59]\n"
    "v_mfma_f32_16x16x32_bf16 a[56:59], v[4:7], v[140:143], a[56:59]\n"
    "v_mfma_f32_16x16x32_bf16 a[56:59], v[40:43], v[168:171], a[56:59]\n"
    "v_accvgpr_read_b32 v56, a48\n"
    "v_accvgpr_read_b32 v57, a49\n"
    "v_mfma_f32_16x16x32_bf16 a[56:59], v[44:47], v[216:219], a[56:59]\n"
    "v_accvgpr_read_b32 v58, a50\n"
    "v_mfma_f32_16x16x32_bf16 a[60:63], v[32:35], v[116:119], a[60:63]\n"
    "v_accvgpr_read_b32 v59, a51\n"
    "v_mul_f32 v56, v56, v25\n"
    "v_mfma_f32_16x16x32_bf16 a[60:63], v[36:39], v[140:143], a[60:63]\n"
    "s_waitcnt lgkmcnt(0)\n"
    "s_barrier\n"
    "v_mfma_f32_16x16x32_bf16 a[60:63], v[48:51], v[168:171], a[60:63]\n"
    "v_mul_f32 v57, v57, v25\n"
    "v_mul_f32 v58, v58, v25\n"
    "v_mfma_f32_16x16x32_bf16 a[60:63], v[52:55], v[216:219], a[60:63]\n"
    "buffer_load_dwordx4 v23, s[44:47], s36 offen offset:2048 nt lds\n"
    "v_mfma_f32_16x16x32_bf16 a[64:67], v[0:3], v[120:123], a[64:67]\n"
    "v_mul_f32 v59, v59, v25\n"
    "v_cvt_pk_bf16_f32 v88, v56, v57\n"
    "v_mfma_f32_16x16x32_bf16 a[64:67], v[4:7], v[144:147], a[64:67]\n"
    "v_cvt_pk_bf16_f32 v89, v58, v59\n"
    "v_mfma_f32_16x16x32_bf16 a[64:67], v[40:43], v[176:179], a[64:67]\n"
    "v_xor_b32 v18, 64, v18\n"
    "v_subrev_u32 v18, 49152, v18\n"
    "v_mfma_f32_16x16x32_bf16 a[64:67], v[44:47], v[224:227], a[64:67]\n"
    "s_add_u32 m0, s60, 24576\n"
    "v_accvgpr_read_b32 v60, a52\n"
    "v_accvgpr_read_b32 v61, a53\n"
    "v_mfma_f32_16x16x32_bf16 a[68:71], v[32:35], v[120:123], a[68:71]\n"
    "v_accvgpr_read_b32 v62, a54\n"
    "v_accvgpr_read_b32 v63, a55\n"
    "v_mfma_f32_16x16x32_bf16 a[68:71], v[36:39], v[144:147], a[68:71]\n"
    "s_add_u32 s36, s61, 36864\n"
    "v_mul_f32 v60, v60, v25\n"
    "v_mul_f32 v61, v61, v25\n"
    "v_mfma_f32_16x16x32_bf16 a[68:71], v[48:51], v[176:179], a[68:71]\n"
    "v_mul_f32 v62, v62, v25\n"
    "v_mul_f32 v63, v63, v25\n"
    "v_mfma_f32_16x16x32_bf16 a[68:71], v[52:55], v[224:227], a[68:71]\n"
    "buffer_load_dwordx4 v20, s[44:47], s36 offen nt lds\n"
    "v_mfma_f32_16x16x32_bf16 a[72:75], v[0:3], v[124:127], a[72:75]\n"
    "v_cvt_pk_bf16_f32 v90, v60, v61\n"
    "v_cvt_pk_bf16_f32 v91, v62, v63\n"
    "v_mfma_f32_16x16x32_bf16 a[72:75], v[4:7], v[148:151], a[72:75]\n"
    "v_accvgpr_read_b32 v56, a56\n"
    "v_accvgpr_read_b32 v57, a57\n"
    "v_accvgpr_read_b32 v58, a58\n"
    "v_mfma_f32_16x16x32_bf16 a[72:75], v[40:43], v[184:187], a[72:75]\n"
    "v_accvgpr_read_b32 v59, a59\n"
    "v_mul_f32 v56, v56, v26\n"
    "v_mfma_f32_16x16x32_bf16 a[72:75], v[44:47], v[232:235], a[72:75]\n"
    "ds_read_b128 v[64:67], v16 offset:0\n"
    "v_mul_f32 v57, v57, v26\n"
    "v_mul_f32 v58, v58, v26\n"
    "v_mul_f32 v59, v59, v26\n"
    "v_mfma_f32_16x16x32_bf16 a[76:79], v[32:35], v[124:127], a[76:79]\n"
    "v_cvt_pk_bf16_f32 v92, v56, v57\n"
    "v_cvt_pk_bf16_f32 v93, v58, v59\n"
    "v_mfma_f32_16x16x32_bf16 a[76:79], v[36:39], v[148:151], a[76:79]\n"
    "ds_read_b128 v[68:71], v17 offset:0\n"
    "v_accvgpr_read_b32 v60, a60\n"
    "v_accvgpr_read_b32 v61, a61\n"
    "v_accvgpr_read_b32 v62, a62\n"
    "v_mfma_f32_16x16x32_bf16 a[76:79], v[48:51], v[184:187], a[76:79]\n"
    "v_accvgpr_read_b32 v63, a63\n"
    "v_mul_f32 v60, v60, v26\n"
    "v_mfma_f32_16x16x32_bf16 a[76:79], v[52:55], v[232:235], a[76:79]\n"
    "ds_read_b128 v[72:75], v16 offset:1024\n"
    "v_mul_f32 v61, v61, v26\n"
    "v_mul_f32 v62, v62, v26\n"
    "v_mul_f32 v63, v63, v26\n"
    "v_mfma_f32_16x16x32_bf16 a[80:83], v[0:3], v[128:131], a[80:83]\n"
    "v_cvt_pk_bf16_f32 v94, v60, v61\n"
    "v_cvt_pk_bf16_f32 v95, v62, v63\n"
    "v_mfma_f32_16x16x32_bf16 a[80:83], v[4:7], v[152:155], a[80:83]\n"
    "ds_read_b128 v[76:79], v17 offset:1024\n"
    "v_accvgpr_read_b32 v56, a64\n"
    "v_accvgpr_read_b32 v57, a65\n"
    "v_accvgpr_read_b32 v58, a66\n"
    "v_mfma_f32_16x16x32_bf16 a[80:83], v[40:43], v[192:195], a[80:83]\n"
    "v_add_u32 v148, s39, v16\n"
    "v_add_u32 v149, s39, v17\n"
    "v_mfma_f32_16x16x32_bf16 a[80:83], v[44:47], v[240:243], a[80:83]\n"
    "ds_read_b128 v[80:83], v16 offset:2048\n"
    "v_accvgpr_read_b32 v59, a67\n"
    "v_mul_f32 v56, v56, v27\n"
    "v_mul_f32 v57, v57, v27\n"
    "v_mfma_f32_16x16x32_bf16 a[84:87], v[32:35], v[128:131], a[84:87]\n"
    "v_mul_f32 v58, v58, v27\n"
    "v_mul_f32 v59, v59, v27\n"
    "v_mfma_f32_16x16x32_bf16 a[84:87], v[36:39], v[152:155], a[84:87]\n"
    "ds_read_b128 v[84:87], v17 offset:2048\n"
    "v_cvt_pk_bf16_f32 v96, v56, v57\n"
    "v_cvt_pk_bf16_f32 v97, v58, v59\n"
    "v_accvgpr_read_b32 v60, a68\n"
    "v_accvgpr_read_b32 v61, a69\n"
    "v_mfma_f32_16x16x32_bf16 a[84:87], v[48:51], v[192:195], a[84:87]\n"
    "v_accvgpr_read_b32 v62, a70\n"
    "v_accvgpr_read_b32 v63, a71\n"
    "v_mfma_f32_16x16x32_bf16 a[84:87], v[52:55], v[240:243], a[84:87]\n"
    "buffer_load_dwordx4 v22, s[44:47], s36 offen offset:1024 nt lds\n"
    "v_mul_f32 v60, v60, v27\n"
    "v_mul_f32 v61, v61, v27\n"
    "v_mfma_f32_16x16x32_bf16 a[88:91], v[0:3], v[132:135], a[88:91]\n"
    "v_mul_f32 v62, v62, v27\n"
    "v_mul_f32 v63, v63, v27\n"
    "v_mfma_f32_16x16x32_bf16 a[88:91], v[4:7], v[156:159], a[88:91]\n"
    "v_cvt_pk_bf16_f32 v98, v60, v61\n"
    "v_cvt_pk_bf16_f32 v99, v62, v63\n"
    "v_accvgpr_read_b32 v56, a72\n"
    "v_accvgpr_read_b32 v57, a73\n"
    "v_accvgpr_read_b32 v58, a74\n"
    "v_mfma_f32_16x16x32_bf16 a[88:91], v[40:43], v[200:203], a[88:91]\n"
    "v_accvgpr_read_b32 v59, a75\n"
    "v_mul_f32 v56, v56, v28\n"
    "v_mfma_f32_16x16x32_bf16 a[88:91], v[44:47], v[248:251], a[88:91]\n"
    "v_mul_f32 v57, v57, v28\n"
    "v_mul_f32 v58, v58, v28\n"
    "v_mul_f32 v59, v59, v28\n"
    "v_cvt_pk_bf16_f32 v100, v56, v57\n"
    "v_mfma_f32_16x16x32_bf16 a[92:95], v[32:35], v[132:135], a[92:95]\n"
    "v_cvt_pk_bf16_f32 v101, v58, v59\n"
    "v_accvgpr_read_b32 v60, a76\n"
    "v_mfma_f32_16x16x32_bf16 a[92:95], v[36:39], v[156:159], a[92:95]\n"
    "v_accvgpr_read_b32 v61, a77\n"
    "v_accvgpr_read_b32 v62, a78\n"
    "v_accvgpr_read_b32 v63, a79\n"
    "v_mul_f32 v60, v60, v28\n"
    "v_mul_f32 v61, v61, v28\n"
    "v_mfma_f32_16x16x32_bf16 a[92:95], v[48:51], v[200:203], a[92:95]\n"
    "v_mul_f32 v62, v62, v28\n"
    "v_mul_f32 v63, v63, v28\n"
    "v_mfma_f32_16x16x32_bf16 a[92:95], v[52:55], v[248:251], a[92:95]\n"
    "s_waitcnt lgkmcnt(0)\n"
    "buffer_load_dwordx4 v23, s[44:47], s36 offen offset:2048 nt lds\n"
    "v_cvt_pk_bf16_f32 v102, v60, v61\n"
    "v_cvt_pk_bf16_f32 v103, v62, v63\n"
    "s_branch L_top_%=\n"
    "L_tail_%=:\n"
    "v_mfma_f32_16x16x32_bf16 a[56:59], v[0:3], v[116:119], a[56:59]\n"
    "v_mfma_f32_16x16x32_bf16 a[56:59], v[4:7], v[140:143], a[56:59]\n"
    "v_mfma_f32_16x16x32_bf16 a[56:59], v[40:43], v[168:171], a[56:59]\n"
    "v_accvgpr_read_b32 v56, a48\n"
    "v_accvgpr_read_b32 v57, a49\n"
    "v_mfma_f32_16x16x32_bf16 a[56:59], v[44:47], v[216:219], a[56:59]\n"
    "v_accvgpr_read_b32 v58, a50\n"
    "v_accvgpr_read_b32 v59, a51\n"
    "v_mul_f32 v56, v56, v25\n"
    "v_mul_f32 v57, v57, v25\n"
    "v_mul_f32 v58, v58, v25\n"
    "v_mul_f32 v59, v59, v25\n"
    "v_cvt_pk_bf16_f32 v88, v56, v57\n"
    "v_mfma_f32_16x16x32_bf16 a[60:63], v[32:35], v[116:119], a[60:63]\n"
    "v_cvt_pk_bf16_f32 v89, v58, v59\n"
    "v_accvgpr_read_b32 v60, a52\n"
    "v_mfma_f32_16x16x32_bf16 a[60:63], v[36:39], v[140:143], a[60:63]\n"
    "v_accvgpr_read_b32 v61, a53\n"
    "v_accvgpr_read_b32 v62, a54\n"
    "v_accvgpr_read_b32 v63, a55\n"
    "v_mul_f32 v60, v60, v25\n"
    "v_mul_f32 v61, v61, v25\n"
    "v_mul_f32 v62, v62, v25\n"
    "v_mul_f32 v63, v63, v25\n"
    "v_mfma_f32_16x16x32_bf16 a[60:63], v[48:51], v[168:171], a[60:63]\n"
    "v_mfma_f32_16x16x32_bf16 a[60:63], v[52:55], v[216:219], a[60:63]\n"
    "s_mov_b32 s56, 256\n"
    "s_mov_b64 vcc, s[96:97]\n"
    "v_cvt_pk_bf16_f32 v90, v60, v61\n"
    "v_cvt_pk_bf16_f32 v91, v62, v63\n"
    "v_cndmask_b32_dpp v112, v88, v212, vcc row_ror:8 row_mask:0xf bank_mask:0xf\n"
    "v_cndmask_b32_dpp v113, v89, v213, vcc row_ror:8 row_mask:0xf bank_mask:0xf\n"
    "v_cndmask_b32_dpp v114, v90, v214, vcc row_ror:8 row_mask:0xf bank_mask:0xf\n"
    "v_cndmask_b32_dpp v115, v91, v215, vcc row_ror:8 row_mask:0xf bank_mask:0xf\n"
    "v_mfma_f32_16x16x32_bf16 a[64:67], v[0:3], v[120:123], a[64:67]\n"
    "v_mfma_f32_16x16x32_bf16 a[64:67], v[4:7], v[144:147], a[64:67]\n"
    "s_mov_b64 vcc, s[98:99]\n"
    "buffer_store_dwordx4 v[112:115], v24, s[52:55], s56 offen sc0 sc1\n"
    "v_cndmask_b32_dpp v88, v212, v88, vcc row_ror:8 row_mask:0xf bank_mask:0xf\n"
    "v_mfma_f32_16x16x32_bf16 a[64:67], v[40:43], v[176:179], a[64:67]\n"
    "v_cndmask_b32_dpp v89, v213, v89, vcc row_ror:8 row_mask:0xf bank_mask:0xf\n"
    "v_cndmask_b32_dpp v90, v214, v90, vcc row_ror:8 row_mask:0xf bank_mask:0xf\n"
    "v_mfma_f32_16x16x32_bf16 a[64:67], v[44:47], v[224:227], a[64:67]\n"
    "v_cndmask_b32_dpp v91, v215, v91, vcc row_ror:8 row_mask:0xf bank_mask:0xf\n"
    "buffer_store_dwordx4 v[88:91], v24, s[52:55], s56 offen offset:3072 sc0 sc1\n"
    "v_accvgpr_read_b32 v56, a56\n"
    "v_mfma_f32_16x16x32_bf16 a[68:71], v[32:35], v[120:123], a[68:71]\n"
    "v_accvgpr_read_b32 v57, a57\n"
    "v_accvgpr_read_b32 v58, a58\n"
    "v_mfma_f32_16x16x32_bf16 a[68:71], v[36:39], v[144:147], a[68:71]\n"
    "v_accvgpr_read_b32 v59, a59\n"
    "v_mul_f32 v56, v56, v26\n"
    "v_mul_f32 v57, v57, v26\n"
    "v_mul_f32 v58, v58, v26\n"
    "v_mul_f32 v59, v59, v26\n"
    "v_cvt_pk_bf16_f32 v92, v56, v57\n"
    "v_cvt_pk_bf16_f32 v93, v58, v59\n"
    "v_mfma_f32_16x16x32_bf16 a[68:71], v[48:51], v[176:179], a[68:71]\n"
    "v_accvgpr_read_b32 v60, a60\n"
    "v_accvgpr_read_b32 v61, a61\n"
    "v_mfma_f32_16x16x32_bf16 a[68:71], v[52:55], v[224:227], a[68:71]\n"
    "v_accvgpr_read_b32 v62, a62\n"
    "v_accvgpr_read_b32 v63, a63\n"
    "v_mul_f32 v60, v60, v26\n"
    "v_mul_f32 v61, v61, v26\n"
    "v_mul_f32 v62, v62, v26\n"
    "v_mul_f32 v63, v63, v26\n"
    "s_mov_b32 s57, 6400\n"
    "s_mov_b64 vcc, s[96:97]\n"
    "v_mfma_f32_16x16x32_bf16 a[72:75], v[0:3], v[124:127], a[72:75]\n"
    "v_cvt_pk_bf16_f32 v94, v60, v61\n"
    "v_cvt_pk_bf16_f32 v95, v62, v63\n"
    "v_mfma_f32_16x16x32_bf16 a[72:75], v[4:7], v[148:151], a[72:75]\n"
    "v_cndmask_b32_dpp v116, v92, v220, vcc row_ror:8 row_mask:0xf bank_mask:0xf\n"
    "v_cndmask_b32_dpp v117, v93, v221, vcc row_ror:8 row_mask:0xf bank_mask:0xf\n"
    "v_cndmask_b32_dpp v118, v94, v222, vcc row_ror:8 row_mask:0xf bank_mask:0xf\n"
    "v_cndmask_b32_dpp v119, v95, v223, vcc row_ror:8 row_mask:0xf bank_mask:0xf\n"
    "s_mov_b64 vcc, s[98:99]\n"
    "v_mfma_f32_16x16x32_bf16 a[72:75], v[40:43], v[184:187], a[72:75]\n"
    "v_mfma_f32_16x16x32_bf16 a[72:75], v[44:47], v[232:235], a[72:75]\n"
    "buffer_store_dwordx4 v[116:119], v24, s[52:55], s57 offen sc0 sc1\n"
    "v_cndmask_b32_dpp v92, v220, v92, vcc row_ror:8 row_mask:0xf bank_mask:0xf\n"
    "v_cndmask_b32_dpp v93, v221, v93, vcc row_ror:8 row_mask:0xf bank_mask:0xf\n"
    "v_mfma_f32_16x16x32_bf16 a[76:79], v[32:35], v[124:127], a[76:79]\n"
    "v_cndmask_b32_dpp v94, v222, v94, vcc row_ror:8 row_mask:0xf bank_mask:0xf\n"
    "v_cndmask_b32_dpp v95, v223, v95, vcc row_ror:8 row_mask:0xf bank_mask:0xf\n"
    "v_mfma_f32_16x16x32_bf16 a[76:79], v[36:39], v[148:151], a[76:79]\n"
    "buffer_store_dwordx4 v[92:95], v24, s[52:55], s57 offen offset:3072 sc0 sc1\n"
    "v_accvgpr_read_b32 v56, a64\n"
    "v_accvgpr_read_b32 v57, a65\n"
    "v_mfma_f32_16x16x32_bf16 a[76:79], v[48:51], v[184:187], a[76:79]\n"
    "v_accvgpr_read_b32 v58, a66\n"
    "v_accvgpr_read_b32 v59, a67\n"
    "v_mfma_f32_16x16x32_bf16 a[76:79], v[52:55], v[232:235], a[76:79]\n"
    "v_mul_f32 v56, v56, v27\n"
    "v_mul_f32 v57, v57, v27\n"
    "v_mul_f32 v58, v58, v27\n"
    "v_mul_f32 v59, v59, v27\n"
    "v_cvt_pk_bf16_f32 v96, v56, v57\n"
    "v_cvt_pk_bf16_f32 v97, v58, v59\n"
    "v_accvgpr_read_b32 v60, a68\n"
    "v_mfma_f32_16x16x32_bf16 a[80:83], v[0:3], v[128:131], a[80:83]\n"
    "v_accvgpr_read_b32 v61, a69\n"
    "v_accvgpr_read_b32 v62, a70\n"
    "v_mfma_f32_16x16x32_bf16 a[80:83], v[4:7], v[152:155], a[80:83]\n"
    "v_accvgpr_read_b32 v63, a71\n"
    "v_mul_f32 v60, v60, v27\n"
    "v_mul_f32 v61, v61, v27\n"
    "v_mul_f32 v62, v62, v27\n"
    "v_mul_f32 v63, v63, v27\n"
    "s_mov_b32 s56, 12544\n"
    "s_mov_b64 vcc, s[96:97]\n"
    "v_cvt_pk_bf16_f32 v98, v60, v61\n"
    "v_mfma_f32_16x16x32_bf16 a[80:83], v[40:43], v[192:195], a[80:83]\n"
    "v_cvt_pk_bf16_f32 v99, v62, v63\n"
    "v_cndmask_b32_dpp v120, v96, v228, vcc row_ror:8 row_mask:0xf bank_mask:0xf\n"
    "v_mfma_f32_16x16x32_bf16 a[80:83], v[44:47], v[240:243], a[80:83]\n"
    "v_cndmask_b32_dpp v121, v97, v229, vcc row_ror:8 row_mask:0xf bank_mask:0xf\n"
    "v_cndmask_b32_dpp v122, v98, v230, vcc row_ror:8 row_mask:0xf bank_mask:0xf\n"
    "v_cndmask_b32_dpp v123, v99, v231, vcc row_ror:8 row_mask:0xf bank_mask:0xf\n"
    "s_mov_b64 vcc, s[98:99]\n"
    "v_mfma_f32_16x16x32_bf16 a[84:87], v[32:35], v[128:131], a[84:87]\n"
    "v_mfma_f32_16x16x32_bf16 a[84:87], v[36:39], v[152:155], a[84:87]\n"
    "buffer_store_dwordx4 v[120:123], v24, s[52:55], s56 offen sc0 sc1\n"
    "v_cndmask_b32_dpp v96, v228, v96, vcc row_ror:8 row_mask:0xf bank_mask:0xf\n"
    "v_cndmask_b32_dpp v97, v229, v97, vcc row_ror:8 row_mask:0xf bank_mask:0xf\n"
    "v_mfma_f32_16x16x32_bf16 a[84:87], v[48:51], v[192:195], a[84:87]\n"
    "v_cndmask_b32_dpp v98, v230, v98, vcc row_ror:8 row_mask:0xf bank_mask:0xf\n"
    "v_cndmask_b32_dpp v99, v231, v99, vcc row_ror:8 row_mask:0xf bank_mask:0xf\n"
    "v_mfma_f32_16x16x32_bf16 a[84:87], v[52:55], v[240:243], a[84:87]\n"
    "buffer_store_dwordx4 v[96:99], v24, s[52:55], s56 offen offset:3072 sc0 sc1\n"
    "v_accvgpr_read_b32 v56, a72\n"
    "v_accvgpr_read_b32 v57, a73\n"
    "v_mfma_f32_16x16x32_bf16 a[88:91], v[0:3], v[132:135], a[88:91]\n"
    "v_accvgpr_read_b32 v58, a74\n"
    "v_accvgpr_read_b32 v59, a75\n"
    "v_mfma_f32_16x16x32_bf16 a[88:91], v[4:7], v[156:159], a[88:91]\n"
    "v_mul_f32 v56, v56, v28\n"
    "v_mul_f32 v57, v57, v28\n"
    "v_mul_f32 v58, v58, v28\n"
    "v_mul_f32 v59, v59, v28\n"
    "v_cvt_pk_bf16_f32 v100, v56, v57\n"
    "v_cvt_pk_bf16_f32 v101, v58, v59\n"
    "v_accvgpr_read_b32 v60, a76\n"
    "v_mfma_f32_16x16x32_bf16 a[88:91], v[40:43], v[200:203], a[88:91]\n"
    "v_accvgpr_read_b32 v61, a77\n"
    "v_accvgpr_read_b32 v62, a78\n"
    "v_mfma_f32_16x16x32_bf16 a[88:91], v[44:47], v[248:251], a[88:91]\n"
    "v_accvgpr_read_b32 v63, a79\n"
    "v_mul_f32 v60, v60, v28\n"
    "v_mul_f32 v61, v61, v28\n"
    "v_mul_f32 v62, v62, v28\n"
    "v_mul_f32 v63, v63, v28\n"
    "s_mov_b32 s57, 18688\n"
    "s_mov_b64 vcc, s[96:97]\n"
    "v_cvt_pk_bf16_f32 v102, v60, v61\n"
    "v_mfma_f32_16x16x32_bf16 a[92:95], v[32:35], v[132:135], a[92:95]\n"
    "v_cvt_pk_bf16_f32 v103, v62, v63\n"
    "v_cndmask_b32_dpp v124, v100, v236, vcc row_ror:8 row_mask:0xf bank_mask:0xf\n"
    "v_mfma_f32_16x16x32_bf16 a[92:95], v[36:39], v[156:159], a[92:95]\n"
    "v_cndmask_b32_dpp v125, v101, v237, vcc row_ror:8 row_mask:0xf bank_mask:0xf\n"
    "v_cndmask_b32_dpp v126, v102, v238, vcc row_ror:8 row_mask:0xf bank_mask:0xf\n"
    "v_cndmask_b32_dpp v127, v103, v239, vcc row_ror:8 row_mask:0xf bank_mask:0xf\n"
    "s_mov_b64 vcc, s[98:99]\n"
    "v_mfma_f32_16x16x32_bf16 a[92:95], v[48:51], v[200:203], a[92:95]\n"
    "v_mfma_f32_16x16x32_bf16 a[92:95], v[52:55], v[248:251], a[92:95]\n"
    "buffer_store_dwordx4 v[124:127], v24, s[52:55], s57 offen sc0 sc1\n"
    "v_cndmask_b32_dpp v100, v236, v100, vcc row_ror:8 row_mask:0xf bank_mask:0xf\n"
    "v_cndmask_b32_dpp v101, v237, v101, vcc row_ror:8 row_mask:0xf bank_mask:0xf\n"
    "v_cndmask_b32_dpp v102, v238, v102, vcc row_ror:8 row_mask:0xf bank_mask:0xf\n"
    "v_cndmask_b32_dpp v103, v239, v103, vcc row_ror:8 row_mask:0xf bank_mask:0xf\n"
    "buffer_store_dwordx4 v[100:103], v24, s[52:55], s57 offen offset:3072 sc0 sc1\n"
    "v_accvgpr_read_b32 v56, a80\n"
    "v_accvgpr_read_b32 v57, a81\n"
    "v_accvgpr_read_b32 v58, a82\n"
    "v_accvgpr_read_b32 v59, a83\n"
    "v_mul_f32 v56, v56, v29\n"
    "v_mul_f32 v57, v57, v29\n"
    "v_mul_f32 v58, v58, v29\n"
    "v_mul_f32 v59, v59, v29\n"
    "v_cvt_pk_bf16_f32 v104, v56, v57\n"
    "v_cvt_pk_bf16_f32 v105, v58, v59\n"
    "v_accvgpr_read_b32 v60, a84\n"
    "v_accvgpr_read_b32 v61, a85\n"
    "v_accvgpr_read_b32 v62, a86\n"
    "v_accvgpr_read_b32 v63, a87\n"
    "v_mul_f32 v60, v60, v29\n"
    "v_mul_f32 v61, v61, v29\n"
    "v_mul_f32 v62, v62, v29\n"
    "v_mul_f32 v63, v63, v29\n"
    "s_mov_b32 s56, 24832\n"
    "s_mov_b64 vcc, s[96:97]\n"
    "v_cvt_pk_bf16_f32 v106, v60, v61\n"
    "v_cvt_pk_bf16_f32 v107, v62, v63\n"
    "v_cndmask_b32_dpp v128, v104, v244, vcc row_ror:8 row_mask:0xf bank_mask:0xf\n"
    "v_cndmask_b32_dpp v129, v105, v245, vcc row_ror:8 row_mask:0xf bank_mask:0xf\n"
    "v_cndmask_b32_dpp v130, v106, v246, vcc row_ror:8 row_mask:0xf bank_mask:0xf\n"
    "v_cndmask_b32_dpp v131, v107, v247, vcc row_ror:8 row_mask:0xf bank_mask:0xf\n"
    "s_mov_b64 vcc, s[98:99]\n"
    "buffer_store_dwordx4 v[128:131], v24, s[52:55], s56 offen sc0 sc1\n"
    "v_cndmask_b32_dpp v104, v244, v104, vcc row_ror:8 row_mask:0xf bank_mask:0xf\n"
    "v_cndmask_b32_dpp v105, v245, v105, vcc row_ror:8 row_mask:0xf bank_mask:0xf\n"
    "v_cndmask_b32_dpp v106, v246, v106, vcc row_ror:8 row_mask:0xf bank_mask:0xf\n"
    "v_cndmask_b32_dpp v107, v247, v107, vcc row_ror:8 row_mask:0xf bank_mask:0xf\n"
    "buffer_store_dwordx4 v[104:107], v24, s[52:55], s56 offen offset:3072 sc0 sc1\n"
    "v_accvgpr_read_b32 v56, a88\n"
    "v_accvgpr_read_b32 v57, a89\n"
    "v_accvgpr_read_b32 v58, a90\n"
    "v_accvgpr_read_b32 v59, a91\n"
    "v_mul_f32 v56, v56, v30\n"
    "v_mul_f32 v57, v57, v30\n"
    "v_mul_f32 v58, v58, v30\n"
    "v_mul_f32 v59, v59, v30\n"
    "v_cvt_pk_bf16_f32 v108, v56, v57\n"
    "v_cvt_pk_bf16_f32 v109, v58, v59\n"
    "v_accvgpr_read_b32 v60, a92\n"
    "v_accvgpr_read_b32 v61, a93\n"
    "v_accvgpr_read_b32 v62, a94\n"
    "v_accvgpr_read_b32 v63, a95\n"
    "v_mul_f32 v60, v60, v30\n"
    "v_mul_f32 v61, v61, v30\n"
    "v_mul_f32 v62, v62, v30\n"
    "v_mul_f32 v63, v63, v30\n"
    "s_mov_b32 s57, 30976\n"
    "s_mov_b64 vcc, s[96:97]\n"
    "v_cvt_pk_bf16_f32 v110, v60, v61\n"
    "v_cvt_pk_bf16_f32 v111, v62, v63\n"
    "v_cndmask_b32_dpp v132, v108, v252, vcc row_ror:8 row_mask:0xf bank_mask:0xf\n"
    "v_cndmask_b32_dpp v133, v109, v253, vcc row_ror:8 row_mask:0xf bank_mask:0xf\n"
    "v_cndmask_b32_dpp v134, v110, v254, vcc row_ror:8 row_mask:0xf bank_mask:0xf\n"
    "v_cndmask_b32_dpp v135, v111, v255, vcc row_ror:8 row_mask:0xf bank_mask:0xf\n"
    "s_mov_b64 vcc, s[98:99]\n"
    "buffer_store_dwordx4 v[132:135], v24, s[52:55], s57 offen sc0 sc1\n"
    "v_cndmask_b32_dpp v108, v252, v108, vcc row_ror:8 row_mask:0xf bank_mask:0xf\n"
    "v_cndmask_b32_dpp v109, v253, v109, vcc row_ror:8 row_mask:0xf bank_mask:0xf\n"
    "v_cndmask_b32_dpp v110, v254, v110, vcc row_ror:8 row_mask:0xf bank_mask:0xf\n"
    "v_cndmask_b32_dpp v111, v255, v111, vcc row_ror:8 row_mask:0xf bank_mask:0xf\n"
    "buffer_store_dwordx4 v[108:111], v24, s[52:55], s57 offen offset:3072 sc0 sc1\n"
    "s_cmp_eq_u32 s65, 0\n"
    "s_cbranch_scc1 L_fin_%=\n"
    "s_waitcnt vmcnt(0) lgkmcnt(0)\n"
    "L_fin_%=:\n"
    "s_mov_b32 %0, s65\n"
    "s_branch L_end_%=\n"
    "L_pc0_%=:\n"
    "s_waitcnt lgkmcnt(0)\n"
    "s_sleep 1\n"
    "s_memrealtime s[92:93]\n"
    "s_waitcnt lgkmcnt(0)\n"
    "s_sub_u32 s95, s94, s92\n"
    "s_cmp_gt_i32 s95, 3\n"
    "s_cbranch_scc1 L_pc0_%=\n"
    "s_branch L_pr0_%=\n"
    "L_pc1_%=:\n"
    "s_waitcnt lgkmcnt(0)\n"
    "s_sleep 1\n"
    "s_memrealtime s[92:93]\n"
    "s_waitcnt lgkmcnt(0)\n"
    "s_sub_u32 s95, s94, s92\n"
    "s_cmp_gt_i32 s95, 3\n"
    "s_cbranch_scc1 L_pc1_%=\n"
    "s_branch L_pr1_%=\n"
    "L_pc2_%=:\n"
    "s_waitcnt lgkmcnt(0)\n"
    "s_sleep 1\n"
    "s_memrealtime s[92:93]\n"
    "s_waitcnt lgkmcnt(0)\n"
    "s_sub_u32 s95, s94, s92\n"
    "s_cmp_gt_i32 s95, 3\n"
    "s_cbranch_scc1 L_pc2_%=\n"
    "s_branch L_pr2_%=\n"
    "L_pc3_%=:\n"
    "s_waitcnt lgkmcnt(0)\n"
    "s_sleep 1\n"
    "s_memrealtime s[92:93]\n"
    "s_waitcnt lgkmcnt(0)\n"
    "s_sub_u32 s95, s94, s92\n"
    "s_cmp_gt_i32 s95, 3\n"
    "s_cbranch_scc1 L_pc3_%=\n"
    "s_branch L_pr3_%=\n"
    "L_pc4_%=:\n"
    "s_waitcnt lgkmcnt(0)\n"
    "s_sleep 1\n"
    "s_memrealtime s[92:93]\n"
    "s_waitcnt lgkmcnt(0)\n"
    "s_sub_u32 s95, s94, s92\n"
    "s_cmp_gt_i32 s95, 3\n"
    "s_cbranch_scc1 L_pc4_%=\n"
    "s_branch L_pr4_%=\n"
    "L_pc5_%=:\n"
    "s_waitcnt lgkmcnt(0)\n"
    "s_sleep 1\n"
    "s_memrealtime s[92:93]\n"
    "s_waitcnt lgkmcnt(0)\n"
    "s_sub_u32 s95, s94, s92\n"
    "s_cmp_gt_i32 s95, 3\n"
    "s_cbranch_scc1 L_pc5_%=\n"
    "s_branch L_pr5_%=\n"
    "L_pc6_%=:\n"
    "s_waitcnt lgkmcnt(0)\n"
    "s_sleep 1\n"
    "s_memrealtime s[92:93]\n"
    "s_waitcnt lgkmcnt(0)\n"
    "s_sub_u32 s95, s94, s92\n"
    "s_cmp_gt_i32 s95, 3\n"
    "s_cbranch_scc1 L_pc6_%=\n"
    "s_branch L_pr6_%=\n"
    "L_pc7_%=:\n"
    "s_waitcnt lgkmcnt(0)\n"
    "s_sleep 1\n"
    "s_memrealtime s[92:93]\n"
    "s_waitcnt lgkmcnt(0)\n"
    "s_sub_u32 s95, s94, s92\n"
    "s_cmp_gt_i32 s95, 3\n"
    "s_cbranch_scc1 L_pc7_%=\n"
    "s_branch L_pr7_%=\n"
    "L_pc8_%=:\n"
    "s_waitcnt lgkmcnt(0)\n"
    "s_sleep 1\n"
    "s_memrealtime s[92:93]\n"
    "s_waitcnt lgkmcnt(0)\n"
    "s_sub_u32 s95, s94, s92\n"
    "s_cmp_gt_i32 s95, 3\n"
    "s_cbranch_scc1 L_pc8_%=\n"
    "s_branch L_pr8_%=\n"
    "L_pc9_%=:\n"
    "s_waitcnt lgkmcnt(0)\n"
    "s_sleep 1\n"
    "s_memrealtime s[92:93]\n"
    "s_waitcnt lgkmcnt(0)\n"
    "s_sub_u32 s95, s94, s92\n"
    "s_cmp_gt_i32 s95, 3\n"
    "s_cbranch_scc1 L_pc9_%=\n"
    "s_branch L_pr9_%=\n"
    "L_pc10_%=:\n"
    "s_waitcnt lgkmcnt(0)\n"
    "s_sleep 1\n"
    "s_memrealtime s[92:93]\n"
    "s_waitcnt lgkmcnt(0)\n"
    "s_sub_u32 s95, s94, s92\n"
    "s_cmp_gt_i32 s95, 3\n"
    "s_cbranch_scc1 L_pc10_%=\n"
    "s_branch L_pr10_%=\n"
    "L_pc11_%=:\n"
    "s_waitcnt lgkmcnt(0)\n"
    "s_sleep 1\n"
    "s_memrealtime s[92:93]\n"
    "s_waitcnt lgkmcnt(0)\n"
    "s_sub_u32 s95, s94, s92\n"
    "s_cmp_gt_i32 s95, 3\n"
    "s_cbranch_scc1 L_pc11_%=\n"
    "s_branch L_pr11_%=\n"
    "L_end_%=:\n"
    : "=s"(flags)
    : "s"(__builtin_amdgcn_kernarg_segment_ptr()), "s"(blk), "s"(wid), "s"(ldsb)
    : "v0", "v1", "v2", "v3", "v4", "v5", "v6", "v7", "v8", "v9", "v10", "v11", "v12", "v13", "v14", "v15", "v16", "v17", "v18", "v19", "v20", "v21", "v22", "v23", "v24", "v25", "v26", "v27", "v28", "v29", "v30", "v31", "v32", "v33", "v34", "v35", "v36", "v37", "v38", "v39", "v40", "v41", "v42", "v43", "v44", "v45", "v46", "v47", "v48", "v49", "v50", "v51", "v52", "v53", "v54", "v55", "v56", "v57", "v58", "v59", "v60", "v61", "v62", "v63", "v64", "v65", "v66", "v67", "v68", "v69", "v70", "v71", "v72", "v73", "v74", "v75", "v76", "v77", "v78", "v79", "v80", "v81", "v82", "v83", "v84", "v85", "v86", "v87", "v88", "v89", "v90", "v91", "v92", "v93", "v94", "v95", "v96", "v97", "v98", "v99", "v100", "v101", "v102", "v103", "v104", "v105", "v106", "v107", "v108", "v109", "v110", "v111", "v112", "v113", "v114", "v115", "v116", "v117", "v118", "v119", "v120", "v121", "v122", "v123", "v124", "v125", "v126", "v127", "v128", "v129", "v130", "v131", "v132", "v133", "v134", "v135", "v136", "v137", "v138", "v139", "v140", "v141", "v142", "v143", "v144", "v145", "v146", "v147", "v148", "v149", "v150", "v151", "v152", "v153", "v154", "v155", "v156", "v157", "v158", "v159", "v160", "v161", "v162", "v163", "v164", "v165", "v166", "v167", "v168", "v169", "v170", "v171", "v172", "v173", "v174", "v175", "v176", "v177", "v178", "v179", "v180", "v181", "v182", "v183", "v184", "v185", "v186", "v187", "v188", "v189", "v190", "v191", "v192", "v193", "v194", "v195", "v196", "v197", "v198", "v199", "v200", "v201", "v202", "v203", "v204", "v205", "v206", "v207", "v208", "v209", "v210", "v211", "v212", "v213", "v214", "v215", "v216", "v217", "v218", "v219", "v220", "v221", "v222", "v223", "v224", "v225", "v226", "v227", "v228", "v229", "v230", "v231", "v232", "v233", "v234", "v235", "v236", "v237", "v238", "v239", "v240", "v241", "v242", "v243", "v244", "v245", "v246", "v247", "v248", "v249", "v250", "v251", "v252", "v253", "v254", "v255", "a0", "a1", "a2", "a3", "a4", "a5", "a6", "a7", "a8", "a9", "a10", "a11", "a12", "a13", "a14", "a15", "a16", "a17", "a18", "a19", "a20", "a21", "a22", "a23", "a24", "a25", "a26", "a27", "a28", "a29", "a30", "a31", "a32", "a33", "a34", "a35", "a36", "a37", "a38", "a39", "a40", "a41", "a42", "a43", "a44", "a45", "a46", "a47", "a48", "a49", "a50", "a51", "a52", "a53", "a54", "a55", "a56", "a57", "a58", "a59", "a60", "a61", "a62", "a63", "a64", "a65", "a66", "a67", "a68", "a69", "a70", "a71", "a72", "a73", "a74", "a75", "a76", "a77", "a78", "a79", "a80", "a81", "a82", "a83", "a84", "a85", "a86", "a87", "a88", "a89", "a90", "a91", "a92", "a93", "a94", "a95", "a96", "a97", "a98", "a99", "a100", "a101", "a102", "a103", "a104", "a105", "a106", "a107", "a108", "a109", "a110", "a111", "a112", "a113", "a114", "a115", "a116", "a117", "a118", "a119", "a120", "a121", "a122", "a123", "a124", "a125", "a126", "a127", "a128", "a129", "a130", "a131", "a132", "a133", "a134", "a135", "a136", "a137", "a138", "a139", "a140", "a141", "a142", "a143", "a144", "a145", "a146", "a147", "a148", "a149", "a150", "a151", "a152", "a153", "a154", "a155", "a156", "a157", "a158", "a159", "a160", "a161", "a162", "a163", "a164", "a165", "a166", "a167", "a168", "a169", "a170", "a171", "a172", "a173", "a174", "a175", "a176", "a177", "a178", "a179", "a180", "a181", "a182", "a183", "a184", "a185", "a186", "a187", "a188", "a189", "a190", "a191", "a192", "a193", "a194", "a195", "a196", "a197", "a198", "a199", "a200", "a201", "a202", "a203", "a204", "a205", "a206", "a207", "a208", "a209", "a210", "a211", "a212", "a213", "a214", "a215", "a216", "a217", "a218", "a219", "a220", "a221", "a222", "a223", "a224", "a225", "a226", "a227", "a228", "a229", "a230", "a231", "a232", "a233", "a234", "a235", "a236", "a237", "a238", "a239", "a240", "a241", "a242", "a243", "a244", "a245", "a246", "a247", "a248", "a249", "a250", "a251", "a252", "a253", "a254", "a255", "s32", "s33", "s34", "s35", "s36", "s37", "s38", "s39", "s40", "s41", "s42", "s43", "s44", "s45", "s46", "s47", "s48", "s49", "s50", "s51", "s52", "s53", "s54", "s55", "s56", "s57", "s58", "s59", "s60", "s61", "s62", "s63", "s64", "s65", "s66", "s67", "s68", "s69", "s70", "s71", "s72", "s73", "s74", "s75", "s76", "s77", "s78", "s79", "s80", "s81", "s82", "s83", "s84", "s85", "s86", "s87", "s88", "s89", "s90", "s91", "s92", "s93", "s94", "s95", "s96", "s97", "s98", "s99", "s100", "s101", "vcc", "scc", "memory");
  if (flags) {
#pragma unroll 1
    for (unsigned j = 0; j < 4; ++j)
      if ((flags >> j) & 1u) exact_rows(q, k, v, o, (blk & 7u) * 128 + (blk >> 3) + 32 * j, wid);
  }
}

extern "C" void kernel_launch(void* const* d_in, const int* in_sizes, int n_in, void* d_out, int out_size, void* d_ws,
                              size_t ws_size, hipStream_t stream) {
  (void)in_sizes; (void)n_in; (void)out_size; (void)d_ws; (void)ws_size;
  (void)hipFuncSetAttribute((const void*)mha_fwd_kernel, hipFuncAttributeMaxDynamicSharedMemorySize, 163840);
  hipLaunchKernelGGL(mha_fwd_kernel, dim3(256), dim3(256), 163840, stream, (const unsigned short*)d_in[0],
                     (const unsigned short*)d_in[1], (const unsigned short*)d_in[2], (unsigned short*)d_out
  );
}
```
